```python
import torch

def _softmax(x, axis=-1):
    return torch.softmax(x, dim=axis)
def _gelu(x):
    return torch.nn.functional.gelu(x, approximate="tanh")
def _silu(x):
    return torch.nn.functional.silu(x)
def _sigmoid(x):
    return torch.sigmoid(x)
def _layernorm(x, axis=-1, eps=1e-5):
    m = x.mean(dim=axis, keepdim=True)
    v = x.var(dim=axis, keepdim=True, unbiased=False)
    return (x - m) / torch.sqrt(v + eps)
def _scatter_add(tgt, idx, src, axis=0):
    return tgt.clone().index_add_(axis, idx, src)
def _segment_sum(src, seg, n):
    out = torch.zeros((n,) + src.shape[1:], dtype=src.dtype)
    return out.index_add_(0, seg, src)
def _pad(x, pw):
    flat = tuple(v for pair in reversed(pw) for v in pair)
    return torch.nn.functional.pad(x, flat)

kernel_name = 'concat_heads__linear_attn_block__ssm_conv_s4d__prompt'

def setup_inputs():

    _w = (lambda a: (lambda t: t.to(torch.bfloat16) if t.is_floating_point() else t)(torch.as_tensor(a)))
    def _A_setup():
        B, S, D = 1, 2048, 1024
        return {
            "x": _w((lambda *s: torch.randn(*s, dtype=torch.bfloat16) * 0.05)(B, S, D)),
            "wqkv": _w((lambda *s: torch.randn(*s, dtype=torch.bfloat16) * 0.8)(D, 3 * D)),
            "wo": _w((lambda *s: torch.randn(*s, dtype=torch.bfloat16) * 0.05)(D, D)),
        }
    _A_in = _A_setup()

    def _B_setup():
        Bsz, L, D, N = 1, 2048, 1024, 128
        return {
            "u": _w((lambda *s: torch.randn(*s, dtype=torch.bfloat16) * 0.05)(Bsz, L, D)),
            "log_A_real": _w((lambda *s: torch.randn(*s, dtype=torch.bfloat16) * 3.2)(D, N) * 0.5),
            "A_imag": _w((lambda *s: torch.randn(*s, dtype=torch.bfloat16) * 0.05)(D, N) * 3.14),
            "C_real": _w((lambda *s: torch.randn(*s, dtype=torch.bfloat16) * 0.8)(D, N) / N ** 0.5),
            "C_imag": _w((lambda *s: torch.randn(*s, dtype=torch.bfloat16) * 0.8)(D, N) / N ** 0.5),
            "log_dt": _w((lambda *s: torch.randn(*s, dtype=torch.bfloat16) * 0.05)(D) * 0.5 - 4.0),
            "D_skip": _w((lambda *s: torch.randn(*s, dtype=torch.bfloat16) * 0.1)(D)),
        }
    _B_in = _B_setup()

    return {
        "A_x": _A_in["x"],
        "A_wqkv": _A_in["wqkv"],
        "A_wo": _A_in["wo"],
        "B_u": _B_in["u"],
        "B_log_A_real": _B_in["log_A_real"],
        "B_A_imag": _B_in["A_imag"],
        "B_C_real": _B_in["C_real"],
        "B_C_imag": _B_in["C_imag"],
        "B_log_dt": _B_in["log_dt"],
        "B_D_skip": _B_in["D_skip"],
    }

def reference(A_x, A_wqkv, A_wo, B_u, B_log_A_real, B_A_imag, B_C_real, B_C_imag, B_log_dt, B_D_skip):
    def _A_ref(x, wqkv, wo):
        B, S, D = x.shape
        H = 32; Dh = D // H
        qkv = (lambda x, *d: x.permute(*d))((x @ wqkv).reshape(B, S, 3, H, Dh), 2, 0, 3, 1, 4)
        q, k, v = qkv[0], qkv[1], qkv[2]
        phi = lambda t: torch.where(t > 0, t + 1, torch.exp(t)) + 1e-6
        pq, pk = phi(q), phi(k)
        kv = torch.einsum("bhsd,bhse->bhde", pk, v)
        z = torch.einsum("bhsd->bhd", pk)
        num = torch.einsum("bhqd,bhde->bhqe", pq, kv)
        den = torch.einsum("bhqd,bhd->bhq", pq, z)[..., None] + 1e-6
        o = num / den
        return (lambda x, *d: x.permute(*d))(o, 0, 2, 1, 3).reshape(B, S, D) @ wo

    def _B_ref(u, log_A_real, A_imag, C_real, C_imag, log_dt, D_skip):
        L = u.shape[1]
        dt = torch.exp(log_dt)
        A = -torch.exp(log_A_real) + 1j * A_imag
        C = (C_real + 1j * C_imag) * dt[:, None]
        pos = (lambda x, dt: x.to(dt))(torch.arange(L), log_dt.dtype)
        P = torch.exp((A * dt[:, None])[:, :, None] * pos[None, None, :])
        K = 2.0 * torch.einsum("dn,dnl->dl", C, P).real
        L2 = 2 * L
        u_t = (lambda x, *d: x.permute(*d))(u, 0, 2, 1)
        Kf = (lambda x, *a, **k: torch.fft.rfft(x.float(), *a, **k))(K, L2)
        Uf = (lambda x, *a, **k: torch.fft.rfft(x.float(), *a, **k))(u_t, L2)
        y = (lambda x, *a, **k: torch.fft.irfft(x, *a, **k).to(torch.bfloat16))(Uf * Kf[None, :, :], L2)[:, :, :L]
        y = (lambda x, *d: x.permute(*d))(y, 0, 2, 1)
        return y + u * D_skip[None, None, :]

    a = _A_ref(A_x, A_wqkv, A_wo)
    b = _B_ref(B_u, B_log_A_real, B_A_imag, B_C_real, B_C_imag, B_log_dt, B_D_skip)
    return torch.concatenate([a, b], axis=-1)
```

```cpp
#include <hip/hip_runtime.h>
#include <hip/hip_bf16.h>
#define POS_BF16 1
typedef __hip_bfloat16 bf16;
__device__ __forceinline__ float b2f(bf16 x){ return __bfloat162float(x); }
__device__ __forceinline__ bf16 f2b(float x){ return __float2bfloat16(x); }
__device__ __forceinline__ float rb(float x){ return b2f(f2b(x)); }

__global__ void gemm_naive(const bf16* A, const bf16* B, bf16* C, int M, int N, int K, int ldc){
  __shared__ float As[16][17], Bs[16][17];
  int tx = threadIdx.x, ty = threadIdx.y;
  int row = blockIdx.y*16+ty, col = blockIdx.x*16+tx;
  float acc = 0.f;
  for(int k0=0;k0<K;k0+=16){
    As[ty][tx] = b2f(A[(size_t)row*K + k0+tx]);
    Bs[ty][tx] = b2f(B[(size_t)(k0+ty)*N + col]);
    __syncthreads();
    for(int k=0;k<16;k++) acc += As[ty][k]*Bs[k][tx];
    __syncthreads();
  }
  C[(size_t)row*ldc+col] = f2b(acc);
}
__device__ __forceinline__ float phi(float t){ float r = t>0.f ? rb(t+1.f) : rb(expf(t)); return rb(r+1e-6f); }

__global__ void kv_naive(const bf16* qkv, float* kv, float* z){
  int h = blockIdx.x; int d = threadIdx.x>>5, e = threadIdx.x&31;
  float acc=0.f, zz=0.f;
  for(int s=0;s<2048;s++){
    float pk = phi(b2f(qkv[(size_t)s*3072+1024+h*32+d]));
    float v = b2f(qkv[(size_t)s*3072+2048+h*32+e]);
    acc += pk*v; zz += pk;
  }
  kv[(h*32+d)*32+e] = rb(acc);
  if(e==0) z[h*32+d] = rb(zz);
}
__global__ void o_naive(const bf16* qkv, const float* kv, const float* z, bf16* o){
  int s = blockIdx.x; int h = threadIdx.x>>5, e = threadIdx.x&31;
  float num=0.f, den=0.f;
  for(int d=0;d<32;d++){
    float pq = phi(b2f(qkv[(size_t)s*3072+h*32+d]));
    num += pq*kv[(h*32+d)*32+e]; den += pq*z[h*32+d];
  }
  num = rb(num); den = rb(rb(den)+1e-6f);
  o[(size_t)s*1024+h*32+e] = f2b(num/den);
}
__global__ void kgen_naive(const bf16* lar, const bf16* aim, const bf16* cr, const bf16* ci, const bf16* ldt, float* Kout){
  int d = blockIdx.y; int l = blockIdx.x*256+threadIdx.x;
  float dt = rb(expf(b2f(ldt[d])));
  float pos = rb((float)l);
  double acc = 0.0;
  for(int n=0;n<128;n++){
    float ar = -rb(expf(b2f(lar[d*128+n])))*dt;
    float ai = b2f(aim[d*128+n])*dt;
    float c_r = b2f(cr[d*128+n])*dt, c_i = b2f(ci[d*128+n])*dt;
    double e = exp((double)(ar*pos));
    double ang = (double)(ai*pos);
    acc += e*(c_r*cos(ang) - c_i*sin(ang));
  }
  Kout[(size_t)d*2048+l] = 2.f*(float)acc;
}
__global__ void conv_naive(const bf16* u, const float* Kf, const bf16* dsk, bf16* out){
  int d = blockIdx.x*64 + (threadIdx.x&63); int t = blockIdx.y*4 + (threadIdx.x>>6);
  float acc=0.f;
  for(int j=0;j<=t;j++) acc += Kf[(size_t)d*2048+j]*b2f(u[(size_t)(t-j)*1024+d]);
  float y = rb(acc);
  float sk = rb(b2f(u[(size_t)t*1024+d])*b2f(dsk[d]));
  out[(size_t)t*2048+1024+d] = f2b(y+sk);
}
extern "C" void kernel_launch(void* const* d_in, const int* in_sizes, int n_in,
                              void* d_out, int out_size, void* d_ws, size_t ws_size,
                              hipStream_t stream) {
  const bf16* x=(const bf16*)d_in[0]; const bf16* wqkv=(const bf16*)d_in[1]; const bf16* wo=(const bf16*)d_in[2];
  const bf16* u=(const bf16*)d_in[3]; const bf16* lar=(const bf16*)d_in[4]; const bf16* aim=(const bf16*)d_in[5];
  const bf16* cr=(const bf16*)d_in[6]; const bf16* ci=(const bf16*)d_in[7]; const bf16* ldt=(const bf16*)d_in[8]; const bf16* dsk=(const bf16*)d_in[9];
  bf16* out=(bf16*)d_out;
  char* ws=(char*)d_ws;
  bf16* qkv=(bf16*)ws; ws += 2048*3072*2;
  bf16* o=(bf16*)ws; ws += 2048*1024*2;
  float* kv=(float*)ws; ws += 32*32*32*4;
  float* z=(float*)ws; ws += 1024*4;
  float* Kf=(float*)ws; ws += 1024*2048*4;
  gemm_naive<<<dim3(3072/16,2048/16),dim3(16,16),0,stream>>>(x,wqkv,qkv,2048,3072,1024,3072);
  kv_naive<<<32,1024,0,stream>>>(qkv,kv,z);
  o_naive<<<2048,1024,0,stream>>>(qkv,kv,z,o);
  gemm_naive<<<dim3(1024/16,2048/16),dim3(16,16),0,stream>>>(o,wo,out,2048,1024,1024,2048);
  kgen_naive<<<dim3(8,1024),256,0,stream>>>(lar,aim,cr,ci,ldt,Kf);
  conv_naive<<<dim3(16,512),256,0,stream>>>(u,Kf,dsk,out);
}
```

```cpp
#include <hip/hip_runtime.h>
#include <stdint.h>

#define LAS __attribute__((address_space(3)))
#define GAS __attribute__((address_space(1)))
#define DI __device__ __forceinline__

typedef unsigned short u16;
typedef short s16x4 __attribute__((ext_vector_type(4)));
typedef __bf16 bf16x2 __attribute__((ext_vector_type(2)));
typedef __bf16 bf16x8 __attribute__((ext_vector_type(8)));
typedef _Float16 f16x2 __attribute__((ext_vector_type(2)));
typedef _Float16 f16x8 __attribute__((ext_vector_type(8)));
typedef float f32x2 __attribute__((ext_vector_type(2)));
typedef float f32x4 __attribute__((ext_vector_type(4)));
typedef float f32x16 __attribute__((ext_vector_type(16)));
typedef unsigned u32x2 __attribute__((ext_vector_type(2)));
typedef unsigned u32x4 __attribute__((ext_vector_type(4)));

#define CBAR() asm volatile("" ::: "memory")

DI float bf2f(u16 v) { return __uint_as_float((unsigned)v << 16); }
DI unsigned pk_bf16(float a, float b) {
  f32x2 f = {a, b};
  return __builtin_bit_cast(unsigned, __builtin_convertvector(f, bf16x2));
}
DI unsigned pk_f16(float a, float b) {
  f32x2 f = {a, b};
  return __builtin_bit_cast(unsigned, __builtin_convertvector(f, f16x2));
}
DI float rbf(float a) { return __uint_as_float(pk_bf16(a, 0.f) << 16); }

DI float dot2bf(unsigned a, unsigned b, float c) {
  return __builtin_amdgcn_fdot2_f32_bf16(__builtin_bit_cast(bf16x2, a), __builtin_bit_cast(bf16x2, b), c, false);
}
DI u32x2 tr_read(LAS char* p) {
  s16x4 v = __builtin_amdgcn_ds_read_tr16_b64_v4i16((LAS s16x4*)p);
  return __builtin_bit_cast(u32x2, v);
}
DI bf16x8 tr_frag(LAS char* p0, LAS char* p1) {
  u32x2 a = tr_read(p0), b = tr_read(p1);
  u32x4 r = {a.x, a.y, b.x, b.y};
  return __builtin_bit_cast(bf16x8, r);
}
#define MFMA32(a, b, c) __builtin_amdgcn_mfma_f32_32x32x16_bf16((a), (b), (c), 0, 0, 0)
#define MFMA16(a, b, c) __builtin_amdgcn_mfma_f32_16x16x32_bf16((a), (b), (c), 0, 0, 0)
#define MFMA16H(a, b, c) __builtin_amdgcn_mfma_f32_16x16x32_f16((a), (b), (c), 0, 0, 0)
#define GLDS(src, dst) __builtin_amdgcn_global_load_lds((const GAS unsigned*)(src), (LAS unsigned*)(dst), 16, 0, 0)

constexpr size_t WS_PQ = 0;
constexpr size_t WS_KVP = 4u << 20;
constexpr int KVP_STRIDE = 1056;
constexpr size_t WS_KVR = WS_KVP + (size_t)32 * 16 * KVP_STRIDE * 4;
constexpr int KVR_STRIDE = 1088;
constexpr size_t WS_CNT = WS_KVR + (size_t)32 * KVR_STRIDE * 2;

constexpr int SLOT = 40960;
constexpr int L_KB = 3 * SLOT;
constexpr int KB_CH = 4160;
constexpr int L_UB = L_KB + 4 * KB_CH;
constexpr int UB_CH = 4352;
constexpr int L_FLAG = L_UB + 4 * UB_CH;
constexpr int K1_LDS = L_FLAG + 64;

struct cplx { float re, im; };
DI cplx cmul(cplx a, cplx b) {
  cplx r;
  r.re = __builtin_fmaf(a.re, b.re, -(a.im * b.im));
  r.im = __builtin_fmaf(a.re, b.im, a.im * b.re);
  return r;
}
DI cplx zpow(float arl2, float ai, float p) {
  const float INV2PI = 0.15915494309189535f;
  const float INV2PI_LO = (float)(0.15915494309189533576888 - (double)0.15915494309189535f);
  float e = __builtin_amdgcn_exp2f(arl2 * p);
  float r = ai * p;
  float hi = r * INV2PI;
  float lo = __builtin_fmaf(r, INV2PI_LO, __builtin_fmaf(r, INV2PI, -hi));
  float f = (hi - __builtin_rintf(hi)) + lo;
  cplx z;
  z.re = e * __builtin_amdgcn_cosf(f);
  z.im = e * __builtin_amdgcn_sinf(f);
  return z;
}

DI void lds_arrive(LAS unsigned* c, int lane) {
  CBAR();
  if (lane == 0) __hip_atomic_fetch_add(c, 1u, __ATOMIC_RELAXED, __HIP_MEMORY_SCOPE_WORKGROUP);
  CBAR();
}
DI void lds_wait_ge(volatile LAS unsigned* c, unsigned target) {
  CBAR();
  while (__builtin_amdgcn_readfirstlane(*c) < target) { __builtin_amdgcn_s_sleep(1); }
  CBAR();
}
DI unsigned min4(u32x4 f) {
  unsigned a = f.x < f.y ? f.x : f.y, b = f.z < f.w ? f.z : f.w;
  return __builtin_amdgcn_readfirstlane(a < b ? a : b);
}

DI float phi_f(float t) {
  float e = __builtin_amdgcn_exp2f(__builtin_fminf(t * 1.4426950408889634f, 0.f));
  return (__builtin_fmaxf(t, 0.f) + 1e-6f) + e;
}

DI void s4d_part(LAS char* sm, int c, int lane, int d0, const u16* __restrict__ u, const u16* __restrict__ lar,
                 const u16* __restrict__ aim, const u16* __restrict__ cre, const u16* __restrict__ cim,
                 const u16* __restrict__ ldt, const u16* __restrict__ dsk, u16* __restrict__ out) {
  const int tb = c * 64 + lane;
  const int d = d0 + c;
  LAS unsigned* cntB = (LAS unsigned*)(sm + L_FLAG + 32);
  float p_lr[2], p_ai[2], p_cr[2], p_ci[2];
#pragma unroll
  for (int hf = 0; hf < 2; ++hf) {
    const int idx = d * 128 + hf * 64 + lane;
    p_lr[hf] = bf2f(lar[idx]); p_ai[hf] = bf2f(aim[idx]); p_cr[hf] = bf2f(cre[idx]); p_ci[hf] = bf2f(cim[idx]);
  }
  const float v_ldt = bf2f(ldt[d]), v_dsk = bf2f(dsk[d]);
  u32x2 ur[2][4];
#pragma unroll
  for (int g = 0; g < 2; ++g)
#pragma unroll
    for (int i = 0; i < 4; ++i)
      ur[g][i] = *(const u32x2*)(u + (size_t)(4 * (tb + 256 * g) + i) * 1024 + d0);
  LAS char* ub = sm + L_UB + c * UB_CH;
  *(LAS unsigned*)(ub + lane * 4) = 0u;

  LAS char* kb = sm + L_KB + c * KB_CH;
  const float dt = rbf(expf(v_ldt));
  float mx = __builtin_fmaxf(__builtin_fmaxf(__builtin_fabsf(p_cr[0]), __builtin_fabsf(p_ci[0])),
                             __builtin_fmaxf(__builtin_fabsf(p_cr[1]), __builtin_fabsf(p_ci[1])));
#pragma unroll
  for (int o = 32; o >= 1; o >>= 1) mx = __builtin_fmaxf(mx, __shfl_xor(mx, o));
  const int ex = (mx > 0.f && mx < 3.0e38f) ? __builtin_amdgcn_frexp_expf(mx) : 0;
  const float mult = __builtin_amdgcn_ldexpf(2.f * dt, ex);

  const int wr_lo = (lane & 3) * 4, wr_s = lane >> 2;
  const int rr = lane & 15, rg = lane >> 4;
#define SCR_W(row, val) *(LAS unsigned*)(kb + (row) * 256 + (((wr_s ^ (row)) << 4) | wr_lo)) = (val)
#define SCR_R(ks) *(LAS f16x8*)(kb + rr * 256 + (((4 * (ks) + rg) ^ rr) << 4))
  f32x4 t0 = {0, 0, 0, 0}, t1 = {0, 0, 0, 0}, t2 = {0, 0, 0, 0};
#pragma unroll
  for (int hf = 0; hf < 2; ++hf) {
    const float ar = -rbf(expf(p_lr[hf])) * dt;
    const float ai = p_ai[hf] * dt;
    const float arl2 = ar * 1.4426950408889634f;
    const cplx z1 = zpow(arl2, ai, 1.f), z8 = zpow(arl2, ai, 8.f), z128 = zpow(arl2, ai, 128.f);
    f16x8 bfr[4];
    {
      cplx e = {1.f, 0.f};
      SCR_W(0, pk_f16(1.f, 0.f));
      e = z8;
      SCR_W(1, pk_f16(e.re, e.im));
#pragma unroll
      for (int k = 2; k < 16; ++k) { e = cmul(e, z8); SCR_W(k, pk_f16(e.re, e.im)); }
#pragma unroll
      for (int ks = 0; ks < 4; ++ks) bfr[ks] = SCR_R(ks);
    }
    cplx X[8], x16;
    {
      cplx x = {__builtin_amdgcn_ldexpf(p_cr[hf], -ex), __builtin_amdgcn_ldexpf(p_ci[hf], -ex)};
#pragma unroll
      for (int r = 0; r < 16; ++r) {
        if (r < 8) X[r] = x;
        SCR_W(r, pk_f16(x.re, -x.im));
        x = cmul(x, z128);
      }
      x16 = x;
#pragma unroll
      for (int ks = 0; ks < 4; ++ks) { f16x8 a = SCR_R(ks); t0 = MFMA16H(a, bfr[ks], t0); }
    }
    cplx zp[8];
    zp[1] = z1;
#pragma unroll
    for (int k = 2; k < 8; ++k) zp[k] = cmul(zp[k - 1], z1);
    {
      SCR_W(0, pk_f16(x16.re, -x16.im));
#pragma unroll
      for (int k = 1; k < 8; ++k) {
        cplx a = cmul(X[0], zp[k]), b = cmul(X[1], zp[k]);
        SCR_W(k, pk_f16(a.re, -a.im));
        SCR_W(8 + k, pk_f16(b.re, -b.im));
      }
#pragma unroll
      for (int ks = 0; ks < 4; ++ks) { f16x8 a = SCR_R(ks); t1 = MFMA16H(a, bfr[ks], t1); }
    }
    {
#pragma unroll
      for (int k = 0; k < 4; ++k) { cplx a = cmul(X[4 + k], zp[4]); SCR_W(k, pk_f16(a.re, -a.im)); }
#pragma unroll
      for (int k = 0; k < 3; ++k) {
        cplx a = cmul(X[2], zp[2 * k + 2]), b = cmul(X[3], zp[2 * k + 2]);
        SCR_W(4 + k, pk_f16(a.re, -a.im));
        SCR_W(8 + k, pk_f16(b.re, -b.im));
      }
#pragma unroll
      for (int ks = 0; ks < 4; ++ks) { f16x8 a = SCR_R(ks); t2 = MFMA16H(a, bfr[ks], t2); }
    }
  }
#undef SCR_W
#undef SCR_R
  {
    LAS u16* K = (LAS u16*)kb + 16;
    const int DUMMY = 2052;
#pragma unroll
    for (int i = 0; i < 4; ++i) {
      const int row = 4 * rg + i;
      float v0 = t0[i] * mult;
      if (i == 0) v0 += (lane == 0) ? v_dsk : 0.f;
      K[128 * row + 8 * rr] = (u16)pk_bf16(v0, 0.f);
      int p1 = (row >> 3) * 128 + (row & 7) + 8 * rr;
      if (i == 0) p1 = (lane == 0) ? 2048 : ((row & 7) == 0 ? DUMMY : p1);
      K[p1] = (u16)pk_bf16(t1[i] * mult, 0.f);
      int p2 = (rg == 0) ? (512 + 128 * i + 4 + 8 * rr) : (128 + 128 * rg + 2 * (i + 1) + 8 * rr);
      if (rg == 3 || (rg != 0 && i == 3)) p2 = DUMMY;
      K[p2] = (u16)pk_bf16(t2[i] * mult, 0.f);
    }
    if (lane < 8) *(LAS unsigned*)(kb + lane * 4) = 0u;
    const unsigned a1 = K[256 + 4 * lane], b1 = K[258 + 4 * lane], c1 = K[260 + 4 * lane];
    const unsigned a2 = K[512 + 8 * lane], b2 = K[516 + 8 * lane], c2 = K[520 + 8 * lane];
    const unsigned a3 = K[1024 + 16 * lane], b3 = K[1032 + 16 * lane], c3 = K[1040 + 16 * lane];
    u32x2 w1 = {a1 | (a1 << 16), b1 | (c1 << 16)};
    u32x4 w2 = {a2 | (a2 << 16), a2 | (b2 << 16), b2 | (b2 << 16), c2 | (c2 << 16)};
    u32x4 w3a = {a3 | (a3 << 16), a3 | (a3 << 16), a3 | (b3 << 16), b3 | (b3 << 16)};
    u32x4 w3b = {b3 | (b3 << 16), b3 | (b3 << 16), c3 | (c3 << 16), c3 | (c3 << 16)};
    *(LAS u32x2*)(K + 256 + 4 * lane) = w1;
    *(LAS u32x4*)(K + 512 + 8 * lane) = w2;
    *(LAS u32x4*)(K + 1024 + 16 * lane) = w3a;
    *(LAS u32x4*)(K + 1032 + 16 * lane) = w3b;
  }
  const int tq = (lane & 15) >> 2, tp = lane & 3;
  bf16x8 F[16];
  {
    LAS char* fb = kb + 2 * (16 - 12 + 32 * rg + 4 * (tq + tp));
#pragma unroll
    for (int s = 0; s < 16; ++s) F[s] = tr_frag(fb + 256 * s, fb + 256 * s + 32);
  }
#pragma unroll
  for (int g = 0; g < 2; ++g) {
    const int t4 = 4 * (tb + 256 * g);
    u32x2 c0 = {__builtin_amdgcn_perm(ur[g][1].x, ur[g][0].x, 0x05040100), __builtin_amdgcn_perm(ur[g][3].x, ur[g][2].x, 0x05040100)};
    u32x2 c1 = {__builtin_amdgcn_perm(ur[g][1].x, ur[g][0].x, 0x07060302), __builtin_amdgcn_perm(ur[g][3].x, ur[g][2].x, 0x07060302)};
    u32x2 c2 = {__builtin_amdgcn_perm(ur[g][1].y, ur[g][0].y, 0x05040100), __builtin_amdgcn_perm(ur[g][3].y, ur[g][2].y, 0x05040100)};
    u32x2 c3 = {__builtin_amdgcn_perm(ur[g][1].y, ur[g][0].y, 0x07060302), __builtin_amdgcn_perm(ur[g][3].y, ur[g][2].y, 0x07060302)};
    LAS char* w = sm + L_UB + 2 * (128 + t4);
    *(LAS u32x2*)(w + 0 * UB_CH) = c0;
    *(LAS u32x2*)(w + 1 * UB_CH) = c1;
    *(LAS u32x2*)(w + 2 * UB_CH) = c2;
    *(LAS u32x2*)(w + 3 * UB_CH) = c3;
  }
  asm volatile("s_waitcnt lgkmcnt(0)" ::: "memory");
  lds_arrive(cntB, lane);
  lds_wait_ge(cntB, 4);

  LAS char* gb = ub + 2 * (128 + 16 * tp + 12 - 32 * rg - 4 * tq);
  LAS u16* stage = (LAS u16*)(sm + L_KB) + c;
  float pc1 = 0.f, pc2 = 0.f, pc3 = 0.f;
  const bool l0 = (lane == 0);
#pragma unroll
  for (int pass = 0; pass < 2; ++pass) {
    f32x4 acc[16];
#pragma unroll
    for (int i = 0; i < 16; ++i) acc[i] = (f32x4){0, 0, 0, 0};
#pragma unroll
    for (int v = 0; v < 16 * pass + 16; ++v) {
      bf16x8 G = tr_frag(gb + 128 * v, gb + 128 * v - 32);
#pragma unroll
      for (int i = 0; i < 16; ++i) {
        const int T = 16 * pass + i;
        if (T >= v && ((T - v) & 1) == 0) acc[i] = MFMA16(G, F[(T - v) >> 1], acc[i]);
      }
    }
#pragma unroll
    for (int i = 0; i < 16; ++i) {
      const int T = 16 * pass + i;
      float r3 = __int_as_float(__builtin_amdgcn_update_dpp(0, __float_as_int(acc[i][3]), 0x13C, 0xf, 0xf, false));
      float s2 = acc[i][2] + (l0 ? pc3 : r3);
      float r2 = __int_as_float(__builtin_amdgcn_update_dpp(0, __float_as_int(s2), 0x13C, 0xf, 0xf, false));
      float s1 = acc[i][1] + (l0 ? pc2 : r2);
      float r1 = __int_as_float(__builtin_amdgcn_update_dpp(0, __float_as_int(s1), 0x13C, 0xf, 0xf, false));
      float y = acc[i][0] + (l0 ? pc1 : r1);
      pc1 = r1; pc2 = r2; pc3 = r3;
      stage[(64 * T + lane) * 4] = (u16)pk_bf16(y, 0.f);
    }
  }
  asm volatile("s_waitcnt lgkmcnt(0)" ::: "memory");
  lds_arrive(cntB, lane);
  lds_wait_ge(cntB, 8);
#pragma unroll
  for (int i = 0; i < 8; ++i) {
    const int t = tb + 256 * i;
    u32x2 v = *(LAS u32x2*)(sm + L_KB + t * 8);
    *(u32x2*)(out + (size_t)t * 2048 + 1024 + d0) = v;
  }
}

__global__ __launch_bounds__(768, 3) void k1_kernel(const u16* __restrict__ x, const u16* __restrict__ wqkv,
                                                    const u16* __restrict__ u, const u16* __restrict__ lar,
                                                    const u16* __restrict__ aim, const u16* __restrict__ cre,
                                                    const u16* __restrict__ cim, const u16* __restrict__ ldt,
                                                    const u16* __restrict__ dsk, u16* __restrict__ out,
                                                    char* __restrict__ ws) {
  __shared__ __attribute__((aligned(16))) char smem[K1_LDS];
  LAS char* sm = (LAS char*)smem;
  const int tid = threadIdx.x, lane = tid & 63;
  const int wave = __builtin_amdgcn_readfirstlane(tid >> 6);
  const int bid = blockIdx.x, xcd = bid & 7, idx = bid >> 3;
  const int mb = (xcd & 1) * 8 + (idx & 7), hp = (xcd >> 1) * 4 + (idx >> 3);
  volatile LAS unsigned* FULL = (volatile LAS unsigned*)(sm + L_FLAG);
  volatile LAS unsigned* FREE = (volatile LAS unsigned*)(sm + L_FLAG + 16);
  if (tid < 16) ((LAS unsigned*)(sm + L_FLAG))[tid] = 0u;
  if (bid == 0 && tid >= 64 && tid < 96)
    __hip_atomic_store((unsigned*)(ws + WS_CNT) + (tid - 64) * 32, 0u, __ATOMIC_RELAXED, __HIP_MEMORY_SCOPE_AGENT);
  __syncthreads();

  if (wave >= 8) {
    const int w = wave - 8;
    const u16* asrc = x + (size_t)(mb * 128 + 8 * w + (lane >> 3)) * 1024 + (((lane & 7) ^ ((4 * w + (lane >> 4)) & 7)) << 3);
    const u16* bsrc = wqkv + (size_t)(8 * w + (lane >> 3)) * 3072 + hp * 64 + (((lane & 7) ^ (((lane >> 4) & 1) << 2)) << 3);
    int pub = 0;
#pragma unroll 1
    for (int t = 0; t < 16; ++t) {
      const int slot = t % 3;
      if (t >= 3) {
        const unsigned need = (unsigned)(t - 2);
        if (min4(*(volatile LAS u32x4*)FREE) < need) {
          asm volatile("s_waitcnt vmcnt(0)" ::: "memory");
          pub = t;
          FULL[w] = (unsigned)t;
          while (min4(*(volatile LAS u32x4*)FREE) < need) { __builtin_amdgcn_s_sleep(1); }
        }
        CBAR();
      }
      LAS char* sb = sm + slot * SLOT;
#pragma unroll
      for (int j = 0; j < 4; ++j) GLDS(asrc + (size_t)j * 32 * 1024 + t * 64, sb + (4 * j + w) * 1024);
#pragma unroll
      for (int g = 0; g < 3; ++g)
#pragma unroll
        for (int jj = 0; jj < 2; ++jj)
          GLDS(bsrc + (size_t)(t * 64 + 32 * jj) * 3072 + g * 1024, sb + 16384 + g * 8192 + (w + 4 * jj) * 1024);
      if (pub < t) {
        asm volatile("s_waitcnt vmcnt(10)" ::: "memory");
        pub = t;
        FULL[w] = (unsigned)t;
      }
    }
    asm volatile("s_waitcnt vmcnt(0)" ::: "memory");
    FULL[w] = 16u;
    return;
  }
  if (wave >= 4) {
    s4d_part(sm, wave - 4, lane, bid * 4, u, lar, aim, cre, cim, ldt, dsk, out);
    return;
  }
  const int mh = wave & 1, hd = wave >> 1;
  const int r = lane & 31, h = lane >> 5, blk = (lane >> 4) & 1, q = (lane & 15) >> 2, p = lane & 3;
  int aoff[4];
#pragma unroll
  for (int s = 0; s < 4; ++s) aoff[s] = (mh * 64 + r) * 128 + (((2 * s + h) ^ ((r >> 1) & 7)) << 4);
  const int boff = 16384 + (8 * h + q) * 128 + (((hd * 4 + blk * 2 + (p >> 1)) ^ ((q >> 1) << 2)) << 4) + (p & 1) * 8;
  f32x16 acc[2][3];
#pragma unroll
  for (int m = 0; m < 2; ++m)
#pragma unroll
    for (int g = 0; g < 3; ++g)
#pragma unroll
      for (int i = 0; i < 16; ++i) acc[m][g][i] = 0.f;
  bf16x8 fa[2][2], fb[2][3];
#define LOADF(set, slot, s)                                                                               \
  do {                                                                                                    \
    LAS char* sb_ = sm + (slot) * SLOT;                                                                   \
    _Pragma("unroll") for (int m = 0; m < 2; ++m) fa[set][m] = *(LAS bf16x8*)(sb_ + aoff[s] + m * 4096);  \
    _Pragma("unroll") for (int g = 0; g < 3; ++g)                                                         \
        fb[set][g] = tr_frag(sb_ + boff + g * 8192 + (16 * (s)) * 128, sb_ + boff + g * 8192 + (16 * (s) + 4) * 128); \
  } while (0)
#define MMA(set)                                                           \
  do {                                                                     \
    _Pragma("unroll") for (int m = 0; m < 2; ++m) {                        \
      acc[m][0] = MFMA32(fb[set][0], fa[set][m], acc[m][0]);               \
      acc[m][1] = MFMA32(fa[set][m], fb[set][1], acc[m][1]);               \
      acc[m][2] = MFMA32(fa[set][m], fb[set][2], acc[m][2]);               \
    }                                                                      \
  } while (0)
  CBAR();
  while (min4(*(volatile LAS u32x4*)FULL) < 1u) { __builtin_amdgcn_s_sleep(1); }
  CBAR();
  LOADF(0, 0, 0);
#pragma unroll
  for (int t = 0; t < 16; ++t) {
    const int slot = t % 3;
    LOADF(1, slot, 1);
    MMA(0);
    LOADF(0, slot, 2);
    MMA(1);
    LOADF(1, slot, 3);
    CBAR();
    FREE[wave] = (unsigned)(t + 1);
    u32x4 fl = *(volatile LAS u32x4*)FULL;
    CBAR();
    MMA(0);
    if (t < 15) {
      if (min4(fl) < (unsigned)(t + 2)) {
        while (min4(*(volatile LAS u32x4*)FULL) < (unsigned)(t + 2)) {}
      }
      CBAR();
      LOADF(0, (t + 1) % 3, 0);
    }
    MMA(1);
  }
#undef LOADF
#undef MMA
  const int head = hp * 2 + hd;
  f32x16 kv;
#pragma unroll
  for (int i = 0; i < 16; ++i) kv[i] = 0.f;
  float zs = 0.f;
#pragma unroll
  for (int m = 0; m < 2; ++m) {
    const int stile = mb * 4 + mh * 2 + m;
    u32x4* pq = (u32x4*)(ws + WS_PQ) + ((size_t)(stile * 32 + head) * 2) * 64 + lane;
#pragma unroll
    for (int ks = 0; ks < 2; ++ks) {
      u32x4 fq, fk, fv;
#pragma unroll
      for (int j = 0; j < 4; ++j) {
        const int i0 = 8 * ks + 2 * j;
        fq[j] = pk_bf16(phi_f(acc[m][0][i0]), phi_f(acc[m][0][i0 + 1]));
        float k0 = phi_f(acc[m][1][i0]), k1 = phi_f(acc[m][1][i0 + 1]);
        zs += k0 + k1;
        fk[j] = pk_bf16(k0, k1);
        fv[j] = pk_bf16(acc[m][2][i0], acc[m][2][i0 + 1]);
      }
      pq[ks * 64] = fq;
      kv = MFMA32(__builtin_bit_cast(bf16x8, fk), __builtin_bit_cast(bf16x8, fv), kv);
    }
  }
  zs += __shfl_xor(zs, 32);
  LAS float* xb = (LAS float*)(sm + SLOT) + hd * 1088;
  LAS unsigned* cntE = (LAS unsigned*)(sm + L_FLAG + 48) + hd;
  if (mh == 1) {
    CBAR();
    while (min4(*(volatile LAS u32x4*)FREE) < 14u) {}
    CBAR();
#pragma unroll
    for (int i = 0; i < 4; ++i)
      *(LAS f32x4*)(xb + i * 256 + lane * 4) = (f32x4){kv[4 * i], kv[4 * i + 1], kv[4 * i + 2], kv[4 * i + 3]};
    xb[1024 + lane] = zs;
    asm volatile("s_waitcnt lgkmcnt(0)" ::: "memory");
    lds_arrive(cntE, lane);
  } else {
    lds_wait_ge(cntE, 1);
    float* dst = (float*)(ws + WS_KVP) + (size_t)(head * 16 + mb) * KVP_STRIDE;
#pragma unroll
    for (int i = 0; i < 4; ++i) {
      f32x4 o = *(LAS f32x4*)(xb + i * 256 + lane * 4);
      o += (f32x4){kv[4 * i], kv[4 * i + 1], kv[4 * i + 2], kv[4 * i + 3]};
      *(f32x4*)(dst + (i >> 1) * 512 + lane * 8 + (i & 1) * 4) = o;
    }
    float z = zs + xb[1024 + lane];
    if (lane < 32) dst[1024 + lane] = z;
  }
}

constexpr int K2_LDS = 131072 + 64;
__global__ __launch_bounds__(512, 2) void k2_kernel(const u16* __restrict__ wo, u16* __restrict__ out,
                                                    char* __restrict__ ws) {
  __shared__ __attribute__((aligned(16))) char smem[K2_LDS];
  LAS char* sm = (LAS char*)smem;
  const int tid = threadIdx.x, lane = tid & 63;
  const int wave = __builtin_amdgcn_readfirstlane(tid >> 6);
  const int bid = blockIdx.x, xcd = bid & 7, idx = bid >> 3;
  const int mb = (xcd & 3) * 4 + (idx & 3), nb = (xcd >> 2) * 8 + (idx >> 2);
  unsigned* cnt = (unsigned*)(ws + WS_CNT);
  if (wave < 3) {
    const int rh = bid >> 3, part = bid & 7;
    const bool act = (wave < 2) || (part == 0 && lane < 32);
    if (wave < 2 || part == 0) {
      const int e = (wave < 2) ? (part * 128 + wave * 64 + lane) : (1024 + (lane & 31));
      const float* src = (const float*)(ws + WS_KVP) + (size_t)rh * 16 * KVP_STRIDE + e;
      float v[16];
#pragma unroll
      for (int i = 0; i < 16; ++i) v[i] = src[(size_t)i * KVP_STRIDE];
      float s = 0.f;
#pragma unroll
      for (int i = 0; i < 16; ++i) s += v[i];
      int dste = e;
      if (wave == 2) {
        const int dd = lane & 31;
        dste = 1024 + (dd >> 4) * 16 + ((dd >> 2) & 1) * 8 + ((dd >> 3) & 1) * 4 + (dd & 3);
      }
      float s2 = __shfl_xor(s, 1);
      unsigned pk = pk_bf16(s, s2);
      unsigned* dst = (unsigned*)(ws + WS_KVR) + (rh * KVR_STRIDE + dste) / 2;
      if (act && (lane & 1) == 0) __hip_atomic_store(dst, pk, __ATOMIC_RELAXED, __HIP_MEMORY_SCOPE_AGENT);
      asm volatile("s_waitcnt vmcnt(0)" ::: "memory");
      if (lane == 0) __hip_atomic_fetch_add(cnt + rh * 32, 1u, __ATOMIC_RELAXED, __HIP_MEMORY_SCOPE_AGENT);
    }
  }
  {
    const u16* src = wo + (size_t)(8 * wave + (lane >> 3)) * 1024 + nb * 64 + (((lane & 7) ^ (((lane >> 4) & 1) << 2)) << 3);
#pragma unroll
    for (int i = 0; i < 16; ++i) GLDS(src + (size_t)i * 64 * 1024, sm + (wave + 8 * i) * 1024);
  }
  const int rt = wave & 3, kh = wave >> 2;
  const int stile = mb * 4 + rt;
  u32x4 pq[16][2];
  {
    const u32x4* src = (const u32x4*)(ws + WS_PQ) + ((size_t)(stile * 32 + kh * 16) * 2) * 64 + lane;
#pragma unroll
    for (int hh = 0; hh < 16; ++hh) { pq[hh][0] = src[hh * 128]; pq[hh][1] = src[hh * 128 + 64]; }
  }
  if (wave == 0) {
    unsigned* c = cnt + (lane & 31) * 32;
    while (true) {
      unsigned v = __hip_atomic_load(c, __ATOMIC_RELAXED, __HIP_MEMORY_SCOPE_AGENT);
      if (__all(v >= 17u)) break;
      __builtin_amdgcn_s_sleep(2);
    }
    __builtin_amdgcn_fence(__ATOMIC_ACQUIRE, "agent");
  }
  asm volatile("s_waitcnt vmcnt(0)" ::: "memory");
  __syncthreads();

  const int h = lane >> 5, blk = (lane >> 4) & 1, q = (lane & 15) >> 2, p = lane & 3;
  int woff[2];
#pragma unroll
  for (int nt = 0; nt < 2; ++nt)
    woff[nt] = (kh * 512 + 4 * h + q) * 128 + (((nt * 4 + blk * 2 + (p >> 1)) ^ ((q >> 1) << 2)) << 4) + (p & 1) * 8;
  f32x16 acc[2];
#pragma unroll
  for (int i = 0; i < 16; ++i) { acc[0][i] = 0.f; acc[1][i] = 0.f; }
  const u32x4* kvr = (const u32x4*)(ws + WS_KVR + (size_t)(kh * 16) * KVR_STRIDE * 2);
#pragma unroll
  for (int hh = 0; hh < 16; ++hh) {
    const u32x4* kb = kvr + hh * (KVR_STRIDE / 8);
    u32x4 k0 = kb[lane], k1 = kb[64 + lane];
    u32x4 z0 = kb[128 + h], z1 = kb[130 + h];
    f32x16 X;
#pragma unroll
    for (int i = 0; i < 16; ++i) X[i] = 0.f;
    X = MFMA32(__builtin_bit_cast(bf16x8, k0), __builtin_bit_cast(bf16x8, pq[hh][0]), X);
    X = MFMA32(__builtin_bit_cast(bf16x8, k1), __builtin_bit_cast(bf16x8, pq[hh][1]), X);
    float d0 = 0.f, d1 = 0.f;
#pragma unroll
    for (int j = 0; j < 4; ++j) {
      d0 = dot2bf(pq[hh][0][j], z0[j], d0);
      d1 = dot2bf(pq[hh][1][j], z1[j], d1);
    }
    float den = d0 + d1;
    den += __shfl_xor(den, 32);
    const float rc = __builtin_amdgcn_rcpf(den + 1e-6f);
#pragma unroll
    for (int ks = 0; ks < 2; ++ks) {
      u32x4 of;
#pragma unroll
      for (int j = 0; j < 4; ++j) of[j] = pk_bf16(X[8 * ks + 2 * j] * rc, X[8 * ks + 2 * j + 1] * rc);
#pragma unroll
      for (int nt = 0; nt < 2; ++nt) {
        LAS char* wp = sm + woff[nt] + (hh * 32 + 16 * ks) * 128;
        bf16x8 wf = tr_frag(wp, wp + 8 * 128);
        acc[nt] = MFMA32(wf, __builtin_bit_cast(bf16x8, of), acc[nt]);
      }
    }
  }
  __syncthreads();
  {
    LAS f32x4* xb = (LAS f32x4*)sm + (wave * 4) * 64 + lane;
#pragma unroll
    for (int i = 0; i < 4; ++i) {
      f32x4 s0 = {acc[0][4 * i], acc[0][4 * i + 1], acc[0][4 * i + 2], acc[0][4 * i + 3]};
      f32x4 s1 = {acc[1][4 * i], acc[1][4 * i + 1], acc[1][4 * i + 2], acc[1][4 * i + 3]};
      xb[i * 64] = kh ? s0 : s1;
    }
  }
  __syncthreads();
  {
    LAS f32x4* xr = (LAS f32x4*)sm + ((wave ^ 4) * 4) * 64 + lane;
    u16* orow = out + (size_t)(stile * 32 + (lane & 31)) * 2048 + nb * 64 + kh * 32 + 4 * h;
#pragma unroll
    for (int i = 0; i < 4; ++i) {
      f32x4 o = xr[i * 64];
      f32x4 s0 = {acc[0][4 * i], acc[0][4 * i + 1], acc[0][4 * i + 2], acc[0][4 * i + 3]};
      f32x4 s1 = {acc[1][4 * i], acc[1][4 * i + 1], acc[1][4 * i + 2], acc[1][4 * i + 3]};
      o += kh ? s1 : s0;
      u32x2 w = {pk_bf16(o[0], o[1]), pk_bf16(o[2], o[3])};
      *(u32x2*)(orow + 8 * i) = w;
    }
  }
}

extern "C" void kernel_launch(void* const* d_in, const int* in_sizes, int n_in, void* d_out, int out_size,
                              void* d_ws, size_t ws_size, hipStream_t stream) {
  const u16* x = (const u16*)d_in[0];
  const u16* wqkv = (const u16*)d_in[1];
  const u16* wo = (const u16*)d_in[2];
  const u16* u = (const u16*)d_in[3];
  const u16* lar = (const u16*)d_in[4];
  const u16* aim = (const u16*)d_in[5];
  const u16* cre = (const u16*)d_in[6];
  const u16* cim = (const u16*)d_in[7];
  const u16* ldt = (const u16*)d_in[8];
  const u16* dsk = (const u16*)d_in[9];
  u16* out = (u16*)d_out;
  char* ws = (char*)d_ws;
  k1_kernel<<<256, 768, 0, stream>>>(x, wqkv, u, lar, aim, cre, cim, ldt, dsk, out, ws);
  k2_kernel<<<256, 512, 0, stream>>>(wo, out, ws);
}
```

```cpp
#include <hip/hip_runtime.h>
#include <stdint.h>

#define LAS __attribute__((address_space(3)))
#define GAS __attribute__((address_space(1)))
#define DI __device__ __forceinline__

typedef unsigned short u16;
typedef short s16x4 __attribute__((ext_vector_type(4)));
typedef __bf16 bf16x2 __attribute__((ext_vector_type(2)));
typedef __bf16 bf16x8 __attribute__((ext_vector_type(8)));
typedef _Float16 f16x2 __attribute__((ext_vector_type(2)));
typedef _Float16 f16x8 __attribute__((ext_vector_type(8)));
typedef float f32x2 __attribute__((ext_vector_type(2)));
typedef float f32x4 __attribute__((ext_vector_type(4)));
typedef float f32x16 __attribute__((ext_vector_type(16)));
typedef unsigned u32x2 __attribute__((ext_vector_type(2)));
typedef unsigned u32x4 __attribute__((ext_vector_type(4)));
typedef __amdgpu_buffer_rsrc_t rsrc_t;

#define CBAR() asm volatile("" ::: "memory")
#define SB() __builtin_amdgcn_sched_barrier(0)
#define WAITVM(n) asm volatile("s_waitcnt vmcnt(" #n ")" ::: "memory")
#define TSDECL
#define TS(i)
#define TC(i)
#define TSDUMP()
#define PRIO_C 2
#define PRIO_L 3
#define NRING 12

DI float bf2f(u16 v) { return __uint_as_float((unsigned)v << 16); }
DI unsigned pk_bf16(float a, float b) {
  f32x2 f = {a, b};
  return __builtin_bit_cast(unsigned, __builtin_convertvector(f, bf16x2));
}
DI unsigned pk_f16(float a, float b) {
  f32x2 f = {a, b};
  return __builtin_bit_cast(unsigned, __builtin_convertvector(f, f16x2));
}
DI float rbf(float a) { return __uint_as_float(pk_bf16(a, 0.f) << 16); }
DI u32x2 tr_read(LAS char* p) {
  s16x4 v = __builtin_amdgcn_ds_read_tr16_b64_v4i16((LAS s16x4*)p);
  return __builtin_bit_cast(u32x2, v);
}
DI u32x4 join(u32x2 a, u32x2 b) { u32x4 r = {a.x, a.y, b.x, b.y}; return r; }
DI u32x4 tr_pair(LAS char* p0, LAS char* p1) { return join(tr_read(p0), tr_read(p1)); }
DI bf16x8 asbf(u32x4 v) { return __builtin_bit_cast(bf16x8, v); }
DI bf16x8 tr_frag(LAS char* p0, LAS char* p1) { return asbf(tr_pair(p0, p1)); }
#define MFMA32(a, b, c) __builtin_amdgcn_mfma_f32_32x32x16_bf16((a), (b), (c), 0, 0, 0)
#define MFMA16(a, b, c) __builtin_amdgcn_mfma_f32_16x16x32_bf16((a), (b), (c), 0, 0, 0)
#define MFMA16H(a, b, c) __builtin_amdgcn_mfma_f32_16x16x32_f16((a), (b), (c), 0, 0, 0)
#define AMFMA16(c, a, b) asm volatile("v_mfma_f32_16x16x32_bf16 %0, %1, %2, %0" : "+v"(c) : "v"(a), "v"(b))
#define GLDS(src, dst) __builtin_amdgcn_global_load_lds((const GAS unsigned*)(src), (LAS unsigned*)(dst), 16, 0, 0)
#define SC1 16
DI u32x4 ld16(rsrc_t r, int voff, int soff) { return __builtin_amdgcn_raw_buffer_load_b128(r, voff, soff, SC1); }
DI void st16(u32x4 v, rsrc_t r, int voff, int soff) {
  __builtin_amdgcn_raw_buffer_store_b128(v, r, voff, soff, SC1);
  asm volatile("s_nop 1" :: "v"(v) : "memory");
}

constexpr int WS_EPOCH = 0;
constexpr int WS_FU = 1024, WS_FP = 3072, WS_FY = 5120, WS_FR = 7168;
constexpr int WS_PQ = 65536;
constexpr int WS_UT = WS_PQ + (4 << 20);
constexpr int WS_YT = WS_UT + (4 << 20);
constexpr int WS_KVP = WS_YT + (4 << 20);
constexpr int WS_ZP = WS_KVP + 32 * 16 * 4096;
constexpr int WS_KVR = WS_ZP + 32 * 16 * 128;
constexpr int WS_END = WS_KVR + 32 * 2048;

constexpr int SLOT = 40960;
constexpr int L_UB = 131072, UB_CH = 4352;
constexpr int L_X = L_UB + 4 * UB_CH;
constexpr int L_Z = L_X + 2 * 4352;
constexpr int L_FLAG = L_Z + 512;
constexpr int LDS_BYTES = L_FLAG + 256;
enum { F_FULL = 0, F_FREE = 4, F_XE = 8, F_EQ = 10, F_LW, F_TAG, F_TU, F_TS, F_CY, F_T2, F_LM = 20 };

struct cplx { float re, im; };
DI cplx cmul(cplx a, cplx b) {
  cplx r;
  r.re = __builtin_fmaf(a.re, b.re, -(a.im * b.im));
  r.im = __builtin_fmaf(a.re, b.im, a.im * b.re);
  return r;
}
DI cplx zpow(float arl2, float ai, float p) {
  const float INV2PI = 0.15915494309189535f;
  const float INV2PI_LO = (float)(0.15915494309189533576888 - (double)0.15915494309189535f);
  float e = __builtin_amdgcn_exp2f(arl2 * p);
  float r = ai * p;
  float hi = r * INV2PI;
  float lo = __builtin_fmaf(r, INV2PI_LO, __builtin_fmaf(r, INV2PI, -hi));
  float f = (hi - __builtin_rintf(hi)) + lo;
  cplx z;
  z.re = e * __builtin_amdgcn_cosf(f);
  z.im = e * __builtin_amdgcn_sinf(f);
  return z;
}
DI float phi_f(float t) {
  float e = __builtin_amdgcn_fmed3f(__builtin_amdgcn_exp2f(t * 1.4426950408889634f), 0.f, 1.f);
  return (__builtin_amdgcn_fmed3f(t, 0.f, __builtin_inff()) + 1e-6f) + e;
}
DI u32x4 tag_put(f32x4 v, unsigned tag) {
  u32x4 b = __builtin_bit_cast(u32x4, v), r;
  r[0] = __builtin_amdgcn_perm(b[0], tag, 0x07060500u);
  r[1] = __builtin_amdgcn_perm(b[1], tag, 0x07060501u);
  r[2] = __builtin_amdgcn_perm(b[2], tag, 0x07060502u);
  r[3] = __builtin_amdgcn_perm(b[3], tag, 0x07060503u);
  return r;
}
DI unsigned tag_get(u32x4 b) {
  unsigned lo = __builtin_amdgcn_perm(b[1], b[0], 0x0c0c0400u), hi = __builtin_amdgcn_perm(b[3], b[2], 0x04000c0cu);
  return lo | hi;
}
DI f32x4 tag_val(u32x4 b) { return __builtin_bit_cast(f32x4, b & 0xffffff00u); }

DI void sig(LAS unsigned* f) {
  asm volatile("s_mov_b64 exec, 1\n\tds_add_u32 %0, %1\n\ts_mov_b64 exec, -1" :: "v"((unsigned)(size_t)f), "v"(1u) : "memory");
}
DI unsigned sig_ret(LAS unsigned* f) {
  unsigned r;
  asm volatile("s_mov_b64 exec, 1\n\tds_add_rtn_u32 %0, %1, %2\n\ts_mov_b64 exec, -1\n\ts_waitcnt lgkmcnt(0)"
               : "=&v"(r) : "v"((unsigned)(size_t)f), "v"(1u) : "memory");
  return __builtin_amdgcn_readfirstlane(r);
}
DI unsigned ldsword(LAS unsigned* f) { return __builtin_amdgcn_readfirstlane(*(volatile LAS unsigned*)f); }
DI void waitge(LAS unsigned* f, unsigned n) {
  CBAR();
#pragma unroll 1
  for (int it = 0; it < (1 << 22); ++it) {
    if (ldsword(f) >= n) break;
    __builtin_amdgcn_s_sleep(1);
  }
  CBAR();
}
DI void gflag(char* ws, int off, unsigned tag, int lane) {
  CBAR();
  if (lane == 0) __hip_atomic_store((unsigned*)(ws + off), tag, __ATOMIC_RELAXED, __HIP_MEMORY_SCOPE_AGENT);
  CBAR();
}
DI void gpoll(unsigned* p, unsigned tag) {
  CBAR();
#pragma unroll 1
  for (int it = 0; it < (1 << 18); ++it) {
    unsigned v = __hip_atomic_load(p, __ATOMIC_RELAXED, __HIP_MEMORY_SCOPE_AGENT);
    if (__all(v == tag)) break;
    __builtin_amdgcn_s_sleep(2);
  }
  CBAR();
}

__global__ __launch_bounds__(768, 3) void fused_kernel(const u16* __restrict__ x, const u16* __restrict__ wqkv,
                                                       const u16* __restrict__ wo, const u16* __restrict__ u,
                                                       const u16* __restrict__ lar, const u16* __restrict__ aim,
                                                       const u16* __restrict__ cre, const u16* __restrict__ cim,
                                                       const u16* __restrict__ ldt, const u16* __restrict__ dsk,
                                                       u16* __restrict__ out, char* __restrict__ ws) {
  __shared__ __attribute__((aligned(16))) char smem[LDS_BYTES];
  LAS char* sm = (LAS char*)smem;
  LAS unsigned* FL = (LAS unsigned*)(sm + L_FLAG);
  const int tid = threadIdx.x, lane = tid & 63;
  const int wave = __builtin_amdgcn_readfirstlane(tid >> 6);
  const int bid = blockIdx.x, xcd = bid & 7, idx = bid >> 3;
  const int ml = idx & 7, hl = idx >> 3;
  const int mb = (xcd & 1) * 8 + ml, hp = (xcd >> 1) * 4 + hl;
  const int mb2 = (xcd & 3) * 4 + (idx & 3), nb = (xcd >> 2) * 8 + (idx >> 2);
  const int rot = (idx >> 2) * 4;
  const rsrc_t rw = __builtin_amdgcn_make_buffer_rsrc((void*)ws, 0, WS_END, 0x00020000);
  TSDECL; TS(0);
  const int q4 = (lane & 15) >> 2, p4 = lane & 3, g4 = lane >> 4;

  if ((wave >> 2) == 1) {
    __builtin_amdgcn_s_setprio(PRIO_L);
    const int hw = wave - 4;
    unsigned ep = 0;
    if (hw == 0) ep = __hip_atomic_load((unsigned*)(ws + WS_EPOCH), __ATOMIC_RELAXED, __HIP_MEMORY_SCOPE_AGENT);
    const int koff = 2 * ml + 4 * hl + (hl >> 1);
    const unsigned vx = (unsigned)((mb * 128 + hw * 32 + (lane >> 3)) * 2048);
    const unsigned sx0 = ((lane & 7) ^ (lane >> 4)) << 4, sx1 = sx0 ^ 64;
    const unsigned vw = (unsigned)((lane >> 3) * 6144 + hp * 128 + (((lane & 7) ^ (((lane >> 4) & 1) << 2)) << 4));
#define ILIM 1
#define ISSUE(tt_, slot_)                                                                                  \
  do {                                                                                                     \
    const int k_ = ((tt_) + koff) & 15;                                                                    \
    const char* xa_ = (const char*)x + k_ * 128;                                                           \
    const char* wa_ = (const char*)wqkv + (size_t)k_ * 393216;                                             \
    LAS char* sb_ = sm + (slot_) * SLOT;                                                                   \
    _Pragma("unroll") for (int i = 0; i < 4 * ILIM; ++i)                                                   \
        GLDS(xa_ + i * 16384 + (vx + ((i & 1) ? sx1 : sx0)), sb_ + (hw * 4 + i) * 1024);                   \
    _Pragma("unroll") for (int i = 0; i < 6 * ILIM; ++i) {                                                 \
      const int qq_ = hw * 6 + i;                                                                          \
      GLDS(wa_ + ((qq_ & 7) * 49152 + (qq_ >> 3) * 2048) + vw, sb_ + 16384 + qq_ * 1024);                  \
    }                                                                                                      \
  } while (0)
    ISSUE(0, 0);
    __builtin_amdgcn_s_barrier();
    CBAR();
    TS(13);
    ISSUE(1, 1);
    ISSUE(2, 2);
    TS(14);
    WAITVM(20);
    if (hw == 0) {
      const unsigned tg = __builtin_amdgcn_readfirstlane(ep) * 2654435761u + 0x9E3779B1u;
      *(volatile LAS unsigned*)(FL + F_TAG) = tg;
    }
    sig(FL + F_FULL);
    TS(1);
    {
      int sp = 1, sf = 0, needf = 4;
#pragma unroll 1
      for (int p = 1; p < 14; ++p) {
        WAITVM(10);
        sig(FL + F_FULL + sp);
        waitge(FL + F_FREE + sf, (unsigned)needf);
        ISSUE(p + 2, sf);
        sf = sp;
        if (sf == 0) needf += 4;
        sp = (sp == 2) ? 0 : sp + 1;
      }
    }
#undef ISSUE
    TS(2);
    {
      const char* src = (const char*)wo + (size_t)((8 * hw + (lane >> 3)) * 2048 + nb * 128 + (((lane & 7) ^ (((lane >> 4) & 1) << 2)) << 4));
#define WOP(s_) GLDS(src + (size_t)((((s_) + rot) & 31) * 65536), sm + ((s_) * 4 + hw) * 1024)
      WOP(30); WOP(31);
      WAITVM(12);
      sig(FL + F_FULL + 2);
      waitge(FL + F_FREE + 1, 20);
#pragma unroll
      for (int i = 10; i < 20; ++i) WOP(i);
      WAITVM(12);
      sig(FL + F_FULL + 0);
      waitge(FL + F_FREE + 2, 20);
#pragma unroll
      for (int i = 20; i < 30; ++i) WOP(i);
      waitge(FL + F_FREE + 0, 24);
#pragma unroll
      for (int i = 0; i < 10; ++i) WOP(i);
#undef WOP
      TS(3);
      WAITVM(0);
      sig(FL + F_LW);
    }
    TS(4);
    const unsigned tag = ldsword(FL + F_TAG);
    if (hw == 0) {
      const int rh = bid >> 3, part = bid & 7;
      const int ks = part >> 2, ln = (part & 3) * 16 + (lane & 15);
      const int so = WS_KVP + rh * 65536 + ks * 2048;
      const int vo = g4 * 16384 + ln * 16;
      u32x4 a[8];
#pragma unroll 1
      for (int it = 0; it < (1 << 17); ++it) {
        CBAR();
#pragma unroll
        for (int i = 0; i < 8; ++i) a[i] = ld16(rw, vo + (i >> 1) * 4096 + (i & 1) * 1024, so);
        unsigned bad = 0;
#pragma unroll
        for (int i = 0; i < 8; ++i) bad |= tag_get(a[i]) ^ tag;
        if (__all(bad == 0u)) break;
        __builtin_amdgcn_s_sleep(4);
      }
      TS(5);
      f32x4 s0 = (tag_val(a[0]) + tag_val(a[2])) + (tag_val(a[4]) + tag_val(a[6]));
      f32x4 s1 = (tag_val(a[1]) + tag_val(a[3])) + (tag_val(a[5]) + tag_val(a[7]));
#pragma unroll
      for (int o = 16; o <= 32; o <<= 1)
#pragma unroll
        for (int i = 0; i < 4; ++i) { s0[i] += __shfl_xor(s0[i], o); s1[i] += __shfl_xor(s1[i], o); }
      u32x4 w = {pk_bf16(s0[0], s0[1]), pk_bf16(s0[2], s0[3]), pk_bf16(s1[0], s1[1]), pk_bf16(s1[2], s1[3])};
      if (lane < 16) st16(w, rw, (ks * 64 + ln) * 16, WS_KVR + rh * 2048);
      WAITVM(0);
      gflag(ws, WS_FR + bid * 4, tag, lane);
      TS(6);
    }
    gpoll((unsigned*)(ws + WS_FR) + (((hw + 4 * (lane >> 3) + rot) & 31) * 8 + (lane & 7)), tag);
    TS(7);
    {
      u32x4 kf[8][2];
#pragma unroll
      for (int i = 0; i < 8; ++i)
#pragma unroll
        for (int ks = 0; ks < 2; ++ks) kf[i][ks] = ld16(rw, ks * 1024 + lane * 16, WS_KVR + ((hw + 4 * i + rot) & 31) * 2048);
      waitge(FL + F_LW, 4);
      const int h = lane >> 5, blk = (lane >> 4) & 1, n = lane & 31;
      LAS char* wb = sm + hw * 4096;
      const int w0 = (4 * h + q4) * 128 + (((blk * 2 + (p4 >> 1)) ^ ((q4 >> 1) << 2)) << 4) + (p4 & 1) * 8;
      const int m0 = ((n >> 2) & 1) * 1024 + (h * 16 + 4 * (n >> 3) + (n & 3)) * 16;
#pragma unroll
      for (int i = 0; i < 8; ++i) {
        bf16x8 wf[2][2];
#pragma unroll
        for (int nt = 0; nt < 2; ++nt)
#pragma unroll
          for (int ks = 0; ks < 2; ++ks) {
            LAS char* pp = wb + i * 16384 + ks * 2048 + (w0 ^ (nt << 6));
            wf[nt][ks] = tr_frag(pp, pp + 1024);
          }
#pragma unroll
        for (int nt = 0; nt < 2; ++nt) {
          f32x16 D;
#pragma unroll
          for (int e = 0; e < 16; ++e) D[e] = 0.f;
          D = MFMA32(asbf(kf[i][0]), wf[nt][0], D);
          D = MFMA32(asbf(kf[i][1]), wf[nt][1], D);
#pragma unroll
          for (int ks = 0; ks < 2; ++ks) {
            u32x4 mw = {pk_bf16(D[8 * ks], D[8 * ks + 1]), pk_bf16(D[8 * ks + 2], D[8 * ks + 3]),
                        pk_bf16(D[8 * ks + 4], D[8 * ks + 5]), pk_bf16(D[8 * ks + 6], D[8 * ks + 7])};
            *(LAS u32x4*)(wb + i * 16384 + nt * 2048 + ks * 512 + m0) = mw;
          }
        }
        if (i & 1) sig(FL + F_LM + (i >> 1));
      }
    }
    TS(9);
    TSDUMP();
    return;
  }

  if (wave < 4) {
    if (wave == 0) FL[lane] = 0u;
    asm volatile("s_waitcnt lgkmcnt(0)" ::: "memory");
    __builtin_amdgcn_s_barrier();
    CBAR();
    __builtin_amdgcn_s_setprio(PRIO_C);
    const int mh = wave & 1, hd = wave >> 1;
    const int r32 = lane & 31, h = lane >> 5, blk = (lane >> 4) & 1;
    const int a0 = (mh * 64 + r32) * 128 + ((h ^ ((r32 >> 1) & 7)) << 4);
    const int b0 = 16384 + (8 * h + q4) * 128 + (((hd * 4 + blk * 2 + (p4 >> 1)) ^ ((q4 >> 1) << 2)) << 4) + (p4 & 1) * 8;
    f32x16 acc[2][3];
#pragma unroll
    for (int m = 0; m < 2; ++m)
#pragma unroll
      for (int g = 0; g < 3; ++g)
#pragma unroll
        for (int i = 0; i < 16; ++i) acc[m][g][i] = 0.f;
    bf16x8 fa[2][2];
    u32x2 fl[2][3], fh[2][3];
#define RA(set, m, so, s) fa[set][m] = *(LAS bf16x8*)(sm + ((a0 ^ ((s) << 5)) + (so)) + (m) * 4096)
#define RL(set, g, so, s) fl[set][g] = tr_read(sm + (b0 + (so)) + (g) * 8192 + (s) * 2048)
#define RH(set, g, so, s) fh[set][g] = tr_read(sm + (b0 + (so)) + (g) * 8192 + (s) * 2048 + 512)
#define FB(set, g) asbf(join(fl[set][g], fh[set][g]))
#define MQ(set, m) acc[m][0] = MFMA32(FB(set, 0), fa[set][m], acc[m][0])
#define MK(set, m, g) acc[m][g] = MFMA32(fa[set][m], FB(set, g), acc[m][g])
#define STEP(c, n, so, s, EXTRA)                                   \
  do {                                                             \
    MQ(c, 0);    RA(n, 0, so, s); RL(n, 0, so, s); SB();           \
    MK(c, 0, 1); RH(n, 0, so, s); RL(n, 1, so, s); SB();           \
    MK(c, 0, 2); RH(n, 1, so, s); RL(n, 2, so, s); SB();           \
    MQ(c, 1);    RH(n, 2, so, s); RA(n, 1, so, s); SB();           \
    MK(c, 1, 1); EXTRA; SB();                                      \
    MK(c, 1, 2); SB();                                             \
  } while (0)
    waitge(FL + F_FULL, 4);
    TS(3); TC(0);
    RA(0, 0, 0, 0); RL(0, 0, 0, 0); RH(0, 0, 0, 0); RL(0, 1, 0, 0); RH(0, 1, 0, 0); RL(0, 2, 0, 0); RH(0, 2, 0, 0); RA(0, 1, 0, 0);
    {
      int slot = 0, need = 4;
#pragma unroll 1
      for (int t = 0; t < 16; ++t) {
        const int so = slot * SLOT;
        const int ns = (slot == 2) ? 0 : slot + 1;
        if (ns == 0) need += 4;
        if (t == 15) need = 0;
        unsigned fv;
        SB();
        STEP(0, 1, so, 1, );
        STEP(1, 0, so, 2, { fv = *(volatile LAS unsigned*)(FL + F_FULL + ns); });
        STEP(0, 1, so, 3, { sig(FL + F_FREE + slot); });
        if (__builtin_amdgcn_readfirstlane(fv) < (unsigned)need) {
#pragma unroll 1
          for (int it = 0; it < (1 << 24); ++it) { if (ldsword(FL + F_FULL + ns) >= (unsigned)need) break; }
        }
        CBAR();
        SB();
        STEP(1, 0, ns * SLOT, 0, );
        slot = ns;
      }
    }
#undef RA
#undef RL
#undef RH
#undef FB
#undef MQ
#undef MK
#undef STEP
    TS(4); TC(1);
    const unsigned tag = ldsword(FL + F_TAG);
    const int head = hp * 2 + hd;
    {
      f32x16 kv;
#pragma unroll
      for (int i = 0; i < 16; ++i) kv[i] = 0.f;
      f32x2 zs2 = {0.f, 0.f};
#pragma unroll
      for (int m = 0; m < 2; ++m)
#pragma unroll
        for (int ks = 0; ks < 2; ++ks) {
          u32x4 fk, fv;
#pragma unroll
          for (int j = 0; j < 4; ++j) {
            const int i0 = 8 * ks + 2 * j;
            f32x2 kk = {phi_f(acc[m][1][i0]), phi_f(acc[m][1][i0 + 1])};
            zs2 += kk;
            fk[j] = pk_bf16(kk[0], kk[1]);
            fv[j] = pk_bf16(acc[m][2][i0], acc[m][2][i0 + 1]);
          }
          kv = MFMA32(asbf(fv), asbf(fk), kv);
        }
      float zs = zs2[0] + zs2[1];
      zs += __shfl_xor(zs, 32);
      LAS float* xb = (LAS float*)(sm + L_X + hd * 4352);
      if (mh == 1) {
#pragma unroll
        for (int i = 0; i < 4; ++i)
          *(LAS f32x4*)(xb + i * 256 + lane * 4) = (f32x4){kv[4 * i], kv[4 * i + 1], kv[4 * i + 2], kv[4 * i + 3]};
        xb[1024 + lane] = zs;
        sig(FL + F_XE + hd);
      } else {
        waitge(FL + F_XE + hd, 1);
        xb[1024 + lane] += zs;
        if (lane < 8) st16(tag_put(*(LAS f32x4*)(xb + 1024 + lane * 4), tag), rw, lane * 16, WS_ZP + (head * 16 + mb) * 128);
        const int so = WS_KVP + (head * 16 + mb) * 4096;
#pragma unroll
        for (int i = 0; i < 4; ++i) {
          f32x4 o = *(LAS f32x4*)(xb + i * 256 + lane * 4);
          o += (f32x4){kv[4 * i], kv[4 * i + 1], kv[4 * i + 2], kv[4 * i + 3]};
          st16(tag_put(o, tag), rw, i * 1024 + lane * 16, so);
        }
      }
    }
    TS(5);
#pragma unroll
    for (int m = 0; m < 2; ++m)
#pragma unroll
      for (int i = 0; i < 16; ++i) acc[m][0][i] = phi_f(acc[m][0][i]);
    asm volatile("" : "+v"(acc[0][0]), "+v"(acc[1][0]));
    {
      u32x4 za, zb;
#pragma unroll 1
      for (int it = 0; it < (1 << 17); ++it) {
        CBAR();
        za = ld16(rw, lane * 16, WS_ZP + head * 2048);
        zb = ld16(rw, 1024 + lane * 16, WS_ZP + head * 2048);
        if (__all(((tag_get(za) ^ tag) | (tag_get(zb) ^ tag)) == 0u)) break;
        __builtin_amdgcn_s_sleep(2);
      }
      TS(6);
      f32x4 z = tag_val(za) + tag_val(zb);
#pragma unroll
      for (int o = 8; o <= 32; o <<= 1)
#pragma unroll
        for (int i = 0; i < 4; ++i) z[i] += __shfl_xor(z[i], o);
      LAS f32x4* zl = (LAS f32x4*)(sm + L_Z + wave * 128);
      if (lane < 8) zl[lane] = z;
      f32x4 zr[4];
#pragma unroll
      for (int i = 0; i < 4; ++i) zr[i] = zl[2 * i + h];
      const int vo = (r32 >> 4) * 1024 + (h * 16 + (lane & 15)) * 16;
#pragma unroll
      for (int m = 0; m < 2; ++m) {
        float d0 = 0.f, d1 = 0.f;
#pragma unroll
        for (int i = 0; i < 8; ++i) {
          d0 = __builtin_fmaf(acc[m][0][2 * i], zr[i >> 1][(2 * i) & 3], d0);
          d1 = __builtin_fmaf(acc[m][0][2 * i + 1], zr[i >> 1][(2 * i + 1) & 3], d1);
        }
        float den = d0 + d1;
        den += __shfl_xor(den, 32);
        const float rc = __builtin_amdgcn_rcpf(den + 1e-6f);
        const int so = WS_PQ + ((mb * 4 + mh * 2 + m) * 32 + head) * 2048;
#pragma unroll
        for (int ks = 0; ks < 2; ++ks) {
          u32x4 fq;
#pragma unroll
          for (int j = 0; j < 4; ++j) fq[j] = pk_bf16(acc[m][0][8 * ks + 2 * j] * rc, acc[m][0][8 * ks + 2 * j + 1] * rc);
          st16(fq, rw, vo + ks * 512, so);
        }
      }
    }
    TS(7);
    WAITVM(0);
    if (sig_ret(FL + F_EQ) == 3u) gflag(ws, WS_FP + mb * 128 + hp * 4, tag, lane);
    TS(8);
    gpoll((unsigned*)(ws + WS_FP + mb2 * 128) + (lane & 15), tag);
    TS(9);
    {
      const int pqs = WS_PQ + (mb2 * 4 + wave) * 65536;
      u32x4 pb[NRING][2];
#define LDQ(i_) do { const int so_ = pqs + ((((i_) + rot) & 31) << 11);                              \
        pb[(i_) % NRING][0] = ld16(rw, lane * 16, so_); pb[(i_) % NRING][1] = ld16(rw, 1024 + lane * 16, so_); } while (0)
#pragma unroll
      for (int i = 0; i < NRING; ++i) LDQ(i);
      f32x4 oc[2][4];
#pragma unroll
      for (int s = 0; s < 2; ++s)
#pragma unroll
        for (int t = 0; t < 4; ++t) oc[s][t] = (f32x4){0.f, 0.f, 0.f, 0.f};
      asm volatile("" : "+v"(oc[0][0]), "+v"(oc[0][1]), "+v"(oc[0][2]), "+v"(oc[0][3]),
                   "+v"(oc[1][0]), "+v"(oc[1][1]), "+v"(oc[1][2]), "+v"(oc[1][3]));
      LAS char* mp = sm + lane * 16;
      bf16x8 ma[4];
      waitge(FL + F_LM, 4);
      if (bid == 0 && wave == 0) gflag(ws, WS_EPOCH, tag, lane);
      TS(10); TC(2);
#pragma unroll
      for (int t = 0; t < 4; ++t) ma[t] = *(LAS bf16x8*)(mp + t * 1024);
#pragma unroll
      for (int i = 0; i < 32; ++i) {
        if ((i & 7) == 7 && i < 31) waitge(FL + F_LM + (i >> 3) + 1, 4);
        SB();
        const u32x4 b0_ = pb[i % NRING][0], b1_ = pb[i % NRING][1];
#pragma unroll
        for (int t = 0; t < 4; ++t) {
          AMFMA16(oc[0][t], ma[t], b0_);
          AMFMA16(oc[1][t], ma[t], b1_);
          if (i < 31) ma[t] = *(LAS bf16x8*)(mp + (i + 1) * 4096 + t * 1024);
          SB();
        }
        if (i + NRING < 32) LDQ(i + NRING);
      }
#undef LDQ
      asm volatile("s_nop 7\n\ts_nop 3" : "+v"(oc[0][0]), "+v"(oc[0][1]), "+v"(oc[0][2]), "+v"(oc[0][3]),
                   "+v"(oc[1][0]), "+v"(oc[1][1]), "+v"(oc[1][2]), "+v"(oc[1][3]));
      TS(11); TC(3);
#pragma unroll
      for (int s = 0; s < 2; ++s)
#pragma unroll
        for (int j = 0; j < 2; ++j) {
          u32x4 w = {pk_bf16(oc[s][2 * j][0], oc[s][2 * j][1]), pk_bf16(oc[s][2 * j][2], oc[s][2 * j][3]),
                     pk_bf16(oc[s][2 * j + 1][0], oc[s][2 * j + 1][1]), pk_bf16(oc[s][2 * j + 1][2], oc[s][2 * j + 1][3])};
          *(u32x4*)(out + (size_t)((mb2 * 4 + wave) * 32 + s * 16 + (lane & 15)) * 2048 + nb * 64 + 32 * j + 8 * g4) = w;
        }
    }
    TS(12);
    TSDUMP();
    return;
  }

  const int c = wave - 8, d = bid * 4 + c;
  const int cg = bid >> 4, tt16 = bid & 15;
  float p_lr[2], p_ai[2], p_cr[2], p_ci[2];
#pragma unroll
  for (int hf = 0; hf < 2; ++hf) {
    const int i = d * 128 + hf * 64 + lane;
    p_lr[hf] = bf2f(lar[i]); p_ai[hf] = bf2f(aim[i]); p_cr[hf] = bf2f(cre[i]); p_ci[hf] = bf2f(cim[i]);
  }
  const float v_ldt = bf2f(ldt[d]), v_dsk = bf2f(dsk[d]);
  __builtin_amdgcn_s_barrier();
  CBAR();
  LAS char* ub = sm + L_UB + c * UB_CH;
  const int rr = lane & 15, rg = lane >> 4;
  unsigned kp[6];
  {
    const float dt = rbf(expf(v_ldt));
    float mx = __builtin_fmaxf(__builtin_fmaxf(__builtin_fabsf(p_cr[0]), __builtin_fabsf(p_ci[0])),
                               __builtin_fmaxf(__builtin_fabsf(p_cr[1]), __builtin_fabsf(p_ci[1])));
#pragma unroll
    for (int o = 32; o >= 1; o >>= 1) mx = __builtin_fmaxf(mx, __shfl_xor(mx, o));
    const int ex = (mx > 0.f && mx < 3.0e38f) ? __builtin_amdgcn_frexp_expf(mx) : 0;
    const float mult = __builtin_amdgcn_ldexpf(2.f * dt, ex);
    TS(1); TC(4);
    const int wr_lo = (lane & 3) * 4, wr_s = lane >> 2;
#define SCR_W(row, val) *(LAS unsigned*)(ub + (row) * 256 + (((wr_s ^ (row)) << 4) | wr_lo)) = (val)
#define SCR_R(ks) *(LAS f16x8*)(ub + rr * 256 + (((4 * (ks) + rg) ^ rr) << 4))
    f32x4 t0 = {0, 0, 0, 0}, t1 = {0, 0, 0, 0}, t2 = {0, 0, 0, 0};
#pragma unroll 1
    for (int hf = 0; hf < 2; ++hf) {
      const float lr = hf ? p_lr[1] : p_lr[0], aiv = hf ? p_ai[1] : p_ai[0];
      const float cr = hf ? p_cr[1] : p_cr[0], ci = hf ? p_ci[1] : p_ci[0];
      const float ar = -rbf(expf(lr)) * dt;
      const float ai = aiv * dt;
      const float arl2 = ar * 1.4426950408889634f;
      const cplx z1 = zpow(arl2, ai, 1.f), z8 = zpow(arl2, ai, 8.f), z128 = zpow(arl2, ai, 128.f);
      f16x8 bfr[4];
      {
        SCR_W(0, pk_f16(1.f, 0.f));
        cplx e = z8;
        SCR_W(1, pk_f16(e.re, e.im));
#pragma unroll
        for (int k = 2; k < 16; ++k) { e = cmul(e, z8); SCR_W(k, pk_f16(e.re, e.im)); }
#pragma unroll
        for (int ks = 0; ks < 4; ++ks) bfr[ks] = SCR_R(ks);
      }
      cplx X[8], x16;
      {
        cplx xx = {__builtin_amdgcn_ldexpf(cr, -ex), __builtin_amdgcn_ldexpf(ci, -ex)};
#pragma unroll
        for (int r = 0; r < 16; ++r) {
          if (r < 8) X[r] = xx;
          SCR_W(r, pk_f16(xx.re, -xx.im));
          xx = cmul(xx, z128);
        }
        x16 = xx;
#pragma unroll
        for (int ks = 0; ks < 4; ++ks) { f16x8 a = SCR_R(ks); t0 = MFMA16H(a, bfr[ks], t0); }
      }
      cplx zp[8];
      zp[1] = z1;
#pragma unroll
      for (int k = 2; k < 8; ++k) zp[k] = cmul(zp[k - 1], z1);
      {
        SCR_W(0, pk_f16(x16.re, -x16.im));
#pragma unroll
        for (int k = 1; k < 8; ++k) {
          cplx a = cmul(X[0], zp[k]), b = cmul(X[1], zp[k]);
          SCR_W(k, pk_f16(a.re, -a.im));
          SCR_W(8 + k, pk_f16(b.re, -b.im));
        }
#pragma unroll
        for (int ks = 0; ks < 4; ++ks) { f16x8 a = SCR_R(ks); t1 = MFMA16H(a, bfr[ks], t1); }
      }
      {
#pragma unroll
        for (int k = 0; k < 4; ++k) { cplx a = cmul(X[4 + k], zp[4]); SCR_W(k, pk_f16(a.re, -a.im)); }
#pragma unroll
        for (int k = 0; k < 3; ++k) {
          cplx a = cmul(X[2], zp[2 * k + 2]), b = cmul(X[3], zp[2 * k + 2]);
          SCR_W(4 + k, pk_f16(a.re, -a.im));
          SCR_W(8 + k, pk_f16(b.re, -b.im));
        }
#pragma unroll
        for (int ks = 0; ks < 4; ++ks) { f16x8 a = SCR_R(ks); t2 = MFMA16H(a, bfr[ks], t2); }
      }
    }
#undef SCR_W
#undef SCR_R
    kp[0] = pk_bf16(t0[0] * mult + ((lane == 0) ? v_dsk : 0.f), t0[1] * mult);
    kp[1] = pk_bf16(t0[2] * mult, t0[3] * mult);
    kp[2] = pk_bf16(t1[0] * mult, t1[1] * mult);
    kp[3] = pk_bf16(t1[2] * mult, t1[3] * mult);
    kp[4] = pk_bf16(t2[0] * mult, t2[1] * mult);
    kp[5] = pk_bf16(t2[2] * mult, t2[3] * mult);
  }
  TS(2); TC(5);
  bf16x8 F[16];
  {
    LAS u16* K = (LAS u16*)ub + 16;
    const int DUMMY = 2052;
#pragma unroll
    for (int i = 0; i < 4; ++i) {
      const int row = 4 * rg + i;
      const unsigned v0 = kp[i >> 1], v1 = kp[2 + (i >> 1)], v2 = kp[4 + (i >> 1)];
      K[128 * row + 8 * rr] = (u16)((i & 1) ? (v0 >> 16) : v0);
      int p1 = (row >> 3) * 128 + (row & 7) + 8 * rr;
      if (i == 0) p1 = (lane == 0) ? 2048 : ((row & 7) == 0 ? DUMMY : p1);
      K[p1] = (u16)((i & 1) ? (v1 >> 16) : v1);
      int p2 = (rg == 0) ? (512 + 128 * i + 4 + 8 * rr) : (128 + 128 * rg + 2 * (i + 1) + 8 * rr);
      if (rg == 3 || (rg != 0 && i == 3)) p2 = DUMMY;
      K[p2] = (u16)((i & 1) ? (v2 >> 16) : v2);
    }
    if (lane < 8) *(LAS unsigned*)(ub + lane * 4) = 0u;
    const unsigned a1 = K[256 + 4 * lane], b1 = K[258 + 4 * lane], c1 = K[260 + 4 * lane];
    const unsigned a2 = K[512 + 8 * lane], b2 = K[516 + 8 * lane], c2 = K[520 + 8 * lane];
    const unsigned a3 = K[1024 + 16 * lane], b3 = K[1032 + 16 * lane], c3 = K[1040 + 16 * lane];
    u32x2 w1 = {a1 | (a1 << 16), b1 | (c1 << 16)};
    u32x4 w2 = {a2 | (a2 << 16), a2 | (b2 << 16), b2 | (b2 << 16), c2 | (c2 << 16)};
    u32x4 w3a = {a3 | (a3 << 16), a3 | (a3 << 16), a3 | (b3 << 16), b3 | (b3 << 16)};
    u32x4 w3b = {b3 | (b3 << 16), b3 | (b3 << 16), c3 | (c3 << 16), c3 | (c3 << 16)};
    *(LAS u32x2*)(K + 256 + 4 * lane) = w1;
    *(LAS u32x4*)(K + 512 + 8 * lane) = w2;
    *(LAS u32x4*)(K + 1024 + 16 * lane) = w3a;
    *(LAS u32x4*)(K + 1032 + 16 * lane) = w3b;
    CBAR();
    LAS char* fbp = ub + 2 * (16 - 12 + 32 * rg + 4 * (q4 + p4));
#pragma unroll
    for (int s = 0; s < 16; ++s) F[s] = tr_frag(fbp + 256 * s, fbp + 256 * s + 32);
    asm volatile("s_waitcnt lgkmcnt(0)" ::: "memory");
  }
  TS(3);
  sig(FL + F_TU);
  waitge(FL + F_TU, 4);
#pragma unroll
  for (int i = 0; i < 4; ++i) {
    const int pc = 4 * c + i;
    const int f1 = ((lane >> 4) & 1) | ((i & 1) << 1);
    GLDS((const char*)u + (size_t)((tt16 * 128 + pc * 8 + (lane >> 3)) * 2048 + cg * 128 + (((lane & 7) ^ (f1 << 1)) << 4)),
         sm + L_UB + pc * 1024);
  }
  WAITVM(0);
  sig(FL + F_TU);
  waitge(FL + F_TU, 8);
  TS(4);
  {
    const int f1 = (q4 >> 1) | ((g4 & 1) << 1);
#pragma unroll
    for (int j = 0; j < 4; ++j) {
      LAS char* pp = sm + L_UB + (32 * j + 8 * g4 + q4) * 128 + (((2 * c + (p4 >> 1)) ^ (f1 << 1)) << 4) + (p4 & 1) * 8;
      u32x4 v = tr_pair(pp, pp + 512);
      st16(v, rw, (16 * c + (lane & 15)) * 4096 + (32 * j + 8 * g4) * 2, WS_UT + cg * 64 * 4096 + tt16 * 256);
    }
  }
  waitge(FL + F_FULL, 4);
  const unsigned tag = ldsword(FL + F_TAG);
  WAITVM(0);
  if (sig_ret(FL + F_TS) == 3u) gflag(ws, WS_FU + cg * 128 + tt16 * 4, tag, lane);
  TS(5);
  gpoll((unsigned*)(ws + WS_FU + cg * 128) + (lane & 15), tag);
  TS(6);
  {
    u32x4 ur[4];
#pragma unroll
    for (int i = 0; i < 4; ++i) ur[i] = ld16(rw, i * 1024 + lane * 16, WS_UT + d * 4096);
    *(LAS unsigned*)(ub + lane * 4) = 0u;
#pragma unroll
    for (int i = 0; i < 4; ++i) *(LAS u32x4*)(ub + 256 + i * 1024 + lane * 16) = ur[i];
  }
  CBAR();
  TS(8); TC(2);
  unsigned yp[16];
  {
    LAS char* gb = ub + 2 * (128 + 16 * p4 + 12 - 32 * rg - 4 * q4);
    float pc1 = 0.f, pc2 = 0.f, pc3 = 0.f, yprev = 0.f;
    const bool l0 = (lane == 0);
#define ROR1(v) __int_as_float(__builtin_amdgcn_update_dpp(0, __float_as_int(v), 0x13C, 0xf, 0xf, false))
#define FIN(i)                                                                       \
  do {                                                                               \
    float r3 = ROR1(ca[i][3]);                                                       \
    float s2 = ca[i][2] + (l0 ? pc3 : r3);                                           \
    float r2 = ROR1(s2);                                                             \
    float s1 = ca[i][1] + (l0 ? pc2 : r2);                                           \
    float r1 = ROR1(s1);                                                             \
    float y = ca[i][0] + (l0 ? pc1 : r1);                                            \
    pc1 = r1; pc2 = r2; pc3 = r3;                                                    \
    if ((i) & 1) yp[8 * pass + ((i) >> 1)] = pk_bf16(yprev, y); else yprev = y;      \
  } while (0)
#pragma unroll
    for (int pass = 0; pass < 2; ++pass) {
      f32x4 ca[16];
#pragma unroll
      for (int i = 0; i < 16; ++i) ca[i] = (f32x4){0, 0, 0, 0};
      bf16x8 G = tr_frag(gb, gb - 32);
#pragma unroll
      for (int v = 0; v < 16 * pass + 16; ++v) {
        bf16x8 Gn = G;
        if (v + 1 < 16 * pass + 16) Gn = tr_frag(gb + 128 * (v + 1), gb + 128 * (v + 1) - 32);
        SB();
#pragma unroll
        for (int i = 0; i < 16; ++i) {
          const int T = 16 * pass + i;
          if (T >= v && ((T - v) & 1) == 0) ca[i] = MFMA16(G, F[(T - v) >> 1], ca[i]);
        }
        if (v - 1 >= 16 * pass) FIN(v - 1 - 16 * pass);
        SB();
        G = Gn;
      }
      FIN(15);
      CBAR();
      SB();
    }
#undef FIN
#undef ROR1
  }
  CBAR();
  TS(9); TC(3);
#pragma unroll
  for (int i = 0; i < 16; ++i) {
    ((LAS u16*)ub)[128 * i + lane] = (u16)yp[i];
    ((LAS u16*)ub)[128 * i + 64 + lane] = (u16)(yp[i] >> 16);
  }
  CBAR();
#pragma unroll
  for (int i = 0; i < 4; ++i) st16(*(LAS u32x4*)(ub + i * 1024 + lane * 16), rw, i * 1024 + lane * 16, WS_YT + d * 4096);
  WAITVM(0);
  if (sig_ret(FL + F_CY) == 3u) gflag(ws, WS_FY + cg * 128 + tt16 * 4, tag, lane);
  TS(10);
  gpoll((unsigned*)(ws + WS_FY + cg * 128) + (lane & 15), tag);
  TS(11);
  {
    int ln2 = lane;
    asm volatile("" : "+v"(ln2));
    const int lane = ln2, q4 = (lane & 15) >> 2, p4 = lane & 3, g4 = lane >> 4;
    LAS char* tb = sm + L_UB;
    u32x4 yr[4];
#pragma unroll
    for (int i = 0; i < 4; ++i)
      yr[i] = ld16(rw, (16 * c + 4 * i + g4) * 4096 + (lane & 15) * 16, WS_YT + cg * 64 * 4096 + tt16 * 256);
#pragma unroll
    for (int i = 0; i < 4; ++i) {
      const int row = 16 * c + 4 * i + g4;
      const int f = (row & 3) | (((row >> 3) & 1) << 2);
      *(LAS u32x4*)(tb + row * 256 + (((lane & 15) ^ (f << 1)) << 4)) = yr[i];
    }
    sig(FL + F_T2);
    waitge(FL + F_T2, 4);
    const int f = q4 | ((g4 & 1) << 2);
#pragma unroll
    for (int j = 0; j < 4; ++j) {
      const int tbk = 2 * c + (j >> 1), hf = j & 1;
      LAS char* pp = tb + (32 * hf + 8 * g4 + q4) * 256 + (((2 * tbk + (p4 >> 1)) ^ (f << 1)) << 4) + (p4 & 1) * 8;
      u32x4 v = tr_pair(pp, pp + 1024);
      *(u32x4*)(out + (size_t)(tt16 * 128 + 16 * tbk + (lane & 15)) * 2048 + 1024 + cg * 64 + 32 * hf + 8 * g4) = v;
    }
  }
  TS(12);
  TSDUMP();
}

extern "C" void kernel_launch(void* const* d_in, const int* in_sizes, int n_in, void* d_out, int out_size,
                              void* d_ws, size_t ws_size, hipStream_t stream) {
  fused_kernel<<<256, 768, 0, stream>>>((const u16*)d_in[0], (const u16*)d_in[1], (const u16*)d_in[2], (const u16*)d_in[3],
                                        (const u16*)d_in[4], (const u16*)d_in[5], (const u16*)d_in[6], (const u16*)d_in[7],
                                        (const u16*)d_in[8], (const u16*)d_in[9], (u16*)d_out, (char*)d_ws);
}
```

```cpp
#define OSC
#include <hip/hip_runtime.h>
#include <stdint.h>

#define LAS __attribute__((address_space(3)))
#define GAS __attribute__((address_space(1)))
#define DI __device__ __forceinline__

typedef unsigned short u16;
typedef short s16x4 __attribute__((ext_vector_type(4)));
typedef __bf16 bf16x2 __attribute__((ext_vector_type(2)));
typedef __bf16 bf16x8 __attribute__((ext_vector_type(8)));
typedef _Float16 f16x2 __attribute__((ext_vector_type(2)));
typedef _Float16 f16x8 __attribute__((ext_vector_type(8)));
typedef float f32x2 __attribute__((ext_vector_type(2)));
typedef float f32x4 __attribute__((ext_vector_type(4)));
typedef float f32x16 __attribute__((ext_vector_type(16)));
typedef unsigned u32x2 __attribute__((ext_vector_type(2)));
typedef unsigned u32x4 __attribute__((ext_vector_type(4)));
typedef __amdgpu_buffer_rsrc_t rsrc_t;

#define CBAR() asm volatile("" ::: "memory")
#define SB() __builtin_amdgcn_sched_barrier(0)
#define WAITVM(n) asm volatile("s_waitcnt vmcnt(" #n ")" ::: "memory")
#define TSDECL
#define TS(i)
#define TC(i)
#define TSDUMP()
#define PRIO_C 2
#define PRIO_L 3
#define NRING 12

#define OST(p, v) asm volatile("global_store_dwordx4 %0, %1, off sc1\n\ts_nop 1" :: "v"(p), "v"(v) : "memory")
DI float bf2f(u16 v) { return __uint_as_float((unsigned)v << 16); }
DI unsigned pk_bf16(float a, float b) {
  f32x2 f = {a, b};
  return __builtin_bit_cast(unsigned, __builtin_convertvector(f, bf16x2));
}
DI unsigned pk_f16(float a, float b) {
  f32x2 f = {a, b};
  return __builtin_bit_cast(unsigned, __builtin_convertvector(f, f16x2));
}
DI float rbf(float a) { return __uint_as_float(pk_bf16(a, 0.f) << 16); }
DI u32x2 tr_read(LAS char* p) {
  s16x4 v = __builtin_amdgcn_ds_read_tr16_b64_v4i16((LAS s16x4*)p);
  return __builtin_bit_cast(u32x2, v);
}
DI u32x4 join(u32x2 a, u32x2 b) { u32x4 r = {a.x, a.y, b.x, b.y}; return r; }
DI u32x4 tr_pair(LAS char* p0, LAS char* p1) { return join(tr_read(p0), tr_read(p1)); }
DI bf16x8 asbf(u32x4 v) { return __builtin_bit_cast(bf16x8, v); }
DI bf16x8 tr_frag(LAS char* p0, LAS char* p1) { return asbf(tr_pair(p0, p1)); }
#define MFMA32(a, b, c) __builtin_amdgcn_mfma_f32_32x32x16_bf16((a), (b), (c), 0, 0, 0)
#define MFMA16(a, b, c) __builtin_amdgcn_mfma_f32_16x16x32_bf16((a), (b), (c), 0, 0, 0)
#define MFMA16H(a, b, c) __builtin_amdgcn_mfma_f32_16x16x32_f16((a), (b), (c), 0, 0, 0)
#define AMFMA16(c, a, b) asm volatile("v_mfma_f32_16x16x32_bf16 %0, %1, %2, %0" : "+v"(c) : "v"(a), "v"(b))
#define GLDS(src, dst) __builtin_amdgcn_global_load_lds((const GAS unsigned*)(src), (LAS unsigned*)(dst), 16, 0, 0)
#define SC1 16
DI u32x4 ld16(rsrc_t r, int voff, int soff) { return __builtin_amdgcn_raw_buffer_load_b128(r, voff, soff, SC1); }
DI void st16(u32x4 v, rsrc_t r, int voff, int soff) {
  __builtin_amdgcn_raw_buffer_store_b128(v, r, voff, soff, SC1);
  asm volatile("s_nop 1" :: "v"(v) : "memory");
}

constexpr int WS_EPOCH = 0;
constexpr int WS_FU = 1024, WS_FP = 3072, WS_FY = 5120, WS_FR = 7168;
constexpr int WS_PQ = 65536;
constexpr int WS_UT = WS_PQ + (4 << 20);
constexpr int WS_YT = WS_UT + (4 << 20);
constexpr int WS_KVP = WS_YT + (4 << 20);
constexpr int WS_ZP = WS_KVP + 32 * 16 * 4096;
constexpr int WS_KVR = WS_ZP + 32 * 16 * 128;
constexpr int WS_END = WS_KVR + 32 * 2048;

constexpr int SLOT = 40960;
constexpr int L_UB = 131072, UB_CH = 4352;
constexpr int L_X = L_UB + 4 * UB_CH;
constexpr int L_Z = L_X + 2 * 4352;
constexpr int L_FLAG = L_Z + 512;
constexpr int LDS_BYTES = L_FLAG + 256;
enum { F_FULL = 0, F_FREE = 4, F_XE = 8, F_EQ = 10, F_LW, F_TAG, F_TU, F_TS, F_CY, F_T2, F_LM = 20 };

struct cplx { float re, im; };
DI cplx cmul(cplx a, cplx b) {
  cplx r;
  r.re = __builtin_fmaf(a.re, b.re, -(a.im * b.im));
  r.im = __builtin_fmaf(a.re, b.im, a.im * b.re);
  return r;
}
DI cplx zpow(float arl2, float ai, float p) {
  const float INV2PI = 0.15915494309189535f;
  const float INV2PI_LO = (float)(0.15915494309189533576888 - (double)0.15915494309189535f);
  float e = __builtin_amdgcn_exp2f(arl2 * p);
  float r = ai * p;
  float hi = r * INV2PI;
  float lo = __builtin_fmaf(r, INV2PI_LO, __builtin_fmaf(r, INV2PI, -hi));
  float f = (hi - __builtin_rintf(hi)) + lo;
  cplx z;
  z.re = e * __builtin_amdgcn_cosf(f);
  z.im = e * __builtin_amdgcn_sinf(f);
  return z;
}
DI float phi_f(float t) {
  float e = __builtin_amdgcn_fmed3f(__builtin_amdgcn_exp2f(t * 1.4426950408889634f), 0.f, 1.f);
  return (__builtin_amdgcn_fmed3f(t, 0.f, __builtin_inff()) + 1e-6f) + e;
}
DI u32x4 tag_put(f32x4 v, unsigned tag) {
  u32x4 b = __builtin_bit_cast(u32x4, v), r;
  r[0] = __builtin_amdgcn_perm(b[0], tag, 0x07060500u);
  r[1] = __builtin_amdgcn_perm(b[1], tag, 0x07060501u);
  r[2] = __builtin_amdgcn_perm(b[2], tag, 0x07060502u);
  r[3] = __builtin_amdgcn_perm(b[3], tag, 0x07060503u);
  return r;
}
DI unsigned tag_get(u32x4 b) {
  unsigned lo = __builtin_amdgcn_perm(b[1], b[0], 0x0c0c0400u), hi = __builtin_amdgcn_perm(b[3], b[2], 0x04000c0cu);
  return lo | hi;
}
DI f32x4 tag_val(u32x4 b) { return __builtin_bit_cast(f32x4, b & 0xffffff00u); }

DI void sig(LAS unsigned* f) {
  asm volatile("s_mov_b64 exec, 1\n\tds_add_u32 %0, %1\n\ts_mov_b64 exec, -1" :: "v"((unsigned)(size_t)f), "v"(1u) : "memory");
}
DI unsigned sig_ret(LAS unsigned* f) {
  unsigned r;
  asm volatile("s_mov_b64 exec, 1\n\tds_add_rtn_u32 %0, %1, %2\n\ts_mov_b64 exec, -1\n\ts_waitcnt lgkmcnt(0)"
               : "=&v"(r) : "v"((unsigned)(size_t)f), "v"(1u) : "memory");
  return __builtin_amdgcn_readfirstlane(r);
}
DI unsigned ldsword(LAS unsigned* f) { return __builtin_amdgcn_readfirstlane(*(volatile LAS unsigned*)f); }
DI void waitge(LAS unsigned* f, unsigned n) {
  CBAR();
#pragma unroll 1
  for (int it = 0; it < (1 << 22); ++it) {
    if (ldsword(f) >= n) break;
    __builtin_amdgcn_s_sleep(1);
  }
  CBAR();
}
DI void gflag(char* ws, int off, unsigned tag, int lane) {
  CBAR();
  if (lane == 0) __hip_atomic_store((unsigned*)(ws + off), tag, __ATOMIC_RELAXED, __HIP_MEMORY_SCOPE_AGENT);
  CBAR();
}
DI void gpoll(unsigned* p, unsigned tag) {
  CBAR();
#pragma unroll 1
  for (int it = 0; it < (1 << 18); ++it) {
    unsigned v = __hip_atomic_load(p, __ATOMIC_RELAXED, __HIP_MEMORY_SCOPE_AGENT);
    if (__all(v == tag)) break;
    __builtin_amdgcn_s_sleep(2);
  }
  CBAR();
}

__global__ __launch_bounds__(768, 3) void fused_kernel(const u16* __restrict__ x, const u16* __restrict__ wqkv,
                                                       const u16* __restrict__ wo, const u16* __restrict__ u,
                                                       const u16* __restrict__ lar, const u16* __restrict__ aim,
                                                       const u16* __restrict__ cre, const u16* __restrict__ cim,
                                                       const u16* __restrict__ ldt, const u16* __restrict__ dsk,
                                                       u16* __restrict__ out, char* __restrict__ ws) {
  __shared__ __attribute__((aligned(16))) char smem[LDS_BYTES];
  unsigned long long pc0;
  asm volatile("s_getpc_b64 %0" : "=s"(pc0));
  LAS char* sm = (LAS char*)smem;
  LAS unsigned* FL = (LAS unsigned*)(sm + L_FLAG);
  const int tid = threadIdx.x, lane = tid & 63;
  const int wave = __builtin_amdgcn_readfirstlane(tid >> 6);
  const int bid = blockIdx.x, xcd = bid & 7, idx = bid >> 3;
  const int ml = idx & 7, hl = idx >> 3;
  const int mb = (xcd & 1) * 8 + ml, hp = (xcd >> 1) * 4 + hl;
  const int mb2 = (xcd & 3) * 4 + (idx & 3), nb = (xcd >> 2) * 8 + (idx >> 2);
  const int rot = (idx >> 2) * 4;
  const rsrc_t rw = __builtin_amdgcn_make_buffer_rsrc((void*)ws, 0, WS_END, 0x00020000);
  TSDECL; TS(0);
  const int q4 = (lane & 15) >> 2, p4 = lane & 3, g4 = lane >> 4;

  if ((wave >> 2) == 1) {
    __builtin_amdgcn_s_setprio(PRIO_L);
    const int hw = wave - 4;
    unsigned ep = 0;
    if (hw == 0) ep = __hip_atomic_load((unsigned*)(ws + WS_EPOCH), __ATOMIC_RELAXED, __HIP_MEMORY_SCOPE_AGENT);
    const int koff = 2 * ml + 4 * hl + (hl >> 1);
    const unsigned vx = (unsigned)((mb * 128 + hw * 32 + (lane >> 3)) * 2048);
    const unsigned sx0 = ((lane & 7) ^ (lane >> 4)) << 4, sx1 = sx0 ^ 64;
    const unsigned vw = (unsigned)((lane >> 3) * 6144 + hp * 128 + (((lane & 7) ^ (((lane >> 4) & 1) << 2)) << 4));
#define ILIM 1
#define ISSUE(tt_, slot_)                                                                                  \
  do {                                                                                                     \
    const int k_ = ((tt_) + koff) & 15;                                                                    \
    const char* xa_ = (const char*)x + k_ * 128;                                                           \
    const char* wa_ = (const char*)wqkv + (size_t)k_ * 393216;                                             \
    LAS char* sb_ = sm + (slot_) * SLOT;                                                                   \
    _Pragma("unroll") for (int i = 0; i < 4 * ILIM; ++i)                                                   \
        GLDS(xa_ + i * 16384 + (vx + ((i & 1) ? sx1 : sx0)), sb_ + (hw * 4 + i) * 1024);                   \
    _Pragma("unroll") for (int i = 0; i < 6 * ILIM; ++i) {                                                 \
      const int qq_ = hw * 6 + i;                                                                          \
      GLDS(wa_ + ((qq_ & 7) * 49152 + (qq_ >> 3) * 2048) + vw, sb_ + 16384 + qq_ * 1024);                  \
    }                                                                                                      \
  } while (0)
    ISSUE(0, 0);
    __builtin_amdgcn_s_barrier();
    CBAR();
    TS(13);
    ISSUE(1, 1);
    ISSUE(2, 2);
    TS(14);
    WAITVM(20);
    if (hw == 0) {
      const unsigned tg = __builtin_amdgcn_readfirstlane(ep) * 2654435761u + 0x9E3779B1u;
      *(volatile LAS unsigned*)(FL + F_TAG) = tg;
    }
    sig(FL + F_FULL);
    TS(1);
    {
      int sp = 1, sf = 0, needf = 4;
#pragma unroll 1
      for (int p = 1; p < 14; ++p) {
        WAITVM(10);
        sig(FL + F_FULL + sp);
        waitge(FL + F_FREE + sf, (unsigned)needf);
        ISSUE(p + 2, sf);
        sf = sp;
        if (sf == 0) needf += 4;
        sp = (sp == 2) ? 0 : sp + 1;
      }
    }
#undef ISSUE
    TS(2);
    {
      const char* src = (const char*)wo + (size_t)((8 * hw + (lane >> 3)) * 2048 + nb * 128 + (((lane & 7) ^ (((lane >> 4) & 1) << 2)) << 4));
#define WOP(s_) GLDS(src + (size_t)((((s_) + rot) & 31) * 65536), sm + ((s_) * 4 + hw) * 1024)
      WOP(30); WOP(31);
      WAITVM(12);
      sig(FL + F_FULL + 2);
      waitge(FL + F_FREE + 1, 20);
#pragma unroll
      for (int i = 10; i < 20; ++i) WOP(i);
      WAITVM(12);
      sig(FL + F_FULL + 0);
      waitge(FL + F_FREE + 2, 20);
#pragma unroll
      for (int i = 20; i < 30; ++i) WOP(i);
      waitge(FL + F_FREE + 0, 24);
#pragma unroll
      for (int i = 0; i < 10; ++i) WOP(i);
#undef WOP
      TS(3);
      WAITVM(0);
      sig(FL + F_LW);
    }
    TS(4);
    const unsigned tag = ldsword(FL + F_TAG);
    if (hw == 0) {
      const int rh = bid >> 3, part = bid & 7;
      const int ks = part >> 2, ln = (part & 3) * 16 + (lane & 15);
      const int so = WS_KVP + rh * 65536 + ks * 2048;
      const int vo = g4 * 16384 + ln * 16;
      u32x4 a[8];
#pragma unroll 1
      for (int it = 0; it < (1 << 17); ++it) {
        CBAR();
#pragma unroll
        for (int i = 0; i < 8; ++i) a[i] = ld16(rw, vo + (i >> 1) * 4096 + (i & 1) * 1024, so);
        unsigned bad = 0;
#pragma unroll
        for (int i = 0; i < 8; ++i) bad |= tag_get(a[i]) ^ tag;
        if (__all(bad == 0u)) break;
        __builtin_amdgcn_s_sleep(4);
      }
      TS(5);
      f32x4 s0 = (tag_val(a[0]) + tag_val(a[2])) + (tag_val(a[4]) + tag_val(a[6]));
      f32x4 s1 = (tag_val(a[1]) + tag_val(a[3])) + (tag_val(a[5]) + tag_val(a[7]));
#pragma unroll
      for (int o = 16; o <= 32; o <<= 1)
#pragma unroll
        for (int i = 0; i < 4; ++i) { s0[i] += __shfl_xor(s0[i], o); s1[i] += __shfl_xor(s1[i], o); }
      u32x4 w = {pk_bf16(s0[0], s0[1]), pk_bf16(s0[2], s0[3]), pk_bf16(s1[0], s1[1]), pk_bf16(s1[2], s1[3])};
      if (lane < 16) st16(w, rw, (ks * 64 + ln) * 16, WS_KVR + rh * 2048);
      WAITVM(0);
      gflag(ws, WS_FR + bid * 4, tag, lane);
      TS(6);
    }
    gpoll((unsigned*)(ws + WS_FR) + (((hw + 4 * (lane >> 3) + rot) & 31) * 8 + (lane & 7)), tag);
    TS(7);
    {
      u32x4 kf[8][2];
#pragma unroll
      for (int i = 0; i < 8; ++i)
#pragma unroll
        for (int ks = 0; ks < 2; ++ks) kf[i][ks] = ld16(rw, ks * 1024 + lane * 16, WS_KVR + ((hw + 4 * i + rot) & 31) * 2048);
      waitge(FL + F_LW, 4);
      const int h = lane >> 5, blk = (lane >> 4) & 1, n = lane & 31;
      LAS char* wb = sm + hw * 4096;
      const int w0 = (4 * h + q4) * 128 + (((blk * 2 + (p4 >> 1)) ^ ((q4 >> 1) << 2)) << 4) + (p4 & 1) * 8;
      const int m0 = ((n >> 2) & 1) * 1024 + (h * 16 + 4 * (n >> 3) + (n & 3)) * 16;
#pragma unroll
      for (int i = 0; i < 8; ++i) {
        bf16x8 wf[2][2];
#pragma unroll
        for (int nt = 0; nt < 2; ++nt)
#pragma unroll
          for (int ks = 0; ks < 2; ++ks) {
            LAS char* pp = wb + i * 16384 + ks * 2048 + (w0 ^ (nt << 6));
            wf[nt][ks] = tr_frag(pp, pp + 1024);
          }
#pragma unroll
        for (int nt = 0; nt < 2; ++nt) {
          f32x16 D;
#pragma unroll
          for (int e = 0; e < 16; ++e) D[e] = 0.f;
          D = MFMA32(asbf(kf[i][0]), wf[nt][0], D);
          D = MFMA32(asbf(kf[i][1]), wf[nt][1], D);
#pragma unroll
          for (int ks = 0; ks < 2; ++ks) {
            u32x4 mw = {pk_bf16(D[8 * ks], D[8 * ks + 1]), pk_bf16(D[8 * ks + 2], D[8 * ks + 3]),
                        pk_bf16(D[8 * ks + 4], D[8 * ks + 5]), pk_bf16(D[8 * ks + 6], D[8 * ks + 7])};
            *(LAS u32x4*)(wb + i * 16384 + nt * 2048 + ks * 512 + m0) = mw;
          }
        }
        if (i & 1) sig(FL + F_LM + (i >> 1));
      }
    }
    TS(9);
    TSDUMP();
    return;
  }

  if (wave < 4) {
    if (wave == 0) FL[lane] = 0u;
    asm volatile("s_waitcnt lgkmcnt(0)" ::: "memory");
    __builtin_amdgcn_s_barrier();
    CBAR();
    __builtin_amdgcn_s_setprio(PRIO_C);
    const int mh = wave & 1, hd = wave >> 1;
    const int r32 = lane & 31, h = lane >> 5, blk = (lane >> 4) & 1;
    const int a0 = (mh * 64 + r32) * 128 + ((h ^ ((r32 >> 1) & 7)) << 4);
    const int b0 = 16384 + (8 * h + q4) * 128 + (((hd * 4 + blk * 2 + (p4 >> 1)) ^ ((q4 >> 1) << 2)) << 4) + (p4 & 1) * 8;
    f32x16 acc[2][3];
#pragma unroll
    for (int m = 0; m < 2; ++m)
#pragma unroll
      for (int g = 0; g < 3; ++g)
#pragma unroll
        for (int i = 0; i < 16; ++i) acc[m][g][i] = 0.f;
    bf16x8 fa[2][2];
    u32x2 fl[2][3], fh[2][3];
#define RA(set, m, so, s) fa[set][m] = *(LAS bf16x8*)(sm + ((a0 ^ ((s) << 5)) + (so)) + (m) * 4096)
#define RL(set, g, so, s) fl[set][g] = tr_read(sm + (b0 + (so)) + (g) * 8192 + (s) * 2048)
#define RH(set, g, so, s) fh[set][g] = tr_read(sm + (b0 + (so)) + (g) * 8192 + (s) * 2048 + 512)
#define FB(set, g) asbf(join(fl[set][g], fh[set][g]))
#define MQ(set, m) acc[m][0] = MFMA32(FB(set, 0), fa[set][m], acc[m][0])
#define MK(set, m, g) acc[m][g] = MFMA32(fa[set][m], FB(set, g), acc[m][g])
#define STEP(c, n, so, s, EXTRA)                                   \
  do {                                                             \
    MQ(c, 0);    RA(n, 0, so, s); RL(n, 0, so, s); SB();           \
    MK(c, 0, 1); RH(n, 0, so, s); RL(n, 1, so, s); SB();           \
    MK(c, 0, 2); RH(n, 1, so, s); RL(n, 2, so, s); SB();           \
    MQ(c, 1);    RH(n, 2, so, s); RA(n, 1, so, s); SB();           \
    MK(c, 1, 1); EXTRA; SB();                                      \
    MK(c, 1, 2); SB();                                             \
  } while (0)
    waitge(FL + F_FULL, 4);
    TS(3); TC(0);
    RA(0, 0, 0, 0); RL(0, 0, 0, 0); RH(0, 0, 0, 0); RL(0, 1, 0, 0); RH(0, 1, 0, 0); RL(0, 2, 0, 0); RH(0, 2, 0, 0); RA(0, 1, 0, 0);
    {
      int slot = 0, need = 4;
#pragma unroll 1
      for (int t = 0; t < 16; ++t) {
        const int so = slot * SLOT;
        const int ns = (slot == 2) ? 0 : slot + 1;
        if (ns == 0) need += 4;
        if (t == 15) need = 0;
        unsigned fv;
        SB();
        STEP(0, 1, so, 1, );
        STEP(1, 0, so, 2, { fv = *(volatile LAS unsigned*)(FL + F_FULL + ns); });
        STEP(0, 1, so, 3, { sig(FL + F_FREE + slot); });
        if (__builtin_amdgcn_readfirstlane(fv) < (unsigned)need) {
#pragma unroll 1
          for (int it = 0; it < (1 << 24); ++it) { if (ldsword(FL + F_FULL + ns) >= (unsigned)need) break; }
        }
        CBAR();
        SB();
        STEP(1, 0, ns * SLOT, 0, );
        slot = ns;
      }
    }
#undef RA
#undef RL
#undef RH
#undef FB
#undef MQ
#undef MK
#undef STEP
    TS(4); TC(1);
    const unsigned tag = ldsword(FL + F_TAG);
    const int head = hp * 2 + hd;
    {
      f32x16 kv;
#pragma unroll
      for (int i = 0; i < 16; ++i) kv[i] = 0.f;
      f32x2 zs2 = {0.f, 0.f};
#pragma unroll
      for (int m = 0; m < 2; ++m)
#pragma unroll
        for (int ks = 0; ks < 2; ++ks) {
          u32x4 fk, fv;
#pragma unroll
          for (int j = 0; j < 4; ++j) {
            const int i0 = 8 * ks + 2 * j;
            f32x2 kk = {phi_f(acc[m][1][i0]), phi_f(acc[m][1][i0 + 1])};
            zs2 += kk;
            fk[j] = pk_bf16(kk[0], kk[1]);
            fv[j] = pk_bf16(acc[m][2][i0], acc[m][2][i0 + 1]);
          }
          kv = MFMA32(asbf(fv), asbf(fk), kv);
        }
      float zs = zs2[0] + zs2[1];
      zs += __shfl_xor(zs, 32);
      LAS float* xb = (LAS float*)(sm + L_X + hd * 4352);
      if (mh == 1) {
#pragma unroll
        for (int i = 0; i < 4; ++i)
          *(LAS f32x4*)(xb + i * 256 + lane * 4) = (f32x4){kv[4 * i], kv[4 * i + 1], kv[4 * i + 2], kv[4 * i + 3]};
        xb[1024 + lane] = zs;
        sig(FL + F_XE + hd);
      } else {
        waitge(FL + F_XE + hd, 1);
        xb[1024 + lane] += zs;
        if (lane < 8) st16(tag_put(*(LAS f32x4*)(xb + 1024 + lane * 4), tag), rw, lane * 16, WS_ZP + (head * 16 + mb) * 128);
        const int so = WS_KVP + (head * 16 + mb) * 4096;
#pragma unroll
        for (int i = 0; i < 4; ++i) {
          f32x4 o = *(LAS f32x4*)(xb + i * 256 + lane * 4);
          o += (f32x4){kv[4 * i], kv[4 * i + 1], kv[4 * i + 2], kv[4 * i + 3]};
          st16(tag_put(o, tag), rw, i * 1024 + lane * 16, so);
        }
      }
    }
    TS(5);
#pragma unroll
    for (int m = 0; m < 2; ++m)
#pragma unroll
      for (int i = 0; i < 16; ++i) acc[m][0][i] = phi_f(acc[m][0][i]);
    asm volatile("" : "+v"(acc[0][0]), "+v"(acc[1][0]));
    {
      u32x4 za, zb;
#pragma unroll 1
      for (int it = 0; it < (1 << 17); ++it) {
        CBAR();
        za = ld16(rw, lane * 16, WS_ZP + head * 2048);
        zb = ld16(rw, 1024 + lane * 16, WS_ZP + head * 2048);
        if (__all(((tag_get(za) ^ tag) | (tag_get(zb) ^ tag)) == 0u)) break;
        __builtin_amdgcn_s_sleep(2);
      }
      TS(6);
      f32x4 z = tag_val(za) + tag_val(zb);
#pragma unroll
      for (int o = 8; o <= 32; o <<= 1)
#pragma unroll
        for (int i = 0; i < 4; ++i) z[i] += __shfl_xor(z[i], o);
      LAS f32x4* zl = (LAS f32x4*)(sm + L_Z + wave * 128);
      if (lane < 8) zl[lane] = z;
      f32x4 zr[4];
#pragma unroll
      for (int i = 0; i < 4; ++i) zr[i] = zl[2 * i + h];
      const int vo = (r32 >> 4) * 1024 + (h * 16 + (lane & 15)) * 16;
#pragma unroll
      for (int m = 0; m < 2; ++m) {
        float d0 = 0.f, d1 = 0.f;
#pragma unroll
        for (int i = 0; i < 8; ++i) {
          d0 = __builtin_fmaf(acc[m][0][2 * i], zr[i >> 1][(2 * i) & 3], d0);
          d1 = __builtin_fmaf(acc[m][0][2 * i + 1], zr[i >> 1][(2 * i + 1) & 3], d1);
        }
        float den = d0 + d1;
        den += __shfl_xor(den, 32);
        const float rc = __builtin_amdgcn_rcpf(den + 1e-6f);
        const int so = WS_PQ + ((mb * 4 + mh * 2 + m) * 32 + head) * 2048;
#pragma unroll
        for (int ks = 0; ks < 2; ++ks) {
          u32x4 fq;
#pragma unroll
          for (int j = 0; j < 4; ++j) fq[j] = pk_bf16(acc[m][0][8 * ks + 2 * j] * rc, acc[m][0][8 * ks + 2 * j + 1] * rc);
          st16(fq, rw, vo + ks * 512, so);
        }
      }
    }
    TS(7);
    WAITVM(0);
    if (sig_ret(FL + F_EQ) == 3u) gflag(ws, WS_FP + mb * 128 + hp * 4, tag, lane);
    TS(8);
    gpoll((unsigned*)(ws + WS_FP + mb2 * 128) + (lane & 15), tag);
    TS(9);
    {
      const int pqs = WS_PQ + (mb2 * 4 + wave) * 65536;
      u32x4 pb[NRING][2];
#define LDQ(i_) do { const int so_ = pqs + ((((i_) + rot) & 31) << 11);                              \
        pb[(i_) % NRING][0] = ld16(rw, lane * 16, so_); pb[(i_) % NRING][1] = ld16(rw, 1024 + lane * 16, so_); } while (0)
#pragma unroll
      for (int i = 0; i < NRING; ++i) LDQ(i);
      f32x4 oc[2][4];
#pragma unroll
      for (int s = 0; s < 2; ++s)
#pragma unroll
        for (int t = 0; t < 4; ++t) oc[s][t] = (f32x4){0.f, 0.f, 0.f, 0.f};
      asm volatile("" : "+v"(oc[0][0]), "+v"(oc[0][1]), "+v"(oc[0][2]), "+v"(oc[0][3]),
                   "+v"(oc[1][0]), "+v"(oc[1][1]), "+v"(oc[1][2]), "+v"(oc[1][3]));
      LAS char* mp = sm + lane * 16;
      bf16x8 ma[4];
      waitge(FL + F_LM, 4);
      if (bid == 0 && wave == 0) gflag(ws, WS_EPOCH, tag, lane);
      TS(10); TC(2);
#pragma unroll
      for (int t = 0; t < 4; ++t) ma[t] = *(LAS bf16x8*)(mp + t * 1024);
#pragma unroll
      for (int i = 0; i < 32; ++i) {
        if ((i & 7) == 7 && i < 31) waitge(FL + F_LM + (i >> 3) + 1, 4);
        SB();
        const u32x4 b0_ = pb[i % NRING][0], b1_ = pb[i % NRING][1];
#pragma unroll
        for (int t = 0; t < 4; ++t) {
          AMFMA16(oc[0][t], ma[t], b0_);
          AMFMA16(oc[1][t], ma[t], b1_);
          if (i < 31) ma[t] = *(LAS bf16x8*)(mp + (i + 1) * 4096 + t * 1024);
          SB();
        }
        if (i + NRING < 32) LDQ(i + NRING);
      }
#undef LDQ
      asm volatile("s_nop 7\n\ts_nop 3" : "+v"(oc[0][0]), "+v"(oc[0][1]), "+v"(oc[0][2]), "+v"(oc[0][3]),
                   "+v"(oc[1][0]), "+v"(oc[1][1]), "+v"(oc[1][2]), "+v"(oc[1][3]));
      TS(11); TC(3);
#pragma unroll
      for (int s = 0; s < 2; ++s)
#pragma unroll
        for (int j = 0; j < 2; ++j) {
          u32x4 w = {pk_bf16(oc[s][2 * j][0], oc[s][2 * j][1]), pk_bf16(oc[s][2 * j][2], oc[s][2 * j][3]),
                     pk_bf16(oc[s][2 * j + 1][0], oc[s][2 * j + 1][1]), pk_bf16(oc[s][2 * j + 1][2], oc[s][2 * j + 1][3])};
          OST((u32x4*)(out + (size_t)((mb2 * 4 + wave) * 32 + s * 16 + (lane & 15)) * 2048 + nb * 64 + 32 * j + 8 * g4), w);
        }
    }
    TS(12);
    TSDUMP();
    return;
  }

  const int c = wave - 8, d = bid * 4 + c;
  const int cg = bid >> 4, tt16 = bid & 15;
  float p_lr[2], p_ai[2], p_cr[2], p_ci[2];
#pragma unroll
  for (int hf = 0; hf < 2; ++hf) {
    const int i = d * 128 + hf * 64 + lane;
    p_lr[hf] = bf2f(lar[i]); p_ai[hf] = bf2f(aim[i]); p_cr[hf] = bf2f(cre[i]); p_ci[hf] = bf2f(cim[i]);
  }
  const float v_ldt = bf2f(ldt[d]), v_dsk = bf2f(dsk[d]);
  __builtin_amdgcn_s_barrier();
  CBAR();
  LAS char* ub = sm + L_UB + c * UB_CH;
  const int rr = lane & 15, rg = lane >> 4;
  unsigned kp[6];
  {
    const float dt = rbf(expf(v_ldt));
    float mx = __builtin_fmaxf(__builtin_fmaxf(__builtin_fabsf(p_cr[0]), __builtin_fabsf(p_ci[0])),
                               __builtin_fmaxf(__builtin_fabsf(p_cr[1]), __builtin_fabsf(p_ci[1])));
#pragma unroll
    for (int o = 32; o >= 1; o >>= 1) mx = __builtin_fmaxf(mx, __shfl_xor(mx, o));
    const int ex = (mx > 0.f && mx < 3.0e38f) ? __builtin_amdgcn_frexp_expf(mx) : 0;
    const float mult = __builtin_amdgcn_ldexpf(2.f * dt, ex);
    TS(1); TC(4);
    const int wr_lo = (lane & 3) * 4, wr_s = lane >> 2;
#define SCR_W(row, val) *(LAS unsigned*)(ub + (row) * 256 + (((wr_s ^ (row)) << 4) | wr_lo)) = (val)
#define SCR_R(ks) *(LAS f16x8*)(ub + rr * 256 + (((4 * (ks) + rg) ^ rr) << 4))
    f32x4 t0 = {0, 0, 0, 0}, t1 = {0, 0, 0, 0}, t2 = {0, 0, 0, 0};
#pragma unroll 1
    for (int hf = 0; hf < 2; ++hf) {
      const float lr = hf ? p_lr[1] : p_lr[0], aiv = hf ? p_ai[1] : p_ai[0];
      const float cr = hf ? p_cr[1] : p_cr[0], ci = hf ? p_ci[1] : p_ci[0];
      const float ar = -rbf(expf(lr)) * dt;
      const float ai = aiv * dt;
      const float arl2 = ar * 1.4426950408889634f;
      const cplx z1 = zpow(arl2, ai, 1.f), z8 = zpow(arl2, ai, 8.f), z128 = zpow(arl2, ai, 128.f);
      f16x8 bfr[4];
      {
        SCR_W(0, pk_f16(1.f, 0.f));
        cplx e = z8;
        SCR_W(1, pk_f16(e.re, e.im));
#pragma unroll
        for (int k = 2; k < 16; ++k) { e = cmul(e, z8); SCR_W(k, pk_f16(e.re, e.im)); }
#pragma unroll
        for (int ks = 0; ks < 4; ++ks) bfr[ks] = SCR_R(ks);
      }
      cplx X[8], x16;
      {
        cplx xx = {__builtin_amdgcn_ldexpf(cr, -ex), __builtin_amdgcn_ldexpf(ci, -ex)};
#pragma unroll
        for (int r = 0; r < 16; ++r) {
          if (r < 8) X[r] = xx;
          SCR_W(r, pk_f16(xx.re, -xx.im));
          xx = cmul(xx, z128);
        }
        x16 = xx;
#pragma unroll
        for (int ks = 0; ks < 4; ++ks) { f16x8 a = SCR_R(ks); t0 = MFMA16H(a, bfr[ks], t0); }
      }
      cplx zp[8];
      zp[1] = z1;
#pragma unroll
      for (int k = 2; k < 8; ++k) zp[k] = cmul(zp[k - 1], z1);
      {
        SCR_W(0, pk_f16(x16.re, -x16.im));
#pragma unroll
        for (int k = 1; k < 8; ++k) {
          cplx a = cmul(X[0], zp[k]), b = cmul(X[1], zp[k]);
          SCR_W(k, pk_f16(a.re, -a.im));
          SCR_W(8 + k, pk_f16(b.re, -b.im));
        }
#pragma unroll
        for (int ks = 0; ks < 4; ++ks) { f16x8 a = SCR_R(ks); t1 = MFMA16H(a, bfr[ks], t1); }
      }
      {
#pragma unroll
        for (int k = 0; k < 4; ++k) { cplx a = cmul(X[4 + k], zp[4]); SCR_W(k, pk_f16(a.re, -a.im)); }
#pragma unroll
        for (int k = 0; k < 3; ++k) {
          cplx a = cmul(X[2], zp[2 * k + 2]), b = cmul(X[3], zp[2 * k + 2]);
          SCR_W(4 + k, pk_f16(a.re, -a.im));
          SCR_W(8 + k, pk_f16(b.re, -b.im));
        }
#pragma unroll
        for (int ks = 0; ks < 4; ++ks) { f16x8 a = SCR_R(ks); t2 = MFMA16H(a, bfr[ks], t2); }
      }
    }
#undef SCR_W
#undef SCR_R
    kp[0] = pk_bf16(t0[0] * mult + ((lane == 0) ? v_dsk : 0.f), t0[1] * mult);
    kp[1] = pk_bf16(t0[2] * mult, t0[3] * mult);
    kp[2] = pk_bf16(t1[0] * mult, t1[1] * mult);
    kp[3] = pk_bf16(t1[2] * mult, t1[3] * mult);
    kp[4] = pk_bf16(t2[0] * mult, t2[1] * mult);
    kp[5] = pk_bf16(t2[2] * mult, t2[3] * mult);
  }
  TS(2); TC(5);
  bf16x8 F[16];
  {
    LAS u16* K = (LAS u16*)ub + 16;
    const int DUMMY = 2052;
#pragma unroll
    for (int i = 0; i < 4; ++i) {
      const int row = 4 * rg + i;
      const unsigned v0 = kp[i >> 1], v1 = kp[2 + (i >> 1)], v2 = kp[4 + (i >> 1)];
      K[128 * row + 8 * rr] = (u16)((i & 1) ? (v0 >> 16) : v0);
      int p1 = (row >> 3) * 128 + (row & 7) + 8 * rr;
      if (i == 0) p1 = (lane == 0) ? 2048 : ((row & 7) == 0 ? DUMMY : p1);
      K[p1] = (u16)((i & 1) ? (v1 >> 16) : v1);
      int p2 = (rg == 0) ? (512 + 128 * i + 4 + 8 * rr) : (128 + 128 * rg + 2 * (i + 1) + 8 * rr);
      if (rg == 3 || (rg != 0 && i == 3)) p2 = DUMMY;
      K[p2] = (u16)((i & 1) ? (v2 >> 16) : v2);
    }
    if (lane < 8) *(LAS unsigned*)(ub + lane * 4) = 0u;
    const unsigned a1 = K[256 + 4 * lane], b1 = K[258 + 4 * lane], c1 = K[260 + 4 * lane];
    const unsigned a2 = K[512 + 8 * lane], b2 = K[516 + 8 * lane], c2 = K[520 + 8 * lane];
    const unsigned a3 = K[1024 + 16 * lane], b3 = K[1032 + 16 * lane], c3 = K[1040 + 16 * lane];
    u32x2 w1 = {a1 | (a1 << 16), b1 | (c1 << 16)};
    u32x4 w2 = {a2 | (a2 << 16), a2 | (b2 << 16), b2 | (b2 << 16), c2 | (c2 << 16)};
    u32x4 w3a = {a3 | (a3 << 16), a3 | (a3 << 16), a3 | (b3 << 16), b3 | (b3 << 16)};
    u32x4 w3b = {b3 | (b3 << 16), b3 | (b3 << 16), c3 | (c3 << 16), c3 | (c3 << 16)};
    *(LAS u32x2*)(K + 256 + 4 * lane) = w1;
    *(LAS u32x4*)(K + 512 + 8 * lane) = w2;
    *(LAS u32x4*)(K + 1024 + 16 * lane) = w3a;
    *(LAS u32x4*)(K + 1032 + 16 * lane) = w3b;
    CBAR();
    LAS char* fbp = ub + 2 * (16 - 12 + 32 * rg + 4 * (q4 + p4));
#pragma unroll
    for (int s = 0; s < 16; ++s) F[s] = tr_frag(fbp + 256 * s, fbp + 256 * s + 32);
    asm volatile("s_waitcnt lgkmcnt(0)" ::: "memory");
  }
  TS(3);
  sig(FL + F_TU);
  waitge(FL + F_TU, 4);
#pragma unroll
  for (int i = 0; i < 4; ++i) {
    const int pc = 4 * c + i;
    const int f1 = ((lane >> 4) & 1) | ((i & 1) << 1);
    GLDS((const char*)u + (size_t)((tt16 * 128 + pc * 8 + (lane >> 3)) * 2048 + cg * 128 + (((lane & 7) ^ (f1 << 1)) << 4)),
         sm + L_UB + pc * 1024);
  }
  WAITVM(0);
  sig(FL + F_TU);
  waitge(FL + F_TU, 8);
  TS(4);
  {
    const int f1 = (q4 >> 1) | ((g4 & 1) << 1);
#pragma unroll
    for (int j = 0; j < 4; ++j) {
      LAS char* pp = sm + L_UB + (32 * j + 8 * g4 + q4) * 128 + (((2 * c + (p4 >> 1)) ^ (f1 << 1)) << 4) + (p4 & 1) * 8;
      u32x4 v = tr_pair(pp, pp + 512);
      st16(v, rw, (16 * c + (lane & 15)) * 4096 + (32 * j + 8 * g4) * 2, WS_UT + cg * 64 * 4096 + tt16 * 256);
    }
  }
  waitge(FL + F_FULL, 4);
  const unsigned tag = ldsword(FL + F_TAG);
  WAITVM(0);
  if (sig_ret(FL + F_TS) == 3u) gflag(ws, WS_FU + cg * 128 + tt16 * 4, tag, lane);
  TS(5);
  gpoll((unsigned*)(ws + WS_FU + cg * 128) + (lane & 15), tag);
  TS(6);
  {
    u32x4 ur[4];
#pragma unroll
    for (int i = 0; i < 4; ++i) ur[i] = ld16(rw, i * 1024 + lane * 16, WS_UT + d * 4096);
    *(LAS unsigned*)(ub + lane * 4) = 0u;
#pragma unroll
    for (int i = 0; i < 4; ++i) *(LAS u32x4*)(ub + 256 + i * 1024 + lane * 16) = ur[i];
  }
  CBAR();
  TS(8); TC(2);
  unsigned yp[16];
  {
    LAS char* gb = ub + 2 * (128 + 16 * p4 + 12 - 32 * rg - 4 * q4);
    float pc1 = 0.f, pc2 = 0.f, pc3 = 0.f, yprev = 0.f;
    const bool l0 = (lane == 0);
#define ROR1(v) __int_as_float(__builtin_amdgcn_update_dpp(0, __float_as_int(v), 0x13C, 0xf, 0xf, false))
#define FIN(i)                                                                       \
  do {                                                                               \
    float r3 = ROR1(ca[i][3]);                                                       \
    float s2 = ca[i][2] + (l0 ? pc3 : r3);                                           \
    float r2 = ROR1(s2);                                                             \
    float s1 = ca[i][1] + (l0 ? pc2 : r2);                                           \
    float r1 = ROR1(s1);                                                             \
    float y = ca[i][0] + (l0 ? pc1 : r1);                                            \
    pc1 = r1; pc2 = r2; pc3 = r3;                                                    \
    if ((i) & 1) yp[8 * pass + ((i) >> 1)] = pk_bf16(yprev, y); else yprev = y;      \
  } while (0)
#pragma unroll
    for (int pass = 0; pass < 2; ++pass) {
      f32x4 ca[16];
#pragma unroll
      for (int i = 0; i < 16; ++i) ca[i] = (f32x4){0, 0, 0, 0};
      bf16x8 G = tr_frag(gb, gb - 32);
#pragma unroll
      for (int v = 0; v < 16 * pass + 16; ++v) {
        bf16x8 Gn = G;
        if (v + 1 < 16 * pass + 16) Gn = tr_frag(gb + 128 * (v + 1), gb + 128 * (v + 1) - 32);
        SB();
#pragma unroll
        for (int i = 0; i < 16; ++i) {
          const int T = 16 * pass + i;
          if (T >= v && ((T - v) & 1) == 0) ca[i] = MFMA16(G, F[(T - v) >> 1], ca[i]);
        }
        if (v - 1 >= 16 * pass) FIN(v - 1 - 16 * pass);
        SB();
        G = Gn;
      }
      FIN(15);
      CBAR();
      SB();
    }
#undef FIN
#undef ROR1
  }
  CBAR();
  TS(9); TC(3);
#pragma unroll
  for (int i = 0; i < 16; ++i) {
    ((LAS u16*)ub)[128 * i + lane] = (u16)yp[i];
    ((LAS u16*)ub)[128 * i + 64 + lane] = (u16)(yp[i] >> 16);
  }
  CBAR();
#pragma unroll
  for (int i = 0; i < 4; ++i) st16(*(LAS u32x4*)(ub + i * 1024 + lane * 16), rw, i * 1024 + lane * 16, WS_YT + d * 4096);
  WAITVM(0);
  if (sig_ret(FL + F_CY) == 3u) gflag(ws, WS_FY + cg * 128 + tt16 * 4, tag, lane);
  TS(10);
  gpoll((unsigned*)(ws + WS_FY + cg * 128) + (lane & 15), tag);
  TS(11);
  {
    int ln2 = lane;
    asm volatile("" : "+v"(ln2));
    const int lane = ln2, q4 = (lane & 15) >> 2, p4 = lane & 3, g4 = lane >> 4;
    LAS char* tb = sm + L_UB;
    u32x4 yr[4];
#pragma unroll
    for (int i = 0; i < 4; ++i)
      yr[i] = ld16(rw, (16 * c + 4 * i + g4) * 4096 + (lane & 15) * 16, WS_YT + cg * 64 * 4096 + tt16 * 256);
#pragma unroll
    for (int i = 0; i < 4; ++i) {
      const int row = 16 * c + 4 * i + g4;
      const int f = (row & 3) | (((row >> 3) & 1) << 2);
      *(LAS u32x4*)(tb + row * 256 + (((lane & 15) ^ (f << 1)) << 4)) = yr[i];
    }
    sig(FL + F_T2);
    waitge(FL + F_T2, 4);
    const int f = q4 | ((g4 & 1) << 2);
#pragma unroll
    for (int j = 0; j < 4; ++j) {
      const int tbk = 2 * c + (j >> 1), hf = j & 1;
      LAS char* pp = tb + (32 * hf + 8 * g4 + q4) * 256 + (((2 * tbk + (p4 >> 1)) ^ (f << 1)) << 4) + (p4 & 1) * 8;
      u32x4 v = tr_pair(pp, pp + 1024);
      OST((u32x4*)(out + (size_t)(tt16 * 128 + 16 * tbk + (lane & 15)) * 2048 + 1024 + cg * 64 + 32 * hf + 8 * g4), v);
    }
  }
  TS(12);
  TSDUMP();
}

extern "C" void kernel_launch(void* const* d_in, const int* in_sizes, int n_in, void* d_out, int out_size,
                              void* d_ws, size_t ws_size, hipStream_t stream) {
  fused_kernel<<<256, 768, 0, stream>>>((const u16*)d_in[0], (const u16*)d_in[1], (const u16*)d_in[2], (const u16*)d_in[3],
                                        (const u16*)d_in[4], (const u16*)d_in[5], (const u16*)d_in[6], (const u16*)d_in[7],
                                        (const u16*)d_in[8], (const u16*)d_in[9], (u16*)d_out, (char*)d_ws);
}
```

```cpp
#include <hip/hip_runtime.h>
#include <stdint.h>

#define LAS __attribute__((address_space(3)))
#define GAS __attribute__((address_space(1)))
#define DI __device__ __forceinline__

typedef unsigned short u16;
typedef short s16x4 __attribute__((ext_vector_type(4)));
typedef __bf16 bf16x2 __attribute__((ext_vector_type(2)));
typedef __bf16 bf16x8 __attribute__((ext_vector_type(8)));
typedef _Float16 f16x2 __attribute__((ext_vector_type(2)));
typedef _Float16 f16x8 __attribute__((ext_vector_type(8)));
typedef float f32x2 __attribute__((ext_vector_type(2)));
typedef float f32x4 __attribute__((ext_vector_type(4)));
typedef float f32x16 __attribute__((ext_vector_type(16)));
typedef unsigned u32x2 __attribute__((ext_vector_type(2)));
typedef unsigned u32x4 __attribute__((ext_vector_type(4)));
typedef __amdgpu_buffer_rsrc_t rsrc_t;

#define CBAR() asm volatile("" ::: "memory")
#define SB() __builtin_amdgcn_sched_barrier(0)
#define WAITVM(n) asm volatile("s_waitcnt vmcnt(" #n ")" ::: "memory")
#define TSDECL
#define TS(i)
#define TC(i)
#define TSDUMP()
#define PRIO_C 2
#define PRIO_L 3
#define PSLEEP 3
#define NRING 12

DI void ost16(void* p, u32x4 v) { asm volatile("global_store_dwordx4 %0, %1, off sc1\n\ts_nop 1" :: "v"(p), "v"(v) : "memory"); }
DI float sum16(float x) {
  auto r = __builtin_amdgcn_permlane16_swap(__float_as_uint(x), __float_as_uint(x), false, false);
  return __uint_as_float(r[0]) + __uint_as_float(r[1]);
}
DI float sum32(float x) {
  auto r = __builtin_amdgcn_permlane32_swap(__float_as_uint(x), __float_as_uint(x), false, false);
  return __uint_as_float(r[0]) + __uint_as_float(r[1]);
}
DI float sum8(float x) {
  return x + __int_as_float(__builtin_amdgcn_update_dpp(0, __float_as_int(x), 0x128, 0xf, 0xf, false));
}
DI float bf2f(u16 v) { return __uint_as_float((unsigned)v << 16); }
DI unsigned pk_bf16(float a, float b) {
  f32x2 f = {a, b};
  return __builtin_bit_cast(unsigned, __builtin_convertvector(f, bf16x2));
}
DI unsigned pk_f16(float a, float b) {
  f32x2 f = {a, b};
  return __builtin_bit_cast(unsigned, __builtin_convertvector(f, f16x2));
}
DI float rbf(float a) { return __uint_as_float(pk_bf16(a, 0.f) << 16); }
DI u32x2 tr_read(LAS char* p) {
  s16x4 v = __builtin_amdgcn_ds_read_tr16_b64_v4i16((LAS s16x4*)p);
  return __builtin_bit_cast(u32x2, v);
}
DI u32x4 join(u32x2 a, u32x2 b) { u32x4 r = {a.x, a.y, b.x, b.y}; return r; }
DI u32x4 tr_pair(LAS char* p0, LAS char* p1) { return join(tr_read(p0), tr_read(p1)); }
DI bf16x8 asbf(u32x4 v) { return __builtin_bit_cast(bf16x8, v); }
DI bf16x8 tr_frag(LAS char* p0, LAS char* p1) { return asbf(tr_pair(p0, p1)); }
#define MFMA32(a, b, c) __builtin_amdgcn_mfma_f32_32x32x16_bf16((a), (b), (c), 0, 0, 0)
#define MFMA16(a, b, c) __builtin_amdgcn_mfma_f32_16x16x32_bf16((a), (b), (c), 0, 0, 0)
#define MFMA16H(a, b, c) __builtin_amdgcn_mfma_f32_16x16x32_f16((a), (b), (c), 0, 0, 0)
#define AMFMA16(c, a, b) asm volatile("v_mfma_f32_16x16x32_bf16 %0, %1, %2, %0" : "+v"(c) : "v"(a), "v"(b))
#define GLDS(src, dst) __builtin_amdgcn_global_load_lds((const GAS unsigned*)(src), (LAS unsigned*)(dst), 16, 0, 0)
#define SC1 16
DI u32x4 ld16(rsrc_t r, int voff, int soff) { return __builtin_amdgcn_raw_buffer_load_b128(r, voff, soff, SC1); }
DI void st16(u32x4 v, rsrc_t r, int voff, int soff) {
  __builtin_amdgcn_raw_buffer_store_b128(v, r, voff, soff, SC1);
  asm volatile("s_nop 1" :: "v"(v) : "memory");
}

constexpr int WS_EPOCH = 0;
constexpr int WS_FU = 65536, WS_FP = WS_FU + 32768, WS_FY = WS_FP + 32768, WS_FR = WS_FY + 32768;
constexpr int WS_PQ = 262144;
constexpr int WS_UT = WS_PQ + (4 << 20);
constexpr int WS_YT = WS_UT + (4 << 20);
constexpr int WS_KVP = WS_YT + (4 << 20);
constexpr int WS_ZP = WS_KVP + 32 * 16 * 4096;
constexpr int WS_KVR = WS_ZP + 32 * 16 * 128;
constexpr int WS_END = WS_KVR + 32 * 2048;

constexpr int SLOT = 40960;
constexpr int L_UB = 131072, UB_CH = 4352;
constexpr int L_X = L_UB + 4 * UB_CH;
constexpr int L_Z = L_X + 4 * 2176;
constexpr int L_FLAG = L_Z + 512;
constexpr int LDS_BYTES = L_FLAG + 256;
enum { F_FULL = 0, F_FREE = 4, F_XE = 8, F_EQ = 10, F_LW, F_TAG, F_TU, F_TS, F_CY, F_T2, F_XP, F_RD, F_PQ, F_LM = 20, F_Z = 24, F_Y = 26 };

struct cplx { float re, im; };
DI cplx cmul(cplx a, cplx b) {
  cplx r;
  r.re = __builtin_fmaf(a.re, b.re, -(a.im * b.im));
  r.im = __builtin_fmaf(a.re, b.im, a.im * b.re);
  return r;
}
DI cplx zpow(float arl2, float ai, float p) {
  const float INV2PI = 0.15915494309189535f;
  const float INV2PI_LO = (float)(0.15915494309189533576888 - (double)0.15915494309189535f);
  float e = __builtin_amdgcn_exp2f(arl2 * p);
  float r = ai * p;
  float hi = r * INV2PI;
  float lo = __builtin_fmaf(r, INV2PI_LO, __builtin_fmaf(r, INV2PI, -hi));
  float f = (hi - __builtin_rintf(hi)) + lo;
  cplx z;
  z.re = e * __builtin_amdgcn_cosf(f);
  z.im = e * __builtin_amdgcn_sinf(f);
  return z;
}
DI float phi_f(float t) {
  float e = __builtin_amdgcn_fmed3f(__builtin_amdgcn_exp2f(t * 1.4426950408889634f), 0.f, 1.f);
  return (__builtin_amdgcn_fmed3f(t, 0.f, __builtin_inff()) + 1e-6f) + e;
}
DI u32x4 tag_put(f32x4 v, unsigned tag) {
  u32x4 b = __builtin_bit_cast(u32x4, v), r;
  r[0] = __builtin_amdgcn_perm(b[0], tag, 0x07060500u);
  r[1] = __builtin_amdgcn_perm(b[1], tag, 0x07060501u);
  r[2] = __builtin_amdgcn_perm(b[2], tag, 0x07060502u);
  r[3] = __builtin_amdgcn_perm(b[3], tag, 0x07060503u);
  return r;
}
DI unsigned tag_get(u32x4 b) {
  unsigned lo = __builtin_amdgcn_perm(b[1], b[0], 0x0c0c0400u), hi = __builtin_amdgcn_perm(b[3], b[2], 0x04000c0cu);
  return lo | hi;
}
DI f32x4 tag_val(u32x4 b) { return __builtin_bit_cast(f32x4, b & 0xffffff00u); }

DI void sig(LAS unsigned* f) {
  asm volatile("s_mov_b64 exec, 1\n\tds_add_u32 %0, %1\n\ts_mov_b64 exec, -1" :: "v"((unsigned)(size_t)f), "v"(1u) : "memory");
}
DI unsigned sig_ret(LAS unsigned* f) {
  unsigned r;
  asm volatile("s_mov_b64 exec, 1\n\tds_add_rtn_u32 %0, %1, %2\n\ts_mov_b64 exec, -1\n\ts_waitcnt lgkmcnt(0)"
               : "=&v"(r) : "v"((unsigned)(size_t)f), "v"(1u) : "memory");
  return __builtin_amdgcn_readfirstlane(r);
}
DI unsigned ldsword(LAS unsigned* f) { return __builtin_amdgcn_readfirstlane(*(volatile LAS unsigned*)f); }
DI void waitge(LAS unsigned* f, unsigned n) {
  CBAR();
#pragma unroll 1
  for (int it = 0; it < (1 << 22); ++it) {
    if (ldsword(f) >= n) break;
    __builtin_amdgcn_s_sleep(1);
  }
  CBAR();
}
DI void gflag(char* ws, int off, unsigned tag, int lane) {
  CBAR();
  if (lane == 0) __hip_atomic_store((unsigned*)(ws + off), tag, __ATOMIC_RELAXED, __HIP_MEMORY_SCOPE_AGENT);
  CBAR();
}
DI void gpoll(char* p, unsigned tag) {
  CBAR();
#pragma unroll 1
  for (int it = 0; it < (1 << 18); ++it) {
    unsigned v = __hip_atomic_load((unsigned*)p, __ATOMIC_RELAXED, __HIP_MEMORY_SCOPE_AGENT);
    if (__all(v == tag)) break;
    __builtin_amdgcn_s_sleep(PSLEEP);
  }
  CBAR();
}

__global__ __launch_bounds__(768, 3) void fused_kernel(const u16* __restrict__ x, const u16* __restrict__ wqkv,
                                                       const u16* __restrict__ wo, const u16* __restrict__ u,
                                                       const u16* __restrict__ lar, const u16* __restrict__ aim,
                                                       const u16* __restrict__ cre, const u16* __restrict__ cim,
                                                       const u16* __restrict__ ldt, const u16* __restrict__ dsk,
                                                       u16* __restrict__ out, char* __restrict__ ws) {
  __shared__ __attribute__((aligned(16))) char smem[LDS_BYTES];
  LAS char* sm = (LAS char*)smem;
  LAS unsigned* FL = (LAS unsigned*)(sm + L_FLAG);
  const int tid = threadIdx.x, lane = tid & 63;
  const int wave = __builtin_amdgcn_readfirstlane(tid >> 6);
  const int bid = blockIdx.x, xcd = bid & 7, idx = bid >> 3;
  const int ml = idx & 7, hl = idx >> 3;
  const int mb = (xcd & 1) * 8 + ml, hp = (xcd >> 1) * 4 + hl;
  const int mb2 = (xcd & 3) * 4 + (idx & 3), nb = (xcd >> 2) * 8 + (idx >> 2);
  const int rot = (idx >> 2) * 4;
  const rsrc_t rw = __builtin_amdgcn_make_buffer_rsrc((void*)ws, 0, WS_END, 0x00020000);
  TSDECL; TS(0);
  const int q4 = (lane & 15) >> 2, p4 = lane & 3, g4 = lane >> 4, c16 = lane & 15;

  if ((wave >> 2) == 1) {
    __builtin_amdgcn_s_setprio(PRIO_L);
    const int hw = wave - 4;
    unsigned ep = 0;
    if (hw == 0) ep = __hip_atomic_load((unsigned*)(ws + WS_EPOCH), __ATOMIC_RELAXED, __HIP_MEMORY_SCOPE_AGENT);
    const int koff = 2 * ml + 4 * hl + (hl >> 1);
    const unsigned vx0 = (unsigned)((mb * 128 + hw * 32 + (lane >> 3)) * 2048) + (((lane & 7) ^ (lane >> 4)) << 4), vx1 = vx0 ^ 64;
    const unsigned vw0 = (unsigned)((lane >> 3) * 6144 + hp * 128 + (((lane & 7) ^ (((lane >> 4) & 1) << 1)) << 4)), vw1 = vw0 ^ 64;
#define ISSUE(tt_, slot_)                                                                                  \
  do {                                                                                                     \
    const int k_ = ((tt_) + koff) & 15;                                                                    \
    const char* xa_ = (const char*)x + k_ * 128;                                                           \
    const char* wa_ = (const char*)wqkv + (size_t)k_ * 393216;                                             \
    LAS char* sb_ = sm + (slot_) * SLOT;                                                                   \
    _Pragma("unroll") for (int i = 0; i < 4; ++i)                                                          \
        GLDS(xa_ + i * 16384 + ((i & 1) ? vx1 : vx0), sb_ + (hw * 4 + i) * 1024);                          \
    _Pragma("unroll") for (int i = 0; i < 6; ++i) {                                                        \
      const int qq_ = hw * 6 + i;                                                                          \
      GLDS(wa_ + ((qq_ & 7) * 49152 + (qq_ >> 3) * 2048) + ((i & 1) ? vw1 : vw0), sb_ + 16384 + qq_ * 1024); \
    }                                                                                                      \
  } while (0)
    ISSUE(0, 0);
    __builtin_amdgcn_s_barrier();
    CBAR();
    TS(13);
    ISSUE(1, 1);
    TS(14);
    WAITVM(10);
    if (hw == 0) {
      const unsigned tg = __builtin_amdgcn_readfirstlane(ep) * 2654435761u + 0x9E3779B1u;
      *(volatile LAS unsigned*)(FL + F_TAG) = tg;
    }
    sig(FL + F_FULL);
    TS(1);
    ISSUE(2, 2);
    {
      int sp = 1, sf = 0, needf = 4;
#pragma unroll 1
      for (int p = 1; p < 14; ++p) {
        WAITVM(10);
        sig(FL + F_FULL + sp);
        waitge(FL + F_FREE + sf, (unsigned)needf);
        ISSUE(p + 2, sf);
        sf = sp;
        if (sf == 0) needf += 4;
        sp = (sp == 2) ? 0 : sp + 1;
      }
    }
#undef ISSUE
    TS(2);
    {
      const char* src = (const char*)wo + (size_t)((8 * hw + (lane >> 3)) * 2048 + nb * 128 + (((lane & 7) ^ ((lane >> 4) << 1)) << 4));
#define WOP(s_) GLDS(src + (size_t)((((s_) + rot) & 31) * 65536), sm + ((s_) * 4 + hw) * 1024)
      WOP(30); WOP(31);
      WAITVM(12);
      sig(FL + F_FULL + 2);
      waitge(FL + F_FREE + 1, 20);
#pragma unroll
      for (int i = 10; i < 20; ++i) WOP(i);
      WAITVM(12);
      sig(FL + F_FULL + 0);
      waitge(FL + F_FREE + 2, 20);
#pragma unroll
      for (int i = 20; i < 30; ++i) WOP(i);
      waitge(FL + F_FREE + 0, 24);
#pragma unroll
      for (int i = 0; i < 10; ++i) WOP(i);
#undef WOP
      TS(3);
      WAITVM(0);
      sig(FL + F_LW);
    }
    TS(4);
    const unsigned tag = ldsword(FL + F_TAG);
    waitge(FL + F_XP, 4);
    if ((idx & 3) == 0) {
      const int ri = xcd * 8 + (idx >> 2), rh = ri >> 1, td = ri & 1;
      const int ln = hw * 16 + c16;
      const int so = WS_KVP + rh * 65536 + td * 2048;
      const int vo = g4 * 16384 + ln * 16;
      u32x4 a[8];
#pragma unroll 1
      for (int it = 0; it < (1 << 17); ++it) {
        CBAR();
#pragma unroll
        for (int i = 0; i < 8; ++i) a[i] = ld16(rw, vo + (i >> 1) * 4096 + (i & 1) * 1024, so);
        unsigned bad = 0;
#pragma unroll
        for (int i = 0; i < 8; ++i) bad |= tag_get(a[i]) ^ tag;
        if (__all(bad == 0u)) break;
        __builtin_amdgcn_s_sleep(PSLEEP);
      }
      TS(5);
      f32x4 s0 = (tag_val(a[0]) + tag_val(a[2])) + (tag_val(a[4]) + tag_val(a[6]));
      f32x4 s1 = (tag_val(a[1]) + tag_val(a[3])) + (tag_val(a[5]) + tag_val(a[7]));
#pragma unroll
      for (int i = 0; i < 4; ++i) { s0[i] = sum32(sum16(s0[i])); s1[i] = sum32(sum16(s1[i])); }
      u32x4 w = {pk_bf16(s0[0], s0[1]), pk_bf16(s0[2], s0[3]), pk_bf16(s1[0], s1[1]), pk_bf16(s1[2], s1[3])};
      if (lane < 16) st16(w, rw, ln * 16, WS_KVR + ri * 1024);
      WAITVM(0);
      if (sig_ret(FL + F_RD) == 3u) gflag(ws, WS_FR + ri * 128, tag, lane);
      TS(6);
    }
    {
      const int hsw = (q4 >> 1) | ((g4 & 1) << 1);
      LAS char* wb = sm + hw * 4096;
      const int w0 = (4 * g4 + q4) * 128 + (((p4 >> 1) ^ (hsw << 1)) << 4) + (p4 & 1) * 8;
      const int bq = (c16 >> 2) & 1;
      const int m0 = bq * 1024 + g4 * 256 + (4 * ((c16 >> 3) ^ bq) + p4) * 16;
      bf16x8 wf[4][4];
      waitge(FL + F_LW, 4);
#define RWF(i_) _Pragma("unroll") for (int nt = 0; nt < 4; ++nt) {                                  \
        LAS char* pp = wb + (i_) * 16384 + (w0 ^ (nt << 5)); wf[(i_) & 3][nt] = tr_frag(pp, pp + 2048); }
#pragma unroll
      for (int i = 0; i < 4; ++i) RWF(i);
      gpoll(ws + WS_FR + ((((hw + 4 * ((lane >> 1) & 7) + rot) & 31) * 2 + (lane & 1)) << 7), tag);
      TS(7);
      u32x4 kf[8][2];
#pragma unroll
      for (int i = 0; i < 8; ++i)
#pragma unroll
        for (int td = 0; td < 2; ++td) kf[i][td] = ld16(rw, td * 1024 + lane * 16, WS_KVR + ((hw + 4 * i + rot) & 31) * 2048);
#pragma unroll
      for (int i = 0; i < 8; ++i) {
#pragma unroll
        for (int nt = 0; nt < 4; ++nt) {
          const f32x4 z4 = {0.f, 0.f, 0.f, 0.f};
          f32x4 d0 = MFMA16(asbf(kf[i][0]), wf[i & 3][nt], z4);
          f32x4 d1 = MFMA16(asbf(kf[i][1]), wf[i & 3][nt], z4);
          u32x4 mw = {pk_bf16(d0[0], d0[1]), pk_bf16(d0[2], d0[3]), pk_bf16(d1[0], d1[1]), pk_bf16(d1[2], d1[3])};
          *(LAS u32x4*)(wb + i * 16384 + (nt >> 1) * 2048 + (nt & 1) * 128 + m0) = mw;
        }
        if (i & 1) sig(FL + F_LM + (i >> 1));
        if (i < 4) RWF(i + 4);
      }
#undef RWF
    }
    TS(9);
    TSDUMP();
    return;
  }

  if (wave < 4) {
    if (wave == 0) FL[lane] = 0u;
    asm volatile("s_waitcnt lgkmcnt(0)" ::: "memory");
    __builtin_amdgcn_s_barrier();
    CBAR();
    __builtin_amdgcn_s_setprio(PRIO_C);
    const int mh = wave & 1, hd = wave >> 1;
    const int a0 = (mh * 64 + c16) * 128 + ((g4 ^ (c16 >> 1)) << 4);
    const int b0 = 16384 + (8 * g4 + q4) * 128 + (((hd * 4 + (p4 >> 1)) ^ (((q4 >> 1) | ((g4 & 1) << 1)) << 1)) << 4) + (p4 & 1) * 8;
    f32x4 acc[4][6];
#pragma unroll
    for (int m = 0; m < 4; ++m)
#pragma unroll
      for (int g = 0; g < 6; ++g) acc[m][g] = (f32x4){0.f, 0.f, 0.f, 0.f};
#pragma unroll
    for (int m = 0; m < 4; ++m)
      asm volatile("" : "+v"(acc[m][0]), "+v"(acc[m][1]), "+v"(acc[m][2]), "+v"(acc[m][3]), "+v"(acc[m][4]), "+v"(acc[m][5]));
    bf16x8 fa[2][4];
    u32x2 bl[3], bh[3];
#define RA(set, m, pa) fa[set][m] = *(LAS bf16x8*)((pa) + (m) * 2048)
#define RL(b, ks, pe, po) bl[(b) % 3] = tr_read((((b) & 1) ? (po) : (pe)) + ((b) >> 1) * 8192 + (ks) * 4096)
#define RH(b, ks, pe, po) bh[(b) % 3] = tr_read((((b) & 1) ? (po) : (pe)) + ((b) >> 1) * 8192 + (ks) * 4096 + 512)
#define FB(b) asbf(join(bl[(b) % 3], bh[(b) % 3]))
#define MM(set, b, m) acc[m][b] = ((b) < 2) ? MFMA16(FB(b), fa[set][m], acc[m][b]) : MFMA16(fa[set][m], FB(b), acc[m][b])
#define GRP(set, b, X0, X1, X2, X3)                     \
  do {                                                  \
    MM(set, b, 0); X0; SB(); MM(set, b, 1); X1; SB();   \
    MM(set, b, 2); X2; SB(); MM(set, b, 3); X3; SB();   \
  } while (0)
    LAS char* pa = sm + a0;
    LAS char* pe = sm + b0;
    LAS char* po = sm + (b0 ^ 32);
    waitge(FL + F_FULL, 4);
    TS(3); TC(0);
    RA(0, 0, pa); RL(0, 0, pe, po); RH(0, 0, pe, po); RA(0, 1, pa); RA(0, 2, pa); RA(0, 3, pa); RL(1, 0, pe, po); RH(1, 0, pe, po);
    {
      int slot = 0, need = 4;
#pragma unroll 1
      for (int t = 0; t < 16; ++t) {
        const int ns = (slot == 2) ? 0 : slot + 1;
        if (ns == 0) need += 4;
        if (t == 15) need = 0;
        LAS char* pa1 = sm + ((a0 ^ 64) + slot * SLOT);
        LAS char* pan = sm + (a0 + ns * SLOT);
        LAS char* pen = sm + (b0 + ns * SLOT);
        LAS char* pon = sm + ((b0 ^ 32) + ns * SLOT);
        unsigned fv;
        SB();
        GRP(0, 0, RL(2, 0, pe, po), RH(2, 0, pe, po), , );
        GRP(0, 1, RL(3, 0, pe, po), RH(3, 0, pe, po), , );
        GRP(0, 2, RL(4, 0, pe, po), RH(4, 0, pe, po), , );
        GRP(0, 3, RL(5, 0, pe, po), RH(5, 0, pe, po), RA(1, 0, pa1), RA(1, 1, pa1));
        GRP(0, 4, RL(0, 1, pe, po), RH(0, 1, pe, po), RA(1, 2, pa1), RA(1, 3, pa1));
        GRP(0, 5, RL(1, 1, pe, po), RH(1, 1, pe, po), , );
        GRP(1, 0, RL(2, 1, pe, po), RH(2, 1, pe, po), , );
        GRP(1, 1, RL(3, 1, pe, po), RH(3, 1, pe, po), { fv = *(volatile LAS unsigned*)(FL + F_FULL + ns); }, );
        GRP(1, 2, RL(4, 1, pe, po), RH(4, 1, pe, po), , );
        MM(1, 3, 0); RL(5, 1, pe, po); SB();
        MM(1, 3, 1); RH(5, 1, pe, po); sig(FL + F_FREE + slot); SB();
        if (__builtin_amdgcn_readfirstlane(fv) < (unsigned)need) {
#pragma unroll 1
          for (int it = 0; it < (1 << 24); ++it) { if (ldsword(FL + F_FULL + ns) >= (unsigned)need) break; }
        }
        CBAR();
        SB();
        MM(1, 3, 2); RA(0, 0, pan); SB();
        MM(1, 3, 3); RA(0, 1, pan); SB();
        GRP(1, 4, RL(0, 0, pen, pon), RH(0, 0, pen, pon), RA(0, 2, pan), RA(0, 3, pan));
        GRP(1, 5, RL(1, 0, pen, pon), RH(1, 0, pen, pon), , );
        pe = pen; po = pon;
        slot = ns;
      }
    }
#undef RA
#undef RL
#undef RH
#undef FB
#undef MM
#undef GRP
    TS(4); TC(1);
    const unsigned tag = ldsword(FL + F_TAG);
    const int head = hp * 2 + hd;
    {
      f32x4 kvt[2][2], zt[2];
#pragma unroll
      for (int i = 0; i < 2; ++i) { kvt[i][0] = kvt[i][1] = zt[i] = (f32x4){0.f, 0.f, 0.f, 0.f}; }
      const u32x4 ones = {0x3F803F80u, 0x3F803F80u, 0x3F803F80u, 0x3F803F80u};
#pragma unroll
      for (int j = 0; j < 2; ++j) {
        u32x4 fk[2], fv[2];
#pragma unroll
        for (int n = 0; n < 2; ++n)
#pragma unroll
          for (int i = 0; i < 4; ++i) {
            const f32x4 kk = acc[2 * j + (i >> 1)][2 + n], vv = acc[2 * j + (i >> 1)][4 + n];
            fk[n][i] = pk_bf16(phi_f(kk[2 * (i & 1)]), phi_f(kk[2 * (i & 1) + 1]));
            fv[n][i] = pk_bf16(vv[2 * (i & 1)], vv[2 * (i & 1) + 1]);
          }
#pragma unroll
        for (int td = 0; td < 2; ++td) {
          kvt[0][td] = MFMA16(asbf(fv[0]), asbf(fk[td]), kvt[0][td]);
          kvt[1][td] = MFMA16(asbf(fv[1]), asbf(fk[td]), kvt[1][td]);
          zt[td] = MFMA16(asbf(ones), asbf(fk[td]), zt[td]);
        }
      }
      LAS char* xo = sm + L_X + wave * 2176;
      LAS char* xi = sm + L_X + (wave ^ 1) * 2176;
      f32x4 keep[2];
#pragma unroll
      for (int td = 0; td < 2; ++td) {
        f32x4 ka = kvt[0][td], kb = kvt[1][td];
        asm("" : "+v"(ka), "+v"(kb));
        *(LAS f32x4*)(xo + td * 1024 + lane * 16) = mh ? ka : kb;
        keep[td] = mh ? kb : ka;
      }
      *(LAS float*)(xo + 2048 + (lane & 31) * 4) = (g4 & 1) ? zt[1][0] : zt[0][0];
      sig(FL + F_XE + hd);
      waitge(FL + F_XE + hd, 2);
      if (mh == 0) {
        f32x4 zz = *(LAS f32x4*)(xo + 2048 + (lane & 7) * 16) + *(LAS f32x4*)(xi + 2048 + (lane & 7) * 16);
        if (lane < 8) st16(tag_put(zz, tag), rw, lane * 16, WS_ZP + (head * 16 + mb) * 128);
      }
      const int so = WS_KVP + (head * 16 + mb) * 4096 + mh * 1024;
#pragma unroll
      for (int td = 0; td < 2; ++td) {
        f32x4 o = *(LAS f32x4*)(xi + td * 1024 + lane * 16) + keep[td];
        st16(tag_put(o, tag), rw, td * 2048 + lane * 16, so);
      }
      sig(FL + F_XP);
    }
    TS(5);
#pragma unroll
    for (int m = 0; m < 4; ++m)
#pragma unroll
      for (int i = 0; i < 8; ++i) acc[m][i >> 2][i & 3] = phi_f(acc[m][i >> 2][i & 3]);
    asm volatile("" : "+v"(acc[0][0]), "+v"(acc[1][0]), "+v"(acc[2][0]), "+v"(acc[3][0]),
                      "+v"(acc[0][1]), "+v"(acc[1][1]), "+v"(acc[2][1]), "+v"(acc[3][1]));
    {
      LAS f32x4* zl = (LAS f32x4*)(sm + L_Z + hd * 128);
      if (mh == 1) {
        u32x4 za, zb;
#pragma unroll 1
        for (int it = 0; it < (1 << 17); ++it) {
          CBAR();
          za = ld16(rw, lane * 16, WS_ZP + head * 2048);
          zb = ld16(rw, 1024 + lane * 16, WS_ZP + head * 2048);
          if (__all(((tag_get(za) ^ tag) | (tag_get(zb) ^ tag)) == 0u)) break;
          __builtin_amdgcn_s_sleep(PSLEEP);
        }
        f32x4 z = tag_val(za) + tag_val(zb);
#pragma unroll
        for (int i = 0; i < 4; ++i) z[i] = sum32(sum16(sum8(z[i])));
        if (lane < 8) zl[lane] = z;
        sig(FL + F_Z + hd);
      } else {
        waitge(FL + F_Z + hd, 1);
      }
      TS(6);
      const f32x4 z0 = zl[g4], z1 = zl[4 + g4];
#pragma unroll
      for (int m = 0; m < 4; ++m) {
        float d0 = acc[m][0][0] * z0[0], d1 = acc[m][1][0] * z1[0];
#pragma unroll
        for (int i = 1; i < 4; ++i) { d0 = __builtin_fmaf(acc[m][0][i], z0[i], d0); d1 = __builtin_fmaf(acc[m][1][i], z1[i], d1); }
        const float den = sum32(sum16(d0 + d1));
        const float rc = __builtin_amdgcn_rcpf(den + 1e-6f);
        u32x4 fq = {pk_bf16(acc[m][0][0] * rc, acc[m][0][1] * rc), pk_bf16(acc[m][0][2] * rc, acc[m][0][3] * rc),
                    pk_bf16(acc[m][1][0] * rc, acc[m][1][1] * rc), pk_bf16(acc[m][1][2] * rc, acc[m][1][3] * rc)};
        st16(fq, rw, (m & 1) * 1024 + lane * 16, WS_PQ + ((mb * 4 + mh * 2 + (m >> 1)) * 32 + head) * 2048);
      }
    }
    TS(7);
    WAITVM(0);
    if (sig_ret(FL + F_EQ) == 3u) {
      gflag(ws, WS_FP + (mb * 16 + hp) * 128, tag, lane);
      TS(8);
      gpoll(ws + WS_FP + ((mb2 * 16 + c16) << 7), tag);
      sig(FL + F_PQ);
    } else {
      TS(8);
      waitge(FL + F_PQ, 1);
    }
    TS(9);
    {
      const int pqs = WS_PQ + (mb2 * 4 + wave) * 65536;
      u32x4 pb[NRING][2];
#define LDQ(i_) do { const int so_ = pqs + ((((i_) + rot) & 31) << 11);                              \
        pb[(i_) % NRING][0] = ld16(rw, lane * 16, so_); pb[(i_) % NRING][1] = ld16(rw, 1024 + lane * 16, so_); } while (0)
#pragma unroll
      for (int i = 0; i < NRING; ++i) LDQ(i);
      f32x4 oc[2][4];
#pragma unroll
      for (int s = 0; s < 2; ++s)
#pragma unroll
        for (int t = 0; t < 4; ++t) oc[s][t] = (f32x4){0.f, 0.f, 0.f, 0.f};
      asm volatile("" : "+v"(oc[0][0]), "+v"(oc[0][1]), "+v"(oc[0][2]), "+v"(oc[0][3]),
                   "+v"(oc[1][0]), "+v"(oc[1][1]), "+v"(oc[1][2]), "+v"(oc[1][3]));
      LAS char* mp0 = sm + lane * 16;
      LAS char* mp1 = sm + (lane ^ 4) * 16;
      bf16x8 ma[4];
      waitge(FL + F_LM, 4);
      if (bid == 0 && wave == 0) gflag(ws, WS_EPOCH, tag, lane);
      TS(10); TC(2);
#pragma unroll
      for (int t = 0; t < 4; ++t) ma[t] = *(LAS bf16x8*)(((t & 1) ? mp1 : mp0) + t * 1024);
#pragma unroll
      for (int i = 0; i < 32; ++i) {
        if ((i & 7) == 7 && i < 31) waitge(FL + F_LM + (i >> 3) + 1, 4);
        SB();
        const u32x4 b0_ = pb[i % NRING][0], b1_ = pb[i % NRING][1];
#pragma unroll
        for (int t = 0; t < 4; ++t) {
          AMFMA16(oc[0][t], ma[t], b0_);
          AMFMA16(oc[1][t], ma[t], b1_);
          if (i < 31) ma[t] = *(LAS bf16x8*)(((t & 1) ? mp1 : mp0) + (i + 1) * 4096 + t * 1024);
          SB();
        }
        if (i + NRING < 32) LDQ(i + NRING);
      }
#undef LDQ
      asm volatile("s_nop 7\n\ts_nop 3" : "+v"(oc[0][0]), "+v"(oc[0][1]), "+v"(oc[0][2]), "+v"(oc[0][3]),
                   "+v"(oc[1][0]), "+v"(oc[1][1]), "+v"(oc[1][2]), "+v"(oc[1][3]));
      TS(11); TC(3);
#pragma unroll
      for (int s = 0; s < 2; ++s)
#pragma unroll
        for (int j = 0; j < 2; ++j) {
          u32x4 w = {pk_bf16(oc[s][2 * j][0], oc[s][2 * j][1]), pk_bf16(oc[s][2 * j][2], oc[s][2 * j][3]),
                     pk_bf16(oc[s][2 * j + 1][0], oc[s][2 * j + 1][1]), pk_bf16(oc[s][2 * j + 1][2], oc[s][2 * j + 1][3])};
          ost16(out + (size_t)((mb2 * 4 + wave) * 32 + s * 16 + c16) * 2048 + nb * 64 + 32 * j + 8 * g4, w);
        }
    }
    TS(12);
    TSDUMP();
    return;
  }

  const int c = wave - 8, d = bid * 4 + c;
  const int cg = bid >> 4, tt16 = bid & 15;
#pragma unroll
  for (int i = 0; i < 4; ++i) {
    const int pc = 4 * c + i;
    const int f1 = ((lane >> 4) & 1) | ((i & 1) << 1);
    GLDS((const char*)u + (size_t)((tt16 * 128 + pc * 8 + (lane >> 3)) * 2048 + cg * 128 + (((lane & 7) ^ (f1 << 1)) << 4)),
         sm + L_UB + pc * 1024);
  }
  float p_lr[2], p_ai[2], p_cr[2], p_ci[2];
#pragma unroll
  for (int hf = 0; hf < 2; ++hf) {
    const int i = d * 128 + hf * 64 + lane;
    p_lr[hf] = bf2f(lar[i]); p_ai[hf] = bf2f(aim[i]); p_cr[hf] = bf2f(cre[i]); p_ci[hf] = bf2f(cim[i]);
  }
  const float v_ldt = bf2f(ldt[d]), v_dsk = bf2f(dsk[d]);
  __builtin_amdgcn_s_barrier();
  CBAR();
  WAITVM(0);
  sig(FL + F_TU);
  waitge(FL + F_TU, 4);
  TS(4);
  {
    const int f1 = (q4 >> 1) | ((g4 & 1) << 1);
#pragma unroll
    for (int j = 0; j < 4; ++j) {
      LAS char* pp = sm + L_UB + (32 * j + 8 * g4 + q4) * 128 + (((2 * c + (p4 >> 1)) ^ (f1 << 1)) << 4) + (p4 & 1) * 8;
      u32x4 v = tr_pair(pp, pp + 512);
      st16(v, rw, (16 * c + (lane & 15)) * 4096 + (32 * j + 8 * g4) * 2, WS_UT + cg * 64 * 4096 + tt16 * 256);
    }
  }
  sig(FL + F_TU);
  LAS char* ub = sm + L_UB + c * UB_CH;
  const int rr = lane & 15, rg = lane >> 4;
  unsigned kp[6];
  {
    const float dt = rbf(expf(v_ldt));
    float mx = __builtin_fmaxf(__builtin_fmaxf(__builtin_fabsf(p_cr[0]), __builtin_fabsf(p_ci[0])),
                               __builtin_fmaxf(__builtin_fabsf(p_cr[1]), __builtin_fabsf(p_ci[1])));
#pragma unroll
    for (int o = 32; o >= 1; o >>= 1) mx = __builtin_fmaxf(mx, __shfl_xor(mx, o));
    const int ex = (mx > 0.f && mx < 3.0e38f) ? __builtin_amdgcn_frexp_expf(mx) : 0;
    const float mult = __builtin_amdgcn_ldexpf(2.f * dt, ex);
    waitge(FL + F_TU, 8);
    TS(1); TC(4);
    const int wr_lo = (lane & 3) * 4, wr_s = lane >> 2;
#define SCR_W(row, val) *(LAS unsigned*)(ub + (row) * 256 + (((wr_s ^ (row)) << 4) | wr_lo)) = (val)
#define SCR_R(ks) *(LAS f16x8*)(ub + rr * 256 + (((4 * (ks) + rg) ^ rr) << 4))
    f32x4 t0 = {0, 0, 0, 0}, t1 = {0, 0, 0, 0}, t2 = {0, 0, 0, 0};
#pragma unroll 1
    for (int hf = 0; hf < 2; ++hf) {
      const float lr = hf ? p_lr[1] : p_lr[0], aiv = hf ? p_ai[1] : p_ai[0];
      const float cr = hf ? p_cr[1] : p_cr[0], ci = hf ? p_ci[1] : p_ci[0];
      const float ar = -rbf(expf(lr)) * dt;
      const float ai = aiv * dt;
      const float arl2 = ar * 1.4426950408889634f;
      const cplx z1 = zpow(arl2, ai, 1.f), z8 = zpow(arl2, ai, 8.f), z128 = zpow(arl2, ai, 128.f);
      f16x8 bfr[4];
      {
        SCR_W(0, pk_f16(1.f, 0.f));
        cplx e = z8;
        SCR_W(1, pk_f16(e.re, e.im));
#pragma unroll
        for (int k = 2; k < 16; ++k) { e = cmul(e, z8); SCR_W(k, pk_f16(e.re, e.im)); }
#pragma unroll
        for (int ks = 0; ks < 4; ++ks) bfr[ks] = SCR_R(ks);
      }
      cplx X[8], x16;
      {
        cplx xx = {__builtin_amdgcn_ldexpf(cr, -ex), __builtin_amdgcn_ldexpf(ci, -ex)};
#pragma unroll
        for (int r = 0; r < 16; ++r) {
          if (r < 8) X[r] = xx;
          SCR_W(r, pk_f16(xx.re, -xx.im));
          xx = cmul(xx, z128);
        }
        x16 = xx;
#pragma unroll
        for (int ks = 0; ks < 4; ++ks) { f16x8 a = SCR_R(ks); t0 = MFMA16H(a, bfr[ks], t0); }
      }
      cplx zp[8];
      zp[1] = z1;
#pragma unroll
      for (int k = 2; k < 8; ++k) zp[k] = cmul(zp[k - 1], z1);
      {
        SCR_W(0, pk_f16(x16.re, -x16.im));
#pragma unroll
        for (int k = 1; k < 8; ++k) {
          cplx a = cmul(X[0], zp[k]), b = cmul(X[1], zp[k]);
          SCR_W(k, pk_f16(a.re, -a.im));
          SCR_W(8 + k, pk_f16(b.re, -b.im));
        }
#pragma unroll
        for (int ks = 0; ks < 4; ++ks) { f16x8 a = SCR_R(ks); t1 = MFMA16H(a, bfr[ks], t1); }
      }
      {
#pragma unroll
        for (int k = 0; k < 4; ++k) { cplx a = cmul(X[4 + k], zp[4]); SCR_W(k, pk_f16(a.re, -a.im)); }
#pragma unroll
        for (int k = 0; k < 3; ++k) {
          cplx a = cmul(X[2], zp[2 * k + 2]), b = cmul(X[3], zp[2 * k + 2]);
          SCR_W(4 + k, pk_f16(a.re, -a.im));
          SCR_W(8 + k, pk_f16(b.re, -b.im));
        }
#pragma unroll
        for (int ks = 0; ks < 4; ++ks) { f16x8 a = SCR_R(ks); t2 = MFMA16H(a, bfr[ks], t2); }
      }
      if (hf == 0) {
        waitge(FL + F_FULL, 4);
        WAITVM(0);
        if (sig_ret(FL + F_TS) == 3u) gflag(ws, WS_FU + (cg * 16 + tt16) * 128, ldsword(FL + F_TAG), lane);
        TS(5);
      }
    }
#undef SCR_W
#undef SCR_R
    kp[0] = pk_bf16(t0[0] * mult + ((lane == 0) ? v_dsk : 0.f), t0[1] * mult);
    kp[1] = pk_bf16(t0[2] * mult, t0[3] * mult);
    kp[2] = pk_bf16(t1[0] * mult, t1[1] * mult);
    kp[3] = pk_bf16(t1[2] * mult, t1[3] * mult);
    kp[4] = pk_bf16(t2[0] * mult, t2[1] * mult);
    kp[5] = pk_bf16(t2[2] * mult, t2[3] * mult);
  }
  TS(2); TC(5);
  bf16x8 F[16];
  {
    LAS u16* K = (LAS u16*)ub + 16;
    const int DUMMY = 2052;
#pragma unroll
    for (int i = 0; i < 4; ++i) {
      const int row = 4 * rg + i;
      const unsigned v0 = kp[i >> 1], v1 = kp[2 + (i >> 1)], v2 = kp[4 + (i >> 1)];
      K[128 * row + 8 * rr] = (u16)((i & 1) ? (v0 >> 16) : v0);
      int p1 = (row >> 3) * 128 + (row & 7) + 8 * rr;
      if (i == 0) p1 = (lane == 0) ? 2048 : ((row & 7) == 0 ? DUMMY : p1);
      K[p1] = (u16)((i & 1) ? (v1 >> 16) : v1);
      int p2 = (rg == 0) ? (512 + 128 * i + 4 + 8 * rr) : (128 + 128 * rg + 2 * (i + 1) + 8 * rr);
      if (rg == 3 || (rg != 0 && i == 3)) p2 = DUMMY;
      K[p2] = (u16)((i & 1) ? (v2 >> 16) : v2);
    }
    if (lane < 8) *(LAS unsigned*)(ub + lane * 4) = 0u;
    const unsigned a1 = K[256 + 4 * lane], b1 = K[258 + 4 * lane], c1 = K[260 + 4 * lane];
    const unsigned a2 = K[512 + 8 * lane], b2 = K[516 + 8 * lane], c2 = K[520 + 8 * lane];
    const unsigned a3 = K[1024 + 16 * lane], b3 = K[1032 + 16 * lane], c3 = K[1040 + 16 * lane];
    u32x2 w1 = {a1 | (a1 << 16), b1 | (c1 << 16)};
    u32x4 w2 = {a2 | (a2 << 16), a2 | (b2 << 16), b2 | (b2 << 16), c2 | (c2 << 16)};
    u32x4 w3a = {a3 | (a3 << 16), a3 | (a3 << 16), a3 | (b3 << 16), b3 | (b3 << 16)};
    u32x4 w3b = {b3 | (b3 << 16), b3 | (b3 << 16), c3 | (c3 << 16), c3 | (c3 << 16)};
    *(LAS u32x2*)(K + 256 + 4 * lane) = w1;
    *(LAS u32x4*)(K + 512 + 8 * lane) = w2;
    *(LAS u32x4*)(K + 1024 + 16 * lane) = w3a;
    *(LAS u32x4*)(K + 1032 + 16 * lane) = w3b;
    CBAR();
    LAS char* fbp = ub + 2 * (16 - 12 + 32 * rg + 4 * (q4 + p4));
#pragma unroll
    for (int s = 0; s < 16; ++s) F[s] = tr_frag(fbp + 256 * s, fbp + 256 * s + 32);
    asm volatile("s_waitcnt lgkmcnt(0)" ::: "memory");
  }
  TS(3);
  const unsigned tag = ldsword(FL + F_TAG);
  gpoll(ws + WS_FU + ((cg * 16 + c16) << 7), tag);
  TS(6);
  {
    u32x4 ur[4];
#pragma unroll
    for (int i = 0; i < 4; ++i) ur[i] = ld16(rw, i * 1024 + lane * 16, WS_UT + d * 4096);
    *(LAS unsigned*)(ub + lane * 4) = 0u;
#pragma unroll
    for (int i = 0; i < 4; ++i) *(LAS u32x4*)(ub + 256 + i * 1024 + lane * 16) = ur[i];
  }
  CBAR();
  TS(8); TC(2);
  unsigned yp[16];
  {
    LAS char* gb = ub + 2 * (128 + 16 * p4 + 12 - 32 * rg - 4 * q4);
    float pc1 = 0.f, pc2 = 0.f, pc3 = 0.f, yprev = 0.f;
    const bool l0 = (lane == 0);
#define ROR1(v) __int_as_float(__builtin_amdgcn_update_dpp(0, __float_as_int(v), 0x13C, 0xf, 0xf, false))
#define FIN(i)                                                                       \
  do {                                                                               \
    float r3 = ROR1(ca[i][3]);                                                       \
    float s2 = ca[i][2] + (l0 ? pc3 : r3);                                           \
    float r2 = ROR1(s2);                                                             \
    float s1 = ca[i][1] + (l0 ? pc2 : r2);                                           \
    float r1 = ROR1(s1);                                                             \
    float y = ca[i][0] + (l0 ? pc1 : r1);                                            \
    pc1 = r1; pc2 = r2; pc3 = r3;                                                    \
    if ((i) & 1) yp[8 * pass + ((i) >> 1)] = pk_bf16(yprev, y); else yprev = y;      \
  } while (0)
#pragma unroll
    for (int pass = 0; pass < 2; ++pass) {
      f32x4 ca[16];
#pragma unroll
      for (int i = 0; i < 16; ++i) ca[i] = (f32x4){0, 0, 0, 0};
      bf16x8 G = tr_frag(gb, gb - 32);
#pragma unroll
      for (int v = 0; v < 16 * pass + 16; ++v) {
        bf16x8 Gn = G;
        if (v + 1 < 16 * pass + 16) Gn = tr_frag(gb + 128 * (v + 1), gb + 128 * (v + 1) - 32);
        SB();
#pragma unroll
        for (int i = 0; i < 16; ++i) {
          const int T = 16 * pass + i;
          if (T >= v && ((T - v) & 1) == 0) ca[i] = MFMA16(G, F[(T - v) >> 1], ca[i]);
        }
        if (v - 1 >= 16 * pass) FIN(v - 1 - 16 * pass);
        SB();
        G = Gn;
      }
      FIN(15);
      CBAR();
      SB();
    }
#undef FIN
#undef ROR1
  }
  CBAR();
  TS(9); TC(3);
#pragma unroll
  for (int i = 0; i < 16; ++i) {
    ((LAS u16*)ub)[128 * i + lane] = (u16)yp[i];
    ((LAS u16*)ub)[128 * i + 64 + lane] = (u16)(yp[i] >> 16);
  }
  CBAR();
#pragma unroll
  for (int i = 0; i < 4; ++i) st16(*(LAS u32x4*)(ub + i * 1024 + lane * 16), rw, i * 1024 + lane * 16, WS_YT + d * 4096);
  WAITVM(0);
  if (sig_ret(FL + F_CY) == 3u) {
    gflag(ws, WS_FY + (cg * 16 + tt16) * 128, tag, lane);
    TS(10);
    gpoll(ws + WS_FY + ((cg * 16 + (lane & 15)) << 7), tag);
    sig(FL + F_Y);
  } else {
    TS(10);
    waitge(FL + F_Y, 1);
  }
  TS(11);
  {
    int ln2 = lane;
    asm volatile("" : "+v"(ln2));
    const int lane = ln2, q4 = (lane & 15) >> 2, p4 = lane & 3, g4 = lane >> 4;
    LAS char* tb = sm + L_UB;
    u32x4 yr[4];
#pragma unroll
    for (int i = 0; i < 4; ++i)
      yr[i] = ld16(rw, (16 * c + 4 * i + g4) * 4096 + (lane & 15) * 16, WS_YT + cg * 64 * 4096 + tt16 * 256);
#pragma unroll
    for (int i = 0; i < 4; ++i) {
      const int row = 16 * c + 4 * i + g4;
      const int f = (row & 3) | (((row >> 3) & 1) << 2);
      *(LAS u32x4*)(tb + row * 256 + (((lane & 15) ^ (f << 1)) << 4)) = yr[i];
    }
    sig(FL + F_T2);
    waitge(FL + F_T2, 4);
    const int f = q4 | ((g4 & 1) << 2);
#pragma unroll
    for (int j = 0; j < 4; ++j) {
      const int tbk = 2 * c + (j >> 1), hf = j & 1;
      LAS char* pp = tb + (32 * hf + 8 * g4 + q4) * 256 + (((2 * tbk + (p4 >> 1)) ^ (f << 1)) << 4) + (p4 & 1) * 8;
      u32x4 v = tr_pair(pp, pp + 1024);
      ost16(out + (size_t)(tt16 * 128 + 16 * tbk + (lane & 15)) * 2048 + 1024 + cg * 64 + 32 * hf + 8 * g4, v);
    }
  }
  TS(12);
  TSDUMP();
}

extern "C" void kernel_launch(void* const* d_in, const int* in_sizes, int n_in, void* d_out, int out_size,
                              void* d_ws, size_t ws_size, hipStream_t stream) {
  fused_kernel<<<256, 768, 0, stream>>>((const u16*)d_in[0], (const u16*)d_in[1], (const u16*)d_in[2], (const u16*)d_in[3],
                                        (const u16*)d_in[4], (const u16*)d_in[5], (const u16*)d_in[6], (const u16*)d_in[7],
                                        (const u16*)d_in[8], (const u16*)d_in[9], (u16*)d_out, (char*)d_ws);
}
```

```cpp
#include <hip/hip_runtime.h>
#include <stdint.h>

#define LAS __attribute__((address_space(3)))
#define GAS __attribute__((address_space(1)))
#define DI __device__ __forceinline__

typedef unsigned short u16;
typedef short s16x4 __attribute__((ext_vector_type(4)));
typedef __bf16 bf16x2 __attribute__((ext_vector_type(2)));
typedef __bf16 bf16x8 __attribute__((ext_vector_type(8)));
typedef _Float16 f16x2 __attribute__((ext_vector_type(2)));
typedef _Float16 f16x8 __attribute__((ext_vector_type(8)));
typedef float f32x2 __attribute__((ext_vector_type(2)));
typedef float f32x4 __attribute__((ext_vector_type(4)));
typedef float f32x16 __attribute__((ext_vector_type(16)));
typedef unsigned u32x2 __attribute__((ext_vector_type(2)));
typedef unsigned u32x4 __attribute__((ext_vector_type(4)));
typedef __amdgpu_buffer_rsrc_t rsrc_t;

#define CBAR() asm volatile("" ::: "memory")
#define SB() __builtin_amdgcn_sched_barrier(0)
#define WAITVM(n) asm volatile("s_waitcnt vmcnt(" #n ")" ::: "memory")
#define WAITVL(n) asm volatile("s_waitcnt vmcnt(" #n ") lgkmcnt(0)" ::: "memory")
#define BAR() asm volatile("s_barrier" ::: "memory")
#define LBAR() do { CBAR(); __builtin_amdgcn_s_waitcnt(0xC07F); asm volatile("s_barrier" ::: "memory"); } while (0)
#define TSDECL
#define TS(i)
#define TC(i)
#define TSDUMP()
#define KOFF ((ml & 3) * 2 + 8 * (xcd & 1))
#define PRIO_C 2
#define PRIO_L 3
#define PSLEEP 3
#define NRING 12

DI void ost16(void* p, u32x4 v) { asm volatile("global_store_dwordx4 %0, %1, off sc1\n\ts_nop 1" :: "v"(p), "v"(v) : "memory"); }
DI float sum16(float x) {
  auto r = __builtin_amdgcn_permlane16_swap(__float_as_uint(x), __float_as_uint(x), false, false);
  return __uint_as_float(r[0]) + __uint_as_float(r[1]);
}
DI float sum32(float x) {
  auto r = __builtin_amdgcn_permlane32_swap(__float_as_uint(x), __float_as_uint(x), false, false);
  return __uint_as_float(r[0]) + __uint_as_float(r[1]);
}
DI float sum8(float x) {
  return x + __int_as_float(__builtin_amdgcn_update_dpp(0, __float_as_int(x), 0x128, 0xf, 0xf, false));
}
DI float wave_max(float x) {
#define DPPMAX(ctl) x = __builtin_fmaxf(x, __int_as_float(__builtin_amdgcn_update_dpp(0, __float_as_int(x), ctl, 0xf, 0xf, false)))
  DPPMAX(0xB1); DPPMAX(0x4E); DPPMAX(0x141); DPPMAX(0x140);
#undef DPPMAX
  auto r = __builtin_amdgcn_permlane16_swap(__float_as_uint(x), __float_as_uint(x), false, false);
  x = __builtin_fmaxf(__uint_as_float(r[0]), __uint_as_float(r[1]));
  r = __builtin_amdgcn_permlane32_swap(__float_as_uint(x), __float_as_uint(x), false, false);
  return __builtin_fmaxf(__uint_as_float(r[0]), __uint_as_float(r[1]));
}
DI int lane_id() {
  int l = __builtin_amdgcn_mbcnt_hi(~0u, __builtin_amdgcn_mbcnt_lo(~0u, 0u));
  asm volatile("" : "+v"(l));
  return l;
}
DI float bf2f(u16 v) { return __uint_as_float((unsigned)v << 16); }
DI unsigned pk_bf16(float a, float b) {
  f32x2 f = {a, b};
  return __builtin_bit_cast(unsigned, __builtin_convertvector(f, bf16x2));
}
DI unsigned pk_f16(float a, float b) {
  f32x2 f = {a, b};
  return __builtin_bit_cast(unsigned, __builtin_convertvector(f, f16x2));
}
DI float rbf(float a) { return __uint_as_float(pk_bf16(a, 0.f) << 16); }
DI u32x2 tr_read(LAS char* p) {
  s16x4 v = __builtin_amdgcn_ds_read_tr16_b64_v4i16((LAS s16x4*)p);
  return __builtin_bit_cast(u32x2, v);
}
DI u32x4 join(u32x2 a, u32x2 b) { u32x4 r = {a.x, a.y, b.x, b.y}; return r; }
DI u32x4 tr_pair(LAS char* p0, LAS char* p1) { return join(tr_read(p0), tr_read(p1)); }
DI bf16x8 asbf(u32x4 v) { return __builtin_bit_cast(bf16x8, v); }
DI bf16x8 tr_frag(LAS char* p0, LAS char* p1) { return asbf(tr_pair(p0, p1)); }
#define MFMA32(a, b, c) __builtin_amdgcn_mfma_f32_32x32x16_bf16((a), (b), (c), 0, 0, 0)
#define MFMA16(a, b, c) __builtin_amdgcn_mfma_f32_16x16x32_bf16((a), (b), (c), 0, 0, 0)
#define MFMA16H(a, b, c) __builtin_amdgcn_mfma_f32_16x16x32_f16((a), (b), (c), 0, 0, 0)
#define AMFMA16(c, a, b) asm volatile("v_mfma_f32_16x16x32_bf16 %0, %1, %2, %0" : "+v"(c) : "v"(a), "v"(b))
#define GLDS(src, dst) __builtin_amdgcn_global_load_lds((const GAS unsigned*)(src), (LAS unsigned*)(dst), 16, 0, 0)
#define SC1 16
DI u32x4 ld16(rsrc_t r, int voff, int soff) { return __builtin_amdgcn_raw_buffer_load_b128(r, voff, soff, SC1); }
DI void st16(u32x4 v, rsrc_t r, int voff, int soff) {
  __builtin_amdgcn_raw_buffer_store_b128(v, r, voff, soff, SC1);
  asm volatile("s_nop 1" :: "v"(v) : "memory");
}

constexpr int WS_EPOCH = 0;
constexpr int WS_FU = 65536, WS_FP = WS_FU + 32768, WS_FY = WS_FP + 32768, WS_FR = WS_FY + 32768;
constexpr int WS_PQ = 262144;
constexpr int WS_UT = WS_PQ + (4 << 20);
constexpr int WS_YT = WS_UT + (4 << 20);
constexpr int WS_KVP = WS_YT + (4 << 20);
constexpr int WS_ZP = WS_KVP + 32 * 16 * 4096;
constexpr int WS_KVR = WS_ZP + 32 * 16 * 128;
constexpr int WS_END = WS_KVR + 32 * 2048;

constexpr int SLOT = 40960;
constexpr int L_UB = 131072, UB_CH = 4352;
constexpr int L_X = L_UB + 4 * UB_CH;
constexpr int L_Z = L_X + 4 * 2176;
constexpr int L_FLAG = L_Z + 512;
constexpr int LDS_BYTES = L_FLAG + 256;
enum { F_XE = 8, F_EQ = 10, F_LW, F_CY, F_T2, F_XP, F_RD, F_PQ, F_LM = 20, F_Z = 24, F_Y = 26, F_TAG = 32 };

typedef f32x2 cplx;
DI cplx cmul(cplx a, cplx b) {
  cplx t, r;
  asm("v_pk_mul_f32 %0, %1, %2 op_sel:[1,1] op_sel_hi:[1,0] neg_lo:[1,0]" : "=v"(t) : "v"(a), "v"(b));
  asm("v_pk_fma_f32 %0, %1, %2, %3 op_sel:[0,0,0] op_sel_hi:[0,1,1]" : "=v"(r) : "v"(a), "v"(b), "v"(t));
  return r;
}
DI cplx zpow(float arl2, float ai, float p) {
  const float INV2PI = 0.15915494309189535f;
  const float INV2PI_LO = (float)(0.15915494309189533576888 - (double)0.15915494309189535f);
  float e = __builtin_amdgcn_exp2f(arl2 * p);
  float r = ai * p;
  float hi = r * INV2PI;
  float lo = __builtin_fmaf(r, INV2PI_LO, __builtin_fmaf(r, INV2PI, -hi));
  float f = (hi - __builtin_rintf(hi)) + lo;
  cplx z = {e * __builtin_amdgcn_cosf(f), e * __builtin_amdgcn_sinf(f)};
  return z;
}
DI float phi_f(float t) {
  float e = __builtin_amdgcn_fmed3f(__builtin_amdgcn_exp2f(t * 1.4426950408889634f), 0.f, 1.f);
  return (__builtin_amdgcn_fmed3f(t, 0.f, __builtin_inff()) + 1e-6f) + e;
}
DI u32x4 tag_put(f32x4 v, unsigned tag) {
  u32x4 b = __builtin_bit_cast(u32x4, v), r;
  r[0] = __builtin_amdgcn_perm(b[0], tag, 0x07060500u);
  r[1] = __builtin_amdgcn_perm(b[1], tag, 0x07060501u);
  r[2] = __builtin_amdgcn_perm(b[2], tag, 0x07060502u);
  r[3] = __builtin_amdgcn_perm(b[3], tag, 0x07060503u);
  return r;
}
DI unsigned tag_get(u32x4 b) {
  unsigned lo = __builtin_amdgcn_perm(b[1], b[0], 0x0c0c0400u), hi = __builtin_amdgcn_perm(b[3], b[2], 0x04000c0cu);
  return lo | hi;
}
DI f32x4 tag_val(u32x4 b) { return __builtin_bit_cast(f32x4, b & 0xffffff00u); }

DI void sig(LAS unsigned* f) {
  asm volatile("s_mov_b64 exec, 1\n\tds_add_u32 %0, %1\n\ts_mov_b64 exec, -1" :: "v"((unsigned)(size_t)f), "v"(1u) : "memory");
}
DI unsigned sig_ret(LAS unsigned* f) {
  unsigned r;
  asm volatile("s_mov_b64 exec, 1\n\tds_add_rtn_u32 %0, %1, %2\n\ts_mov_b64 exec, -1\n\ts_waitcnt lgkmcnt(0)"
               : "=&v"(r) : "v"((unsigned)(size_t)f), "v"(1u) : "memory");
  return __builtin_amdgcn_readfirstlane(r);
}
DI unsigned ldsword(LAS unsigned* f) { return __builtin_amdgcn_readfirstlane(*(volatile LAS unsigned*)f); }
DI void waitge(LAS unsigned* f, unsigned n) {
  CBAR();
#pragma unroll 1
  for (int it = 0; it < (1 << 22); ++it) {
    if (ldsword(f) >= n) break;
    __builtin_amdgcn_s_sleep(1);
  }
  CBAR();
}
DI void gflag(char* ws, int off, unsigned tag, int lane) {
  CBAR();
  if (lane == 0) __hip_atomic_store((unsigned*)(ws + off), tag, __ATOMIC_RELAXED, __HIP_MEMORY_SCOPE_AGENT);
  CBAR();
}
DI void gpoll(char* p, unsigned tag) {
  CBAR();
#pragma unroll 1
  for (int it = 0; it < (1 << 18); ++it) {
    unsigned v = __hip_atomic_load((unsigned*)p, __ATOMIC_RELAXED, __HIP_MEMORY_SCOPE_AGENT);
    if (__all(v == tag)) break;
    __builtin_amdgcn_s_sleep(PSLEEP);
  }
  CBAR();
}

__global__ __launch_bounds__(768, 3) void fused_kernel(const u16* __restrict__ x, const u16* __restrict__ wqkv,
                                                       const u16* __restrict__ wo, const u16* __restrict__ u,
                                                       const u16* __restrict__ lar, const u16* __restrict__ aim,
                                                       const u16* __restrict__ cre, const u16* __restrict__ cim,
                                                       const u16* __restrict__ ldt, const u16* __restrict__ dsk,
                                                       u16* __restrict__ out, char* __restrict__ ws) {
  __shared__ __attribute__((aligned(16))) char smem[LDS_BYTES];
  LAS char* sm = (LAS char*)smem;
  LAS unsigned* FL = (LAS unsigned*)(sm + L_FLAG);
  const int tid = threadIdx.x, lane = tid & 63;
  const int wave = __builtin_amdgcn_readfirstlane(tid >> 6);
  const int bid = blockIdx.x, xcd = bid & 7, idx = bid >> 3;
  const int ml = idx & 7, hl = idx >> 3;
  const int mb = (xcd & 1) * 8 + ml, hp = (xcd >> 1) * 4 + hl;
  const int mb2 = (xcd & 3) * 4 + (idx & 3), nb = (xcd >> 2) * 8 + (idx >> 2);
  const int rot = (idx >> 2) * 4;
  const rsrc_t rw = __builtin_amdgcn_make_buffer_rsrc((void*)ws, 0, WS_END, 0x00020000);
  TSDECL; TS(0);
  const int q4 = (lane & 15) >> 2, p4 = lane & 3, g4 = lane >> 4, c16 = lane & 15;

  if ((wave >> 2) == 1) {
    __builtin_amdgcn_s_setprio(PRIO_L);
    const int hw = wave - 4;
    unsigned ep = 0;
    if (hw == 0) ep = __hip_atomic_load((unsigned*)(ws + WS_EPOCH), __ATOMIC_RELAXED, __HIP_MEMORY_SCOPE_AGENT);
    const int koff = KOFF;
    const unsigned vx0 = (unsigned)((mb * 128 + hw * 32 + (lane >> 3)) * 2048) + (((lane & 7) ^ (lane >> 4)) << 4), vx1 = vx0 ^ 64;
    const unsigned vw0 = (unsigned)((lane >> 3) * 6144 + hp * 128 + (((lane & 7) ^ (((lane >> 4) & 1) << 1)) << 4)), vw1 = vw0 ^ 64;
#define ISSUE(tt_, slot_)                                                                                  \
  do {                                                                                                     \
    const int k_ = ((tt_) + koff) & 15;                                                                    \
    const char* xa_ = (const char*)x + k_ * 128;                                                           \
    const char* wa_ = (const char*)wqkv + (size_t)k_ * 393216;                                             \
    LAS char* sb_ = sm + (slot_) * SLOT;                                                                   \
    _Pragma("unroll") for (int i = 0; i < 4; ++i)                                                          \
        GLDS(xa_ + i * 16384 + ((i & 1) ? vx1 : vx0), sb_ + (hw * 4 + i) * 1024);                          \
    _Pragma("unroll") for (int i = 0; i < 6; ++i) {                                                        \
      const int qq_ = hw * 6 + i;                                                                          \
      GLDS(wa_ + ((qq_ & 7) * 49152 + (qq_ >> 3) * 2048) + ((i & 1) ? vw1 : vw0), sb_ + 16384 + qq_ * 1024); \
    }                                                                                                      \
  } while (0)
    ISSUE(0, 0);
    TS(13);
    ISSUE(1, 1);
    ISSUE(2, 2);
    TS(14);
    WAITVM(20);
    if (hw == 0) {
      const unsigned tg = __builtin_amdgcn_readfirstlane(ep) * 2654435761u + 0x9E3779B1u;
      *(volatile LAS unsigned*)(FL + F_TAG) = tg;
    }
    TS(1);
    BAR();
    {
      int sf = 0;
#pragma unroll 1
      for (int t = 0; t < 13; ++t) {
        WAITVL(10);
        BAR();
        ISSUE(t + 3, sf);
        sf = (sf == 2) ? 0 : sf + 1;
      }
    }
#undef ISSUE
    TS(2);
    {
      const char* src = (const char*)wo + (size_t)((8 * hw + (lane >> 3)) * 2048 + nb * 128 + (((lane & 7) ^ ((lane >> 4) << 1)) << 4));
#define WOP(s_) GLDS(src + (size_t)((((s_) + rot) & 31) * 65536), sm + ((s_) * 4 + hw) * 1024)
      WAITVM(10);
      BAR();
#pragma unroll
      for (int i = 10; i < 20; ++i) WOP(i);
      WAITVM(10);
      BAR();
#pragma unroll
      for (int i = 20; i < 32; ++i) WOP(i);
      BAR();
#pragma unroll
      for (int i = 0; i < 10; ++i) WOP(i);
#undef WOP
      TS(3);
      WAITVM(0);
      sig(FL + F_LW);
    }
    TS(4);
    const unsigned tag = ldsword(FL + F_TAG);
    waitge(FL + F_XP, 4);
    if ((idx & 3) == 0) {
      const int ri = xcd * 8 + (idx >> 2), rh = ri >> 1, td = ri & 1;
      const int ln = hw * 16 + c16;
      const int so = WS_KVP + rh * 65536 + td * 2048;
      const int vo = g4 * 16384 + ln * 16;
      u32x4 a[8];
#pragma unroll 1
      for (int it = 0; it < (1 << 17); ++it) {
        CBAR();
#pragma unroll
        for (int i = 0; i < 8; ++i) a[i] = ld16(rw, vo + (i >> 1) * 4096 + (i & 1) * 1024, so);
        unsigned bad = 0;
#pragma unroll
        for (int i = 0; i < 8; ++i) bad |= tag_get(a[i]) ^ tag;
        if (__all(bad == 0u)) break;
        __builtin_amdgcn_s_sleep(PSLEEP);
      }
      TS(5);
      f32x4 s0 = (tag_val(a[0]) + tag_val(a[2])) + (tag_val(a[4]) + tag_val(a[6]));
      f32x4 s1 = (tag_val(a[1]) + tag_val(a[3])) + (tag_val(a[5]) + tag_val(a[7]));
#pragma unroll
      for (int i = 0; i < 4; ++i) { s0[i] = sum32(sum16(s0[i])); s1[i] = sum32(sum16(s1[i])); }
      u32x4 w = {pk_bf16(s0[0], s0[1]), pk_bf16(s0[2], s0[3]), pk_bf16(s1[0], s1[1]), pk_bf16(s1[2], s1[3])};
      if (lane < 16) st16(w, rw, ln * 16, WS_KVR + ri * 1024);
      WAITVM(0);
      if (sig_ret(FL + F_RD) == 3u) gflag(ws, WS_FR + ri * 128, tag, lane);
      TS(6);
    }
    {
      const int hsw = (q4 >> 1) | ((g4 & 1) << 1);
      LAS char* wb = sm + hw * 4096;
      const int w0 = (4 * g4 + q4) * 128 + (((p4 >> 1) ^ (hsw << 1)) << 4) + (p4 & 1) * 8;
      const int bq = (c16 >> 2) & 1;
      const int m0 = bq * 1024 + g4 * 256 + (4 * ((c16 >> 3) ^ bq) + p4) * 16;
      bf16x8 wf[4][4];
      waitge(FL + F_LW, 4);
#define RWF(i_) _Pragma("unroll") for (int nt = 0; nt < 4; ++nt) {                                  \
        LAS char* pp = wb + (i_) * 16384 + (w0 ^ (nt << 5)); wf[(i_) & 3][nt] = tr_frag(pp, pp + 2048); }
#pragma unroll
      for (int i = 0; i < 4; ++i) RWF(i);
      gpoll(ws + WS_FR + ((((hw + 4 * ((lane >> 1) & 7) + rot) & 31) * 2 + (lane & 1)) << 7), tag);
      TS(7);
      u32x4 kf[8][2];
#pragma unroll
      for (int i = 0; i < 8; ++i)
#pragma unroll
        for (int td = 0; td < 2; ++td) kf[i][td] = ld16(rw, td * 1024 + lane * 16, WS_KVR + ((hw + 4 * i + rot) & 31) * 2048);
#pragma unroll
      for (int i = 0; i < 8; ++i) {
#pragma unroll
        for (int nt = 0; nt < 4; ++nt) {
          const f32x4 z4 = {0.f, 0.f, 0.f, 0.f};
          f32x4 d0 = MFMA16(asbf(kf[i][0]), wf[i & 3][nt], z4);
          f32x4 d1 = MFMA16(asbf(kf[i][1]), wf[i & 3][nt], z4);
          u32x4 mw = {pk_bf16(d0[0], d0[1]), pk_bf16(d0[2], d0[3]), pk_bf16(d1[0], d1[1]), pk_bf16(d1[2], d1[3])};
          *(LAS u32x4*)(wb + i * 16384 + (nt >> 1) * 2048 + (nt & 1) * 128 + m0) = mw;
        }
        if (i & 1) sig(FL + F_LM + (i >> 1));
        if (i < 4) RWF(i + 4);
      }
#undef RWF
    }
    TS(9);
    TSDUMP();
    return;
  }

  const int c = wave & 3, d = bid * 4 + c;
  LAS char* ub = sm + L_UB + c * UB_CH;
  LAS char* kc = (c < 2) ? sm + 3 * SLOT + c * 4096 : sm + L_X + (c - 2) * 4096;
  f32x4 ta = {0, 0, 0, 0}, tb = {0, 0, 0, 0}, tc_ = {0, 0, 0, 0};
  {
    const int pi = d * 128 + (wave < 4 ? 64 : 0) + lane;
    const float lr = bf2f(lar[pi]), aiv = bf2f(aim[pi]), cr = bf2f(cre[pi]), ci = bf2f(cim[pi]);
    const float dt = rbf(expf(bf2f(ldt[d])));
    LAS char* ks = (wave < 4) ? kc : ub;
    if (wave == 0 && lane < 32) FL[lane] = 0u;
    const float mx = wave_max(__builtin_fmaxf(__builtin_fabsf(cr), __builtin_fabsf(ci)));
    const int ex = (mx > 0.f && mx < 3.0e38f) ? __builtin_amdgcn_frexp_expf(mx) : 0;
    const float mult = __builtin_amdgcn_ldexpf(2.f * dt, ex);
    TS(15);
    const float ar = -rbf(expf(lr)) * dt;
    const float ai = aiv * dt;
    const float arl2 = ar * 1.4426950408889634f;
    const cplx z1 = zpow(arl2, ai, 1.f), z8 = zpow(arl2, ai, 8.f), z128 = zpow(arl2, ai, 128.f);
    LAS char* wp = ks + lane * 64 + (((lane >> 1) & 3) << 4);
    const int h0 = g4 & 1;
    LAS char* rp0 = ks + (g4 * 256 + h0 * 128 + (q4 >> 1) * 64 + (((2 * (q4 & 1) + (p4 >> 1)) ^ ((2 * g4 + h0) & 3)) << 4) + (p4 & 1) * 8);
    LAS char* rp1 = ks + ((unsigned)(rp0 - ks) ^ 144u);
#define KW(v, sg)                                                                                                    \
  _Pragma("unroll") for (int hh = 0; hh < 2; ++hh) {                                                                 \
    u32x4 wr_, wi_;                                                                                                  \
    _Pragma("unroll") for (int i = 0; i < 4; ++i) {                                                                  \
      wr_[i] = pk_f16(v[8 * hh + 2 * i][0], v[8 * hh + 2 * i + 1][0]);                                               \
      wi_[i] = pk_f16(sg v[8 * hh + 2 * i][1], sg v[8 * hh + 2 * i + 1][1]);                                         \
    }                                                                                                                \
    *(LAS u32x4*)(ks + ((unsigned)(wp - ks) ^ (unsigned)(hh << 4))) = wr_;                                           \
    *(LAS u32x4*)(ks + ((unsigned)(wp - ks) ^ (unsigned)((2 + hh) << 4))) = wi_;                                     \
  }
#define KR(ks_) __builtin_bit_cast(f16x8, tr_pair(rp0 + (ks_) * 1024, rp1 + (ks_) * 1024))
    f16x8 bfr[4];
    cplx v[16], X[17], zp[9];
    v[0] = (cplx){1.f, 0.f};
    v[1] = z8;
    v[2] = cmul(z8, z8);
#pragma unroll
    for (int k = 3; k < 16; ++k) v[k] = cmul(v[k - 2], v[2]);
    KW(v, -);
    X[0] = (cplx){__builtin_amdgcn_ldexpf(cr, -ex), __builtin_amdgcn_ldexpf(ci, -ex)};
    X[1] = cmul(X[0], z128);
    {
      const cplx z256 = cmul(z128, z128);
#pragma unroll
      for (int r = 2; r < 17; ++r) X[r] = cmul(X[r - 2], z256);
    }
    zp[1] = z1; zp[8] = z8;
    zp[2] = cmul(z1, z1); zp[3] = cmul(zp[2], z1); zp[4] = cmul(zp[2], zp[2]);
    zp[5] = cmul(zp[4], z1); zp[6] = cmul(zp[4], zp[2]); zp[7] = cmul(zp[4], zp[3]);
#pragma unroll
    for (int k = 0; k < 4; ++k) bfr[k] = KR(k);
#pragma unroll
    for (int g = 0; g < 4; ++g) { v[4 * g] = X[4 + g]; v[4 * g + 1] = cmul(X[4 + g], zp[4]); v[4 * g + 2] = X[8 + g]; v[4 * g + 3] = X[12 + g]; }
    KW(v, );
#pragma unroll
    for (int j = 0; j < 16; ++j) v[j] = (j & 7) ? cmul(X[j >> 3], zp[j & 7]) : X[j >> 3];
    {
      f16x8 a[4];
#pragma unroll
      for (int k = 0; k < 4; ++k) a[k] = KR(k);
      KW(v, );
#pragma unroll
      for (int k = 0; k < 4; ++k) ta = MFMA16H(a[k], bfr[k], ta);
    }
#pragma unroll
    for (int a = 0; a < 2; ++a) {
      v[8 * a] = X[2 + a];
      v[8 * a + 1] = cmul(X[2 + a], zp[2]);
      v[8 * a + 2] = v[8 * a + 4] = cmul(X[2 + a], zp[4]);
      v[8 * a + 5] = cmul(X[2 + a], zp[6]);
      v[8 * a + 6] = cmul(X[2 + a], zp[8]);
      v[8 * a + 3] = v[8 * a + 7] = X[16];
    }
    {
      f16x8 a[4];
#pragma unroll
      for (int k = 0; k < 4; ++k) a[k] = KR(k);
      KW(v, );
#pragma unroll
      for (int k = 0; k < 4; ++k) tc_ = MFMA16H(a[k], bfr[k], tc_);
    }
#pragma unroll
    for (int k = 0; k < 4; ++k) { f16x8 a = KR(k); tb = MFMA16H(a, bfr[k], tb); }
#undef KW
#undef KR
    ta *= mult; tb *= mult; tc_ *= mult;
    TS(2);
  }

  if (wave < 4) {
    *(LAS f32x4*)(kc + lane * 16) = ta;
    *(LAS f32x4*)(kc + 1024 + lane * 16) = tb;
    *(LAS f32x4*)(kc + 2048 + lane * 16) = tc_;
    __builtin_amdgcn_s_setprio(PRIO_C);
    const int mh = wave & 1, hd = wave >> 1;
    const int a0 = (mh * 64 + c16) * 128 + ((g4 ^ (c16 >> 1)) << 4);
    const int b0 = 16384 + (8 * g4 + q4) * 128 + (((hd * 4 + (p4 >> 1)) ^ (((q4 >> 1) | ((g4 & 1) << 1)) << 1)) << 4) + (p4 & 1) * 8;
    f32x4 acc[4][6];
#pragma unroll
    for (int m = 0; m < 4; ++m)
#pragma unroll
      for (int g = 0; g < 6; ++g) acc[m][g] = (f32x4){0.f, 0.f, 0.f, 0.f};
#pragma unroll
    for (int m = 0; m < 4; ++m)
      asm volatile("" : "+v"(acc[m][0]), "+v"(acc[m][1]), "+v"(acc[m][2]), "+v"(acc[m][3]), "+v"(acc[m][4]), "+v"(acc[m][5]));
    bf16x8 fa[2][4];
    u32x2 bl[6], bh[6];
#define RA(set, m, pa) fa[set][m] = *(LAS bf16x8*)((pa) + (m) * 2048)
#define RL(j, pe, po) bl[(j) % 6] = tr_read((((j) & 1) ? (po) : (pe)) + (((j) % 6) >> 1) * 8192 + ((j) / 6) * 4096)
#define RH(j, pe, po) bh[(j) % 6] = tr_read((((j) & 1) ? (po) : (pe)) + (((j) % 6) >> 1) * 8192 + ((j) / 6) * 4096 + 512)
#define FB(j) asbf(join(bl[(j) % 6], bh[(j) % 6]))
#define MM(j, m) acc[m][(j) % 6] = ((j) % 6 < 2) ? MFMA16(FB(j), fa[(j) / 6][m], acc[m][(j) % 6]) : MFMA16(fa[(j) / 6][m], FB(j), acc[m][(j) % 6])
#define GRP(j, X0, X1, X2, X3)                \
  do {                                        \
    MM(j, 0); X0; SB(); MM(j, 1); X1; SB();   \
    MM(j, 2); X2; SB(); MM(j, 3); X3; SB();   \
  } while (0)
    LAS char* pa = sm + a0;
    LAS char* pa1 = sm + (a0 ^ 64);
    LAS char* pe = sm + b0;
    LAS char* po = sm + (b0 ^ 32);
    LBAR();
    TS(3); TC(0);
    RA(0, 0, pa); RL(0, pe, po); RH(0, pe, po); RA(0, 1, pa); RA(0, 2, pa); RA(0, 3, pa);
    RL(1, pe, po); RH(1, pe, po); RL(2, pe, po); RH(2, pe, po);
    {
      int slot = 0;
#pragma unroll 1
      for (int t = 0; t < 16; ++t) {
        const int dl = (slot == 2) ? -2 * SLOT : SLOT;
        slot = (slot == 2) ? 0 : slot + 1;
        SB();
        GRP(0, RL(3, pe, po), RH(3, pe, po), , );
        GRP(1, RL(4, pe, po), RH(4, pe, po), , );
        GRP(2, RL(5, pe, po), RH(5, pe, po), RA(1, 0, pa1), RA(1, 1, pa1));
        GRP(3, RL(6, pe, po), RH(6, pe, po), RA(1, 2, pa1), RA(1, 3, pa1));
        GRP(4, RL(7, pe, po), RH(7, pe, po), RL(8, pe, po), RH(8, pe, po));
        GRP(5, RL(9, pe, po), RH(9, pe, po), RL(10, pe, po), RH(10, pe, po));
        GRP(6, RL(11, pe, po), RH(11, pe, po), , );
        GRP(7, , , , );
        GRP(8, pa += dl, pa1 += dl, pe += dl, po += dl);
        asm volatile("" : "+v"(pa), "+v"(pa1), "+v"(pe), "+v"(po));
        LBAR();
        SB();
        GRP(9, RA(0, 0, pa), RL(0, pe, po), RH(0, pe, po), RA(0, 1, pa));
        GRP(10, RA(0, 2, pa), RA(0, 3, pa), RL(1, pe, po), RH(1, pe, po));
        GRP(11, RL(2, pe, po), RH(2, pe, po), , );
      }
    }
#undef RA
#undef RL
#undef RH
#undef FB
#undef MM
#undef GRP
    TS(4); TC(1);
    {
    const int lane = lane_id(), g4 = lane >> 4, c16 = lane & 15;
    const unsigned tag = ldsword(FL + F_TAG);
    const int head = hp * 2 + hd;
    {
      f32x4 kvt[2][2], zt[2];
#pragma unroll
      for (int i = 0; i < 2; ++i) { kvt[i][0] = kvt[i][1] = zt[i] = (f32x4){0.f, 0.f, 0.f, 0.f}; }
      const u32x4 ones = {0x3F803F80u, 0x3F803F80u, 0x3F803F80u, 0x3F803F80u};
#pragma unroll
      for (int j = 0; j < 2; ++j) {
        u32x4 fk[2], fv[2];
#pragma unroll
        for (int n = 0; n < 2; ++n)
#pragma unroll
          for (int i = 0; i < 4; ++i) {
            const f32x4 kk = acc[2 * j + (i >> 1)][2 + n], vv = acc[2 * j + (i >> 1)][4 + n];
            fk[n][i] = pk_bf16(phi_f(kk[2 * (i & 1)]), phi_f(kk[2 * (i & 1) + 1]));
            fv[n][i] = pk_bf16(vv[2 * (i & 1)], vv[2 * (i & 1) + 1]);
          }
#pragma unroll
        for (int td = 0; td < 2; ++td) {
          kvt[0][td] = MFMA16(asbf(fv[0]), asbf(fk[td]), kvt[0][td]);
          kvt[1][td] = MFMA16(asbf(fv[1]), asbf(fk[td]), kvt[1][td]);
          zt[td] = MFMA16(asbf(ones), asbf(fk[td]), zt[td]);
        }
      }
      LAS char* xo = sm + L_X + wave * 2176;
      LAS char* xi = sm + L_X + (wave ^ 1) * 2176;
      f32x4 keep[2];
#pragma unroll
      for (int td = 0; td < 2; ++td) {
        f32x4 ka = kvt[0][td], kb = kvt[1][td];
        asm("" : "+v"(ka), "+v"(kb));
        *(LAS f32x4*)(xo + td * 1024 + lane * 16) = mh ? ka : kb;
        keep[td] = mh ? kb : ka;
      }
      *(LAS float*)(xo + 2048 + (lane & 31) * 4) = (g4 & 1) ? zt[1][0] : zt[0][0];
      sig(FL + F_XE + hd);
      waitge(FL + F_XE + hd, 2);
      if (mh == 0) {
        f32x4 zz = *(LAS f32x4*)(xo + 2048 + (lane & 7) * 16) + *(LAS f32x4*)(xi + 2048 + (lane & 7) * 16);
        if (lane < 8) st16(tag_put(zz, tag), rw, lane * 16, WS_ZP + (head * 16 + mb) * 128);
      }
      const int so = WS_KVP + (head * 16 + mb) * 4096 + mh * 1024;
#pragma unroll
      for (int td = 0; td < 2; ++td) {
        f32x4 o = *(LAS f32x4*)(xi + td * 1024 + lane * 16) + keep[td];
        st16(tag_put(o, tag), rw, td * 2048 + lane * 16, so);
      }
      sig(FL + F_XP);
    }
    TS(5);
#pragma unroll
    for (int m = 0; m < 4; ++m)
#pragma unroll
      for (int i = 0; i < 8; ++i) acc[m][i >> 2][i & 3] = phi_f(acc[m][i >> 2][i & 3]);
    asm volatile("" : "+v"(acc[0][0]), "+v"(acc[1][0]), "+v"(acc[2][0]), "+v"(acc[3][0]),
                      "+v"(acc[0][1]), "+v"(acc[1][1]), "+v"(acc[2][1]), "+v"(acc[3][1]));
    {
      LAS f32x4* zl = (LAS f32x4*)(sm + L_Z + hd * 128);
      if (mh == 1) {
        u32x4 za, zb;
#pragma unroll 1
        for (int it = 0; it < (1 << 17); ++it) {
          CBAR();
          za = ld16(rw, lane * 16, WS_ZP + head * 2048);
          zb = ld16(rw, 1024 + lane * 16, WS_ZP + head * 2048);
          if (__all(((tag_get(za) ^ tag) | (tag_get(zb) ^ tag)) == 0u)) break;
          __builtin_amdgcn_s_sleep(PSLEEP);
        }
        f32x4 z = tag_val(za) + tag_val(zb);
#pragma unroll
        for (int i = 0; i < 4; ++i) z[i] = sum32(sum16(sum8(z[i])));
        if (lane < 8) zl[lane] = z;
        sig(FL + F_Z + hd);
      } else {
        waitge(FL + F_Z + hd, 1);
      }
      TS(6);
      const f32x4 z0 = zl[g4], z1 = zl[4 + g4];
#pragma unroll
      for (int m = 0; m < 4; ++m) {
        float d0 = acc[m][0][0] * z0[0], d1 = acc[m][1][0] * z1[0];
#pragma unroll
        for (int i = 1; i < 4; ++i) { d0 = __builtin_fmaf(acc[m][0][i], z0[i], d0); d1 = __builtin_fmaf(acc[m][1][i], z1[i], d1); }
        const float den = sum32(sum16(d0 + d1));
        const float rc = __builtin_amdgcn_rcpf(den + 1e-6f);
        u32x4 fq = {pk_bf16(acc[m][0][0] * rc, acc[m][0][1] * rc), pk_bf16(acc[m][0][2] * rc, acc[m][0][3] * rc),
                    pk_bf16(acc[m][1][0] * rc, acc[m][1][1] * rc), pk_bf16(acc[m][1][2] * rc, acc[m][1][3] * rc)};
        st16(fq, rw, (m & 1) * 1024 + lane * 16, WS_PQ + ((mb * 4 + mh * 2 + (m >> 1)) * 32 + head) * 2048);
      }
    }
    TS(7);
    WAITVM(0);
    if (sig_ret(FL + F_EQ) == 3u) {
      gflag(ws, WS_FP + (mb * 16 + hp) * 128, tag, lane);
      TS(8);
      gpoll(ws + WS_FP + ((mb2 * 16 + c16) << 7), tag);
      sig(FL + F_PQ);
    } else {
      TS(8);
      waitge(FL + F_PQ, 1);
    }
    TS(9);
    {
      const int pqs = WS_PQ + (mb2 * 4 + wave) * 65536;
      u32x4 pb[NRING][2];
#define LDQ(i_) do { const int so_ = pqs + ((((i_) + rot) & 31) << 11);                              \
        pb[(i_) % NRING][0] = ld16(rw, lane * 16, so_); pb[(i_) % NRING][1] = ld16(rw, 1024 + lane * 16, so_); } while (0)
#pragma unroll
      for (int i = 0; i < NRING; ++i) LDQ(i);
      f32x4 oc[2][4];
#pragma unroll
      for (int s = 0; s < 2; ++s)
#pragma unroll
        for (int t = 0; t < 4; ++t) oc[s][t] = (f32x4){0.f, 0.f, 0.f, 0.f};
      asm volatile("" : "+v"(oc[0][0]), "+v"(oc[0][1]), "+v"(oc[0][2]), "+v"(oc[0][3]),
                   "+v"(oc[1][0]), "+v"(oc[1][1]), "+v"(oc[1][2]), "+v"(oc[1][3]));
      LAS char* mp0 = sm + lane * 16;
      LAS char* mp1 = sm + (lane ^ 4) * 16;
      bf16x8 ma[4];
      waitge(FL + F_LM, 4);
      if (bid == 0 && wave == 0) gflag(ws, WS_EPOCH, tag, lane);
      TS(10); TC(2);
#pragma unroll
      for (int t = 0; t < 4; ++t) ma[t] = *(LAS bf16x8*)(((t & 1) ? mp1 : mp0) + t * 1024);
#pragma unroll
      for (int i = 0; i < 32; ++i) {
        if ((i & 7) == 7 && i < 31) waitge(FL + F_LM + (i >> 3) + 1, 4);
        SB();
        const u32x4 b0_ = pb[i % NRING][0], b1_ = pb[i % NRING][1];
#pragma unroll
        for (int t = 0; t < 4; ++t) {
          AMFMA16(oc[0][t], ma[t], b0_);
          AMFMA16(oc[1][t], ma[t], b1_);
          if (i < 31) ma[t] = *(LAS bf16x8*)(((t & 1) ? mp1 : mp0) + (i + 1) * 4096 + t * 1024);
          SB();
        }
        if (i + NRING < 32) LDQ(i + NRING);
      }
#undef LDQ
      asm volatile("s_nop 7\n\ts_nop 3" : "+v"(oc[0][0]), "+v"(oc[0][1]), "+v"(oc[0][2]), "+v"(oc[0][3]),
                   "+v"(oc[1][0]), "+v"(oc[1][1]), "+v"(oc[1][2]), "+v"(oc[1][3]));
      TS(11); TC(3);
#pragma unroll
      for (int s = 0; s < 2; ++s)
#pragma unroll
        for (int j = 0; j < 2; ++j) {
          u32x4 w = {pk_bf16(oc[s][2 * j][0], oc[s][2 * j][1]), pk_bf16(oc[s][2 * j][2], oc[s][2 * j][3]),
                     pk_bf16(oc[s][2 * j + 1][0], oc[s][2 * j + 1][1]), pk_bf16(oc[s][2 * j + 1][2], oc[s][2 * j + 1][3])};
          ost16(out + (size_t)((mb2 * 4 + wave) * 32 + s * 16 + c16) * 2048 + nb * 64 + 32 * j + 8 * g4, w);
        }
    }
    TS(12);
    TSDUMP();
    }
    return;
  }

  const int cg = bid >> 4, tt16 = bid & 15;
  const int rg = lane >> 4;
  const float v_dsk = bf2f(dsk[d]);
  BAR();
  bf16x8 F[16];
  unsigned tag, dm = 0, fu;
  {
    {
      const f32x4 oa = *(LAS f32x4*)(kc + lane * 16), ob = *(LAS f32x4*)(kc + 1024 + lane * 16), oc = *(LAS f32x4*)(kc + 2048 + lane * 16);
#pragma unroll
      for (int i = 0; i < 4; ++i) {
        float x0 = ta[i] + oa[i], x1 = tb[i] + ob[i], x2 = tc_[i] + oc[i];
        asm("" : "+v"(x0)); asm("" : "+v"(x1)); asm("" : "+v"(x2));
        ta[i] = x0; tb[i] = x1; tc_[i] = x2;
      }
      CBAR();
    }
#pragma unroll
    for (int i = 0; i < 4; ++i) {
      const int f1 = ((lane >> 4) & 1) | ((i & 1) << 1);
      GLDS((const char*)u + (size_t)((tt16 * 128 + c * 32 + i * 8 + (lane >> 3)) * 2048 + cg * 128 + (((lane & 7) ^ (f1 << 1)) << 4)),
           kc + i * 1024);
    }
#define ROL1(v) (unsigned)__builtin_amdgcn_update_dpp(0, (int)(v), 0x134, 0xf, 0xf, false)
#define MIX(lo, hi) __builtin_amdgcn_perm((hi), (lo), 0x07060100u)
    const bool l0 = (lane == 0);
    const u32x2 w0 = {pk_bf16(tc_[0] + (l0 ? v_dsk : 0.f), tc_[1]), pk_bf16(tc_[2], tc_[3])};
    const u32x2 w1 = {pk_bf16(tb[0], tb[0]), pk_bf16(tb[1], tb[2])};
    const unsigned pa0 = pk_bf16(ta[0], ta[0]), pa1 = pk_bf16(ta[1], ta[1]), pa2 = pk_bf16(ta[2], ta[2]), pa3 = pk_bf16(ta[3], ta[3]);
    const unsigned pe = pk_bf16(tb[3], tb[3]);
    const unsigned n0 = ROL1(l0 ? pa2 : pa0), n2 = ROL1(l0 ? pa3 : pa2), n3 = ROL1(l0 ? pe : pa3);
    const u32x4 w2 = {pa0, MIX(pa0, pa1), pa1, n0};
    const u32x4 w3 = {pa2, pa2, (lane & 1) ? n2 : MIX(pa2, n2), n2};
    const u32x4 w4 = {pa3, pa3, (lane & 1) ? n3 : MIX(pa3, n3), n3};
#undef ROL1
#undef MIX
    LAS char* K = ub + 32;
    const int k01 = 2 * (128 * (rg >> 1) + 8 * c16 + 4 * (rg & 1));
    BAR();
    *(LAS u32x2*)(K + k01) = w0;
    *(LAS u32x2*)(K + 512 + k01) = w1;
    tag = ldsword(FL + F_TAG);
    BAR();
    *(LAS u32x4*)(K + 1024 + lane * 16) = w2;
    BAR();
    *(LAS u32x4*)(K + 2048 + lane * 16) = w3;
    BAR();
    *(LAS u32x4*)(K + 3072 + lane * 16) = w4;
    WAITVM(0);
    BAR();
    TS(4);
    if (lane < 8) *(LAS unsigned*)(ub + lane * 4) = 0u;
    {
      const int f1 = (q4 >> 1) | ((g4 & 1) << 1);
      const int po = (8 * g4 + q4) * 128 + (((2 * c + (p4 >> 1)) ^ (f1 << 1)) << 4) + (p4 & 1) * 8;
#pragma unroll
      for (int j = 0; j < 4; ++j) {
        LAS char* pp = ((j < 2) ? sm + 3 * SLOT + j * 4096 : sm + L_X + (j - 2) * 4096) + po;
        u32x4 v = tr_pair(pp, pp + 512);
        st16(v, rw, (16 * c + (lane & 15)) * 4096 + (32 * j + 8 * g4) * 2, WS_UT + cg * 64 * 4096 + tt16 * 256);
      }
    }
    BAR();
    LAS char* fbp = ub + 2 * (16 - 12 + 32 * rg + 4 * (q4 + p4));
#pragma unroll
    for (int s = 0; s < 16; ++s) F[s] = tr_frag(fbp + 256 * s, fbp + 256 * s + 32);
    LBAR();
    TS(3);
    *(LAS unsigned*)(ub + lane * 4) = 0u;
    WAITVM(0);
    BAR();
    if (c == 0) gflag(ws, WS_FU + (cg * 16 + tt16) * 128, tag, lane);
    TS(5);
    asm volatile("global_load_dword %0, %1, off" : "+v"(dm)
                 : "v"((const char*)wo + (size_t)((idx * 32 + c * 8 + (lane >> 3)) * 2048 + (xcd >> 2) * 1024 + (lane & 7) * 128)) : "memory");
    BAR();
    BAR();
    BAR();
    fu = __hip_atomic_load((unsigned*)(ws + WS_FU + ((cg * 16 + c16) << 7)), __ATOMIC_RELAXED, __HIP_MEMORY_SCOPE_AGENT);
    BAR();
    BAR();
    if (!__all(fu == tag)) gpoll(ws + WS_FU + ((cg * 16 + c16) << 7), tag);
    asm volatile("" :: "v"(dm));
    TS(6);
#pragma unroll
    for (int i = 0; i < 4; ++i)
      __builtin_amdgcn_global_load_lds((const GAS unsigned*)(ws + WS_UT + d * 4096 + i * 1024 + lane * 16), (LAS unsigned*)(ub + 256 + i * 1024), 16, 0, SC1);
    BAR();
    BAR();
    BAR();
    waitge(FL + F_XP, 4);
    WAITVM(0);
  }
  TS(8); TC(2);
  unsigned yp[16];
  {
    LAS char* gb = ub + 2 * (128 + 16 * p4 + 12 - 32 * rg - 4 * q4);
    float pc1 = 0.f, pc2 = 0.f, pc3 = 0.f, yprev = 0.f;
    const bool l0 = (lane == 0);
#define ROR1(v) __int_as_float(__builtin_amdgcn_update_dpp(0, __float_as_int(v), 0x13C, 0xf, 0xf, false))
#define FIN(i)                                                                       \
  do {                                                                               \
    float r3 = ROR1(ca[i][3]);                                                       \
    float s2 = ca[i][2] + (l0 ? pc3 : r3);                                           \
    float r2 = ROR1(s2);                                                             \
    float s1 = ca[i][1] + (l0 ? pc2 : r2);                                           \
    float r1 = ROR1(s1);                                                             \
    float y = ca[i][0] + (l0 ? pc1 : r1);                                            \
    pc1 = r1; pc2 = r2; pc3 = r3;                                                    \
    if ((i) & 1) yp[8 * pass + ((i) >> 1)] = pk_bf16(yprev, y); else yprev = y;      \
  } while (0)
#pragma unroll
    for (int pass = 0; pass < 2; ++pass) {
      f32x4 ca[16];
#pragma unroll
      for (int i = 0; i < 16; ++i) ca[i] = (f32x4){0, 0, 0, 0};
      bf16x8 G = tr_frag(gb, gb - 32);
#pragma unroll
      for (int v = 0; v < 16 * pass + 16; ++v) {
        bf16x8 Gn = G;
        if (v + 1 < 16 * pass + 16) Gn = tr_frag(gb + 128 * (v + 1), gb + 128 * (v + 1) - 32);
        SB();
#pragma unroll
        for (int i = 0; i < 16; ++i) {
          const int T = 16 * pass + i;
          if (T >= v && ((T - v) & 1) == 0) ca[i] = MFMA16(G, F[(T - v) >> 1], ca[i]);
        }
        if (v - 1 >= 16 * pass) FIN(v - 1 - 16 * pass);
        SB();
        G = Gn;
      }
      FIN(15);
      CBAR();
      SB();
    }
#undef FIN
#undef ROR1
  }
  CBAR();
  TS(9); TC(3);
#pragma unroll
  for (int i = 0; i < 16; ++i) {
    ((LAS u16*)ub)[128 * i + lane] = (u16)yp[i];
    ((LAS u16*)ub)[128 * i + 64 + lane] = (u16)(yp[i] >> 16);
  }
  CBAR();
#pragma unroll
  for (int i = 0; i < 4; ++i) st16(*(LAS u32x4*)(ub + i * 1024 + lane * 16), rw, i * 1024 + lane * 16, WS_YT + d * 4096);
  WAITVM(0);
  if (sig_ret(FL + F_CY) == 3u) {
    gflag(ws, WS_FY + (cg * 16 + tt16) * 128, tag, lane);
    TS(10);
    gpoll(ws + WS_FY + ((cg * 16 + (lane & 15)) << 7), tag);
    sig(FL + F_Y);
  } else {
    TS(10);
    waitge(FL + F_Y, 1);
  }
  TS(11);
  {
    int ln2 = lane;
    asm volatile("" : "+v"(ln2));
    const int lane = ln2, q4 = (lane & 15) >> 2, p4 = lane & 3, g4 = lane >> 4;
    LAS char* tb = sm + L_UB;
    u32x4 yr[4];
#pragma unroll
    for (int i = 0; i < 4; ++i)
      yr[i] = ld16(rw, (16 * c + 4 * i + g4) * 4096 + (lane & 15) * 16, WS_YT + cg * 64 * 4096 + tt16 * 256);
#pragma unroll
    for (int i = 0; i < 4; ++i) {
      const int row = 16 * c + 4 * i + g4;
      const int f = (row & 3) | (((row >> 3) & 1) << 2);
      *(LAS u32x4*)(tb + row * 256 + (((lane & 15) ^ (f << 1)) << 4)) = yr[i];
    }
    sig(FL + F_T2);
    waitge(FL + F_T2, 4);
    const int f = q4 | ((g4 & 1) << 2);
#pragma unroll
    for (int j = 0; j < 4; ++j) {
      const int tbk = 2 * c + (j >> 1), hf = j & 1;
      LAS char* pp = tb + (32 * hf + 8 * g4 + q4) * 256 + (((2 * tbk + (p4 >> 1)) ^ (f << 1)) << 4) + (p4 & 1) * 8;
      u32x4 v = tr_pair(pp, pp + 1024);
      ost16(out + (size_t)(tt16 * 128 + 16 * tbk + (lane & 15)) * 2048 + 1024 + cg * 64 + 32 * hf + 8 * g4, v);
    }
  }
  TS(12);
  TSDUMP();
}

extern "C" void kernel_launch(void* const* d_in, const int* in_sizes, int n_in, void* d_out, int out_size,
                              void* d_ws, size_t ws_size, hipStream_t stream) {
  fused_kernel<<<256, 768, 0, stream>>>((const u16*)d_in[0], (const u16*)d_in[1], (const u16*)d_in[2], (const u16*)d_in[3],
                                        (const u16*)d_in[4], (const u16*)d_in[5], (const u16*)d_in[6], (const u16*)d_in[7],
                                        (const u16*)d_in[8], (const u16*)d_in[9], (u16*)d_out, (char*)d_ws);
}
```

```cpp
#include <hip/hip_runtime.h>
#include <stdint.h>

#define LAS __attribute__((address_space(3)))
#define GAS __attribute__((address_space(1)))
#define DI __device__ __forceinline__

typedef unsigned short u16;
typedef short s16x4 __attribute__((ext_vector_type(4)));
typedef __bf16 bf16x2 __attribute__((ext_vector_type(2)));
typedef __bf16 bf16x8 __attribute__((ext_vector_type(8)));
typedef _Float16 f16x2 __attribute__((ext_vector_type(2)));
typedef _Float16 f16x8 __attribute__((ext_vector_type(8)));
typedef float f32x2 __attribute__((ext_vector_type(2)));
typedef float f32x4 __attribute__((ext_vector_type(4)));
typedef float f32x16 __attribute__((ext_vector_type(16)));
typedef unsigned u32x2 __attribute__((ext_vector_type(2)));
typedef unsigned u32x4 __attribute__((ext_vector_type(4)));
typedef __amdgpu_buffer_rsrc_t rsrc_t;

#define CBAR() asm volatile("" ::: "memory")
#define SB() __builtin_amdgcn_sched_barrier(0)
#define WAITVM(n) asm volatile("s_waitcnt vmcnt(" #n ")" ::: "memory")
#define WAITVL(n) asm volatile("s_waitcnt vmcnt(" #n ") lgkmcnt(0)" ::: "memory")
#define BAR() asm volatile("s_barrier" ::: "memory")
#define LBAR() do { CBAR(); __builtin_amdgcn_s_waitcnt(0xC07F); asm volatile("s_barrier" ::: "memory"); } while (0)
#define TSDECL
#define TS(i)
#define TC(i)
#define TSDUMP()
#define KOFF ((ml & 3) * 2 + 4 * (xcd >> 1) + 8 * (xcd & 1))
#define PRIO_C 2
#define PRIO_L 3
#define PSLEEP 3
#define NRING 12

DI void ost16(void* p, u32x4 v) { asm volatile("global_store_dwordx4 %0, %1, off sc1\n\ts_nop 1" :: "v"(p), "v"(v) : "memory"); }
DI float sum16(float x) {
  auto r = __builtin_amdgcn_permlane16_swap(__float_as_uint(x), __float_as_uint(x), false, false);
  float s = __uint_as_float(r[0]) + __uint_as_float(r[1]);
  asm("" : "+v"(s));
  return s;
}
DI float sum32(float x) {
  auto r = __builtin_amdgcn_permlane32_swap(__float_as_uint(x), __float_as_uint(x), false, false);
  float s = __uint_as_float(r[0]) + __uint_as_float(r[1]);
  asm("" : "+v"(s));
  return s;
}
DI float sum8(float x) {
  return x + __int_as_float(__builtin_amdgcn_update_dpp(0, __float_as_int(x), 0x128, 0xf, 0xf, false));
}
DI float wave_max(float x) {
#define DPPMAX(ctl) x = __builtin_fmaxf(x, __int_as_float(__builtin_amdgcn_update_dpp(0, __float_as_int(x), ctl, 0xf, 0xf, false)))
  DPPMAX(0xB1); DPPMAX(0x4E); DPPMAX(0x141); DPPMAX(0x140);
#undef DPPMAX
  auto r = __builtin_amdgcn_permlane16_swap(__float_as_uint(x), __float_as_uint(x), false, false);
  x = __builtin_fmaxf(__uint_as_float(r[0]), __uint_as_float(r[1]));
  r = __builtin_amdgcn_permlane32_swap(__float_as_uint(x), __float_as_uint(x), false, false);
  return __builtin_fmaxf(__uint_as_float(r[0]), __uint_as_float(r[1]));
}
DI int lane_id() {
  int l = __builtin_amdgcn_mbcnt_hi(~0u, __builtin_amdgcn_mbcnt_lo(~0u, 0u));
  asm volatile("" : "+v"(l));
  return l;
}
DI float bf2f(u16 v) { return __uint_as_float((unsigned)v << 16); }
DI unsigned pk_bf16(float a, float b) {
  f32x2 f = {a, b};
  return __builtin_bit_cast(unsigned, __builtin_convertvector(f, bf16x2));
}
DI unsigned pk_f16(float a, float b) {
  f32x2 f = {a, b};
  return __builtin_bit_cast(unsigned, __builtin_convertvector(f, f16x2));
}
DI float rbf(float a) { return __uint_as_float(pk_bf16(a, 0.f) << 16); }
DI u32x2 tr_read(LAS char* p) {
  s16x4 v = __builtin_amdgcn_ds_read_tr16_b64_v4i16((LAS s16x4*)p);
  return __builtin_bit_cast(u32x2, v);
}
DI u32x4 join(u32x2 a, u32x2 b) { u32x4 r = {a.x, a.y, b.x, b.y}; return r; }
DI u32x4 tr_pair(LAS char* p0, LAS char* p1) { return join(tr_read(p0), tr_read(p1)); }
DI bf16x8 asbf(u32x4 v) { return __builtin_bit_cast(bf16x8, v); }
DI bf16x8 tr_frag(LAS char* p0, LAS char* p1) { return asbf(tr_pair(p0, p1)); }
#define MFMA32(a, b, c) __builtin_amdgcn_mfma_f32_32x32x16_bf16((a), (b), (c), 0, 0, 0)
#define MFMA16(a, b, c) __builtin_amdgcn_mfma_f32_16x16x32_bf16((a), (b), (c), 0, 0, 0)
#define MFMA16H(a, b, c) __builtin_amdgcn_mfma_f32_16x16x32_f16((a), (b), (c), 0, 0, 0)
#define AMFMA16(c, a, b) asm volatile("v_mfma_f32_16x16x32_bf16 %0, %1, %2, %0" : "+v"(c) : "v"(a), "v"(b))
#define GLDS(src, dst) __builtin_amdgcn_global_load_lds((const GAS unsigned*)(src), (LAS unsigned*)(dst), 16, 0, 0)
#define SC1 16
DI u32x4 ld16(rsrc_t r, int voff, int soff) { return __builtin_amdgcn_raw_buffer_load_b128(r, voff, soff, SC1); }
DI void st16(u32x4 v, rsrc_t r, int voff, int soff) {
  __builtin_amdgcn_raw_buffer_store_b128(v, r, voff, soff, SC1);
  asm volatile("s_nop 1" :: "v"(v) : "memory");
}

constexpr int WS_EPOCH = 0;
constexpr int WS_FY = 65536, WS_FU = 131072, WS_FP = WS_FU + 131072;
constexpr int WS_O = 524288;
constexpr int WS_UT = WS_O + (4 << 20);
constexpr int WS_YT = WS_UT + (4 << 20);
constexpr int WS_WT = WS_YT + (4 << 20);
constexpr int WS_KVP = WS_WT + (2 << 20);
constexpr int WS_ZP = WS_KVP + 16 * 16 * 16 * 512;
constexpr int WS_KVR = WS_ZP + 32 * 16 * 128;
constexpr int WS_END = WS_KVR + 32 * 4096;

constexpr int SLOT = 40960;
constexpr int L_UB = 131072, UB_CH = 4352;
constexpr int L_X = L_UB + 4 * UB_CH;
constexpr int L_Z = L_X + 4 * 2176;
constexpr int L_FLAG = L_Z + 512;
constexpr int LDS_BYTES = L_FLAG + 256;
enum { F_XE = 8, F_LW = 11, F_CY, F_T2, F_XP, F_Z = 24, F_Y = 26 };

typedef f32x2 cplx;
DI cplx cmul(cplx a, cplx b) {
  cplx t, r;
  asm("v_pk_mul_f32 %0, %1, %2 op_sel:[1,1] op_sel_hi:[1,0] neg_lo:[1,0]" : "=v"(t) : "v"(a), "v"(b));
  asm("v_pk_fma_f32 %0, %1, %2, %3 op_sel:[0,0,0] op_sel_hi:[0,1,1]" : "=v"(r) : "v"(a), "v"(b), "v"(t));
  return r;
}
DI cplx zpow(float arl2, float ai, float p) {
  const float INV2PI = 0.15915494309189535f;
  const float INV2PI_LO = (float)(0.15915494309189533576888 - (double)0.15915494309189535f);
  float e = __builtin_amdgcn_exp2f(arl2 * p);
  float r = ai * p;
  float hi = r * INV2PI;
  float lo = __builtin_fmaf(r, INV2PI_LO, __builtin_fmaf(r, INV2PI, -hi));
  float f = (hi - __builtin_rintf(hi)) + lo;
  cplx z = {e * __builtin_amdgcn_cosf(f), e * __builtin_amdgcn_sinf(f)};
  return z;
}
template <bool NOPK> DI float phi_f(float t) {
  float a = t * 1.4426950408889634f, b = t + 1.f;
  if (NOPK) { asm("" : "+v"(a)); asm("" : "+v"(b)); }
  float r = __builtin_amdgcn_fmed3f(__builtin_amdgcn_exp2f(a), b, 1.f) + 1e-6f;
  if (NOPK) asm("" : "+v"(r));
  return r;
}
DI u32x4 tag_put(f32x4 v, unsigned tag) {
  u32x4 b = __builtin_bit_cast(u32x4, v), r;
  r[0] = __builtin_amdgcn_perm(b[0], tag, 0x07060500u);
  r[1] = __builtin_amdgcn_perm(b[1], tag, 0x07060501u);
  r[2] = __builtin_amdgcn_perm(b[2], tag, 0x07060502u);
  r[3] = __builtin_amdgcn_perm(b[3], tag, 0x07060503u);
  return r;
}
DI unsigned tag_get(u32x4 b) {
  unsigned lo = __builtin_amdgcn_perm(b[1], b[0], 0x0c0c0400u), hi = __builtin_amdgcn_perm(b[3], b[2], 0x04000c0cu);
  return lo | hi;
}
DI f32x4 tag_val(u32x4 b) { return __builtin_bit_cast(f32x4, b & 0xffffff00u); }

DI void sig(LAS unsigned* f) {
  asm volatile("s_mov_b64 exec, 1\n\tds_add_u32 %0, %1\n\ts_mov_b64 exec, -1" :: "v"((unsigned)(size_t)f), "v"(1u) : "memory");
}
DI unsigned sig_ret(LAS unsigned* f) {
  unsigned r;
  asm volatile("s_mov_b64 exec, 1\n\tds_add_rtn_u32 %0, %1, %2\n\ts_mov_b64 exec, -1\n\ts_waitcnt lgkmcnt(0)"
               : "=&v"(r) : "v"((unsigned)(size_t)f), "v"(1u) : "memory");
  return __builtin_amdgcn_readfirstlane(r);
}
DI unsigned ldsword(LAS unsigned* f) { return __builtin_amdgcn_readfirstlane(*(volatile LAS unsigned*)f); }
DI void waitge(LAS unsigned* f, unsigned n) {
  CBAR();
#pragma unroll 1
  for (int it = 0; it < (1 << 22); ++it) {
    if (ldsword(f) >= n) break;
    __builtin_amdgcn_s_sleep(1);
  }
  CBAR();
}
DI void gflag(char* ws, int off, unsigned tag, int lane) {
  CBAR();
  if (lane == 0) __hip_atomic_store((unsigned*)(ws + off), tag, __ATOMIC_RELAXED, __HIP_MEMORY_SCOPE_AGENT);
  CBAR();
}
DI void gpoll(char* p, unsigned tag) {
  CBAR();
#pragma unroll 1
  for (int it = 0; it < (1 << 18); ++it) {
    unsigned v = __hip_atomic_load((unsigned*)p, __ATOMIC_RELAXED, __HIP_MEMORY_SCOPE_AGENT);
    if (__all(v == tag)) break;
    __builtin_amdgcn_s_sleep(PSLEEP);
  }
  CBAR();
}

__global__ __launch_bounds__(768, 3) void fused_kernel(const u16* __restrict__ x, const u16* __restrict__ wqkv,
                                                       const u16* __restrict__ wo, const u16* __restrict__ u,
                                                       const u16* __restrict__ lar, const u16* __restrict__ aim,
                                                       const u16* __restrict__ cre, const u16* __restrict__ cim,
                                                       const u16* __restrict__ ldt, const u16* __restrict__ dsk,
                                                       u16* __restrict__ out, char* __restrict__ ws) {
  __shared__ __attribute__((aligned(16))) char smem[LDS_BYTES];
  LAS char* sm = (LAS char*)smem;
  LAS unsigned* FL = (LAS unsigned*)(sm + L_FLAG);
  const int tid = threadIdx.x, lane = tid & 63;
  const int wave = __builtin_amdgcn_readfirstlane(tid >> 6);
  const int bid = blockIdx.x, xcd = bid & 7, idx = bid >> 3;
  const int ml = idx & 7, hl = idx >> 3;
  const int mb = (xcd & 1) * 8 + ml, hp = (xcd >> 1) * 4 + hl;
  const int mb2 = (xcd & 3) * 4 + (idx & 3), nb = (xcd >> 2) * 8 + (idx >> 2);
  const rsrc_t rw = __builtin_amdgcn_make_buffer_rsrc((void*)ws, 0, WS_END, 0x00020000);
  TSDECL; TS(0);
  asm volatile("" :: "s"(x), "s"(wqkv), "s"(wo), "s"(u), "s"(lar), "s"(aim), "s"(cre), "s"(cim), "s"(ldt), "s"(dsk), "s"(out), "s"(ws));
  unsigned tag;
#define TAGLD() asm volatile("s_load_dword %0, %1, 0x0" : "=s"(tag) : "s"(ws) : "memory")
#define TAGRD() do { asm volatile("s_waitcnt lgkmcnt(0)" : "+s"(tag) :: "memory"); tag = tag * 2654435761u + 0x9E3779B1u; } while (0)
  const int q4 = (lane & 15) >> 2, p4 = lane & 3, g4 = lane >> 4, c16 = lane & 15;

  if ((wave >> 2) == 1) {
    __builtin_amdgcn_s_setprio(PRIO_L);
    const int hw = wave - 4;
    const int koff = KOFF;
    const unsigned vx0 = (unsigned)((mb * 128 + hw * 32 + (lane >> 3)) * 2048) + (((lane & 7) ^ (lane >> 4)) << 4), vx1 = vx0 ^ 64;
    const unsigned vw0 = (unsigned)((lane >> 3) * 6144 + hp * 128 + (((lane & 7) ^ (((lane >> 4) & 1) << 1)) << 4)), vw1 = vw0 ^ 64;
#define ISSUE(tt_, slot_)                                                                                  \
  do {                                                                                                     \
    const int k_ = ((tt_) + koff) & 15;                                                                    \
    const char* xa_ = (const char*)x + k_ * 128;                                                           \
    const char* wa_ = (const char*)wqkv + (size_t)k_ * 393216;                                             \
    LAS char* sb_ = sm + (slot_) * SLOT;                                                                   \
    _Pragma("unroll") for (int i = 0; i < 4; ++i)                                                          \
        GLDS(xa_ + i * 16384 + ((i & 1) ? vx1 : vx0), sb_ + (hw * 4 + i) * 1024);                          \
    _Pragma("unroll") for (int i = 0; i < 6; ++i) {                                                        \
      const int qq_ = hw * 6 + i;                                                                          \
      GLDS(wa_ + ((qq_ & 7) * 49152 + (qq_ >> 3) * 2048) + ((i & 1) ? vw1 : vw0), sb_ + 16384 + qq_ * 1024); \
    }                                                                                                      \
  } while (0)
    ISSUE(0, 0);
    TS(13);
    ISSUE(1, 1);
    ISSUE(2, 2);
    TS(14);
    TAGLD();
    WAITVM(20);
    TAGRD();
    TS(1);
    BAR();
    {
      int sf = 0;
#pragma unroll 1
      for (int t = 0; t < 13; ++t) {
        WAITVM(10);
        BAR();
        ISSUE(t + 3, sf);
        sf = (sf == 2) ? 0 : sf + 1;
      }
    }
#undef ISSUE
    TS(2);
    {
      const char* src = ws + WS_WT + nb * 131072 + hw * 1024 + lane * 16;
#define WOP(s_) __builtin_amdgcn_global_load_lds((const GAS unsigned*)(src + (s_) * 4096), (LAS unsigned*)(sm + ((s_) * 4 + hw) * 1024), 16, 0, SC1)
      WAITVM(10);
      BAR();
#pragma unroll
      for (int i = 10; i < 20; ++i) WOP(i);
      WAITVM(10);
      BAR();
#pragma unroll
      for (int i = 20; i < 32; ++i) WOP(i);
      BAR();
#pragma unroll
      for (int i = 0; i < 10; ++i) WOP(i);
#undef WOP
      TS(3);
      WAITVM(0);
      sig(FL + F_LW);
    }
    TS(4);
    waitge(FL + F_XP, 4);
    {
      const int so = WS_KVP + (hp * 16 + mb) * 8192;
      const int vo = (lane >> 3) * 512 + (hw * 8 + (lane & 7)) * 16;
      u32x4 a, b;
#pragma unroll 1
      for (int it = 0; it < (1 << 17); ++it) {
        CBAR();
        a = ld16(rw, vo, so);
        b = ld16(rw, vo + 4096, so);
        if (__all(((tag_get(a) ^ tag) | (tag_get(b) ^ tag)) == 0u)) break;
        __builtin_amdgcn_s_sleep(PSLEEP);
      }
      TS(5);
      const f32x4 fa = tag_val(a), fb = tag_val(b);
      float s[4];
#pragma unroll
      for (int i = 0; i < 4; ++i) { s[i] = fa[i] + fb[i]; asm("" : "+v"(s[i])); }
#pragma unroll
      for (int i = 0; i < 4; ++i) s[i] = sum32(sum16(sum8(s[i])));
      const unsigned p0 = pk_bf16(s[0], s[1]), p1 = pk_bf16(s[2], s[3]);
      const u32x4 w = {__builtin_amdgcn_perm(p0, tag, 0x05040100u), __builtin_amdgcn_perm(p0, tag, 0x07060302u),
                       __builtin_amdgcn_perm(p1, tag, 0x05040100u), __builtin_amdgcn_perm(p1, tag, 0x07060302u)};
      if (lane < 8) st16(w, rw, lane * 16, WS_KVR + hp * 8192 + mb * 512 + hw * 128);
      TS(6);
    }
    TS(9);
    TSDUMP();
    return;
  }

  const int c = wave & 3, d = bid * 4 + c;
  LAS char* ub = sm + L_UB + c * UB_CH;
  LAS char* kc = (c < 2) ? sm + 3 * SLOT + c * 4096 : sm + L_X + (c - 2) * 4096;
  f32x4 ta = {0, 0, 0, 0}, tb = {0, 0, 0, 0}, tc_ = {0, 0, 0, 0};
  float v_dsk = bf2f(dsk[d]);
  {
    const int pi = d * 128 + (wave < 4 ? 64 : 0) + lane;
    const float lr = bf2f(lar[pi]), aiv = bf2f(aim[pi]), cr = bf2f(cre[pi]), ci = bf2f(cim[pi]);
    const float dt = rbf(expf(bf2f(ldt[d])));
    LAS char* ks = (wave < 4) ? kc : ub;
    TAGLD();
    if (wave == 0 && lane < 32) FL[lane] = 0u;
    const float mx = wave_max(__builtin_fmaxf(__builtin_fabsf(cr), __builtin_fabsf(ci)));
    const int ex = (mx > 0.f && mx < 3.0e38f) ? __builtin_amdgcn_frexp_expf(mx) : 0;
    const float mult = __builtin_amdgcn_ldexpf(2.f * dt, ex);
    asm volatile("" : "+v"(v_dsk));
    TAGRD();
    TS(15);
    const float ar = -rbf(expf(lr)) * dt;
    const float ai = aiv * dt;
    const float arl2 = ar * 1.4426950408889634f;
    const cplx z1 = zpow(arl2, ai, 1.f), z8 = zpow(arl2, ai, 8.f), z128 = zpow(arl2, ai, 128.f);
    LAS char* wp = ks + lane * 64 + (((lane >> 1) & 3) << 4);
    const int h0 = g4 & 1;
    LAS char* rp0 = ks + (g4 * 256 + h0 * 128 + (q4 >> 1) * 64 + (((2 * (q4 & 1) + (p4 >> 1)) ^ ((2 * g4 + h0) & 3)) << 4) + (p4 & 1) * 8);
    LAS char* rp1 = ks + ((unsigned)(rp0 - ks) ^ 144u);
#define KW(v, sg)                                                                                                    \
  _Pragma("unroll") for (int hh = 0; hh < 2; ++hh) {                                                                 \
    u32x4 wr_, wi_;                                                                                                  \
    _Pragma("unroll") for (int i = 0; i < 4; ++i) {                                                                  \
      wr_[i] = pk_f16(v[8 * hh + 2 * i][0], v[8 * hh + 2 * i + 1][0]);                                               \
      wi_[i] = pk_f16(sg v[8 * hh + 2 * i][1], sg v[8 * hh + 2 * i + 1][1]);                                         \
    }                                                                                                                \
    *(LAS u32x4*)(ks + ((unsigned)(wp - ks) ^ (unsigned)(hh << 4))) = wr_;                                           \
    *(LAS u32x4*)(ks + ((unsigned)(wp - ks) ^ (unsigned)((2 + hh) << 4))) = wi_;                                     \
  }
#define KR(ks_) __builtin_bit_cast(f16x8, tr_pair(rp0 + (ks_) * 1024, rp1 + (ks_) * 1024))
    f16x8 bfr[4];
    cplx v[16], X[17], zp[9];
    v[0] = (cplx){1.f, 0.f};
    v[1] = z8;
    v[2] = cmul(z8, z8);
#pragma unroll
    for (int k = 3; k < 16; ++k) v[k] = cmul(v[k - 2], v[2]);
    KW(v, -);
    X[0] = (cplx){__builtin_amdgcn_ldexpf(cr, -ex), __builtin_amdgcn_ldexpf(ci, -ex)};
    X[1] = cmul(X[0], z128);
    {
      const cplx z256 = cmul(z128, z128);
#pragma unroll
      for (int r = 2; r < 17; ++r) X[r] = cmul(X[r - 2], z256);
    }
    zp[1] = z1; zp[8] = z8;
    zp[2] = cmul(z1, z1); zp[3] = cmul(zp[2], z1); zp[4] = cmul(zp[2], zp[2]);
    zp[5] = cmul(zp[4], z1); zp[6] = cmul(zp[4], zp[2]); zp[7] = cmul(zp[4], zp[3]);
#pragma unroll
    for (int k = 0; k < 4; ++k) bfr[k] = KR(k);
#pragma unroll
    for (int g = 0; g < 4; ++g) { v[4 * g] = X[4 + g]; v[4 * g + 1] = cmul(X[4 + g], zp[4]); v[4 * g + 2] = X[8 + g]; v[4 * g + 3] = X[12 + g]; }
    KW(v, );
#pragma unroll
    for (int j = 0; j < 16; ++j) v[j] = (j & 7) ? cmul(X[j >> 3], zp[j & 7]) : X[j >> 3];
    {
      f16x8 a[4];
#pragma unroll
      for (int k = 0; k < 4; ++k) a[k] = KR(k);
      KW(v, );
#pragma unroll
      for (int k = 0; k < 4; ++k) ta = MFMA16H(a[k], bfr[k], ta);
    }
#pragma unroll
    for (int a = 0; a < 2; ++a) {
      v[8 * a] = X[2 + a];
      v[8 * a + 1] = cmul(X[2 + a], zp[2]);
      v[8 * a + 2] = v[8 * a + 4] = cmul(X[2 + a], zp[4]);
      v[8 * a + 5] = cmul(X[2 + a], zp[6]);
      v[8 * a + 6] = cmul(X[2 + a], zp[8]);
      v[8 * a + 3] = v[8 * a + 7] = X[16];
    }
    {
      f16x8 a[4];
#pragma unroll
      for (int k = 0; k < 4; ++k) a[k] = KR(k);
      KW(v, );
#pragma unroll
      for (int k = 0; k < 4; ++k) tc_ = MFMA16H(a[k], bfr[k], tc_);
    }
#pragma unroll
    for (int k = 0; k < 4; ++k) { f16x8 a = KR(k); tb = MFMA16H(a, bfr[k], tb); }
#undef KW
#undef KR
    ta *= mult; tb *= mult; tc_ *= mult;
    TS(2);
  }

  if (wave < 4) {
    *(LAS f32x4*)(kc + lane * 16) = ta;
    *(LAS f32x4*)(kc + 1024 + lane * 16) = tb;
    *(LAS f32x4*)(kc + 2048 + lane * 16) = tc_;
    __builtin_amdgcn_s_setprio(PRIO_C);
    const int mh = wave & 1, hd = wave >> 1;
    const int a0 = (mh * 64 + c16) * 128 + ((g4 ^ (c16 >> 1)) << 4);
    const int b0 = 16384 + (8 * g4 + q4) * 128 + (((hd * 4 + (p4 >> 1)) ^ (((q4 >> 1) | ((g4 & 1) << 1)) << 1)) << 4) + (p4 & 1) * 8;
    f32x4 acc[4][6];
#pragma unroll
    for (int m = 0; m < 4; ++m)
#pragma unroll
      for (int g = 0; g < 6; ++g) acc[m][g] = (f32x4){0.f, 0.f, 0.f, 0.f};
#pragma unroll
    for (int m = 0; m < 4; ++m)
      asm volatile("" : "+v"(acc[m][0]), "+v"(acc[m][1]), "+v"(acc[m][2]), "+v"(acc[m][3]), "+v"(acc[m][4]), "+v"(acc[m][5]));
    bf16x8 fa[2][4];
    u32x2 bl[6], bh[6];
#define RA(set, m, pa) fa[set][m] = *(LAS bf16x8*)((pa) + (m) * 2048)
#define RL(j, pe, po) bl[(j) % 6] = tr_read((((j) & 1) ? (po) : (pe)) + (((j) % 6) >> 1) * 8192 + ((j) / 6) * 4096)
#define RH(j, pe, po) bh[(j) % 6] = tr_read((((j) & 1) ? (po) : (pe)) + (((j) % 6) >> 1) * 8192 + ((j) / 6) * 4096 + 512)
#define FB(j) asbf(join(bl[(j) % 6], bh[(j) % 6]))
#define MM(j, m) acc[m][(j) % 6] = ((j) % 6 < 2) ? MFMA16(FB(j), fa[(j) / 6][m], acc[m][(j) % 6]) : MFMA16(fa[(j) / 6][m], FB(j), acc[m][(j) % 6])
#define GRP(j, X0, X1, X2, X3)                \
  do {                                        \
    MM(j, 0); X0; SB(); MM(j, 1); X1; SB();   \
    MM(j, 2); X2; SB(); MM(j, 3); X3; SB();   \
  } while (0)
    LAS char* pa = sm + a0;
    LAS char* pa1 = sm + (a0 ^ 64);
    LAS char* pe = sm + b0;
    LAS char* po = sm + (b0 ^ 32);
    LBAR();
    TS(3); TC(0);
    RA(0, 0, pa); RL(0, pe, po); RH(0, pe, po); RA(0, 1, pa); RA(0, 2, pa); RA(0, 3, pa);
    RL(1, pe, po); RH(1, pe, po); RL(2, pe, po); RH(2, pe, po);
    {
      int slot = 0;
#pragma unroll 1
      for (int t = 0; t < 16; ++t) {
        const int dl = (slot == 2) ? -2 * SLOT : SLOT;
        slot = (slot == 2) ? 0 : slot + 1;
        SB();
        GRP(0, RL(3, pe, po), RH(3, pe, po), , );
        GRP(1, RL(4, pe, po), RH(4, pe, po), , );
        GRP(2, RL(5, pe, po), RH(5, pe, po), RA(1, 0, pa1), RA(1, 1, pa1));
        GRP(3, RL(6, pe, po), RH(6, pe, po), RA(1, 2, pa1), RA(1, 3, pa1));
        GRP(4, RL(7, pe, po), RH(7, pe, po), RL(8, pe, po), RH(8, pe, po));
        GRP(5, RL(9, pe, po), RH(9, pe, po), RL(10, pe, po), RH(10, pe, po));
        GRP(6, RL(11, pe, po), RH(11, pe, po), , );
        GRP(7, , , , );
        GRP(8, pa += dl, pa1 += dl, pe += dl, po += dl);
        asm volatile("" : "+v"(pa), "+v"(pa1), "+v"(pe), "+v"(po));
        LBAR();
        SB();
        GRP(9, RA(0, 0, pa), RL(0, pe, po), RH(0, pe, po), RA(0, 1, pa));
        GRP(10, RA(0, 2, pa), RA(0, 3, pa), RL(1, pe, po), RH(1, pe, po));
        GRP(11, RL(2, pe, po), RH(2, pe, po), , );
      }
    }
#undef RA
#undef RL
#undef RH
#undef FB
#undef MM
#undef GRP
    TS(4); TC(1);
    {
    const int lane = lane_id(), g4 = lane >> 4, c16 = lane & 15;
    const int head = hp * 2 + hd;
    {
      f32x4 kvt[2][2], zt[2];
#pragma unroll
      for (int i = 0; i < 2; ++i) { kvt[i][0] = kvt[i][1] = zt[i] = (f32x4){0.f, 0.f, 0.f, 0.f}; }
      const u32x4 ones = {0x3F803F80u, 0x3F803F80u, 0x3F803F80u, 0x3F803F80u};
#pragma unroll
      for (int j = 0; j < 2; ++j) {
        u32x4 fk[2], fv[2];
#pragma unroll
        for (int n = 0; n < 2; ++n)
#pragma unroll
          for (int i = 0; i < 4; ++i) {
            const f32x4 kk = acc[2 * j + (i >> 1)][2 + n], vv = acc[2 * j + (i >> 1)][4 + n];
            fk[n][i] = pk_bf16(phi_f<false>(kk[2 * (i & 1)]), phi_f<false>(kk[2 * (i & 1) + 1]));
            fv[n][i] = pk_bf16(vv[2 * (i & 1)], vv[2 * (i & 1) + 1]);
          }
#pragma unroll
        for (int td = 0; td < 2; ++td) {
          kvt[0][td] = MFMA16(asbf(fk[td]), asbf(fv[0]), kvt[0][td]);
          kvt[1][td] = MFMA16(asbf(fk[td]), asbf(fv[1]), kvt[1][td]);
          zt[td] = MFMA16(asbf(ones), asbf(fk[td]), zt[td]);
        }
      }
      LAS char* xo = sm + L_X + wave * 2176;
      LAS char* xi = sm + L_X + (wave ^ 1) * 2176;
      f32x4 keep[2];
#pragma unroll
      for (int td = 0; td < 2; ++td) {
        f32x4 ka = kvt[0][td], kb = kvt[1][td];
        asm("" : "+v"(ka), "+v"(kb));
        *(LAS f32x4*)(xo + td * 1024 + lane * 16) = mh ? ka : kb;
        keep[td] = mh ? kb : ka;
      }
      *(LAS float*)(xo + 2048 + (lane & 31) * 4) = (g4 & 1) ? zt[1][0] : zt[0][0];
      sig(FL + F_XE + hd);
      waitge(FL + F_XE + hd, 2);
      if (mh == 0) {
        f32x4 zz = *(LAS f32x4*)(xo + 2048 + (lane & 7) * 16) + *(LAS f32x4*)(xi + 2048 + (lane & 7) * 16);
        if (lane < 8) st16(tag_put(zz, tag), rw, lane * 16, WS_ZP + (head * 16 + mb) * 128);
      }
      const int so = WS_KVP + (hp * 16 + hd * 8 + mh * 4) * 8192 + mb * 512;
#pragma unroll
      for (int td = 0; td < 2; ++td) {
        f32x4 o = *(LAS f32x4*)(xi + td * 1024 + lane * 16) + keep[td];
        st16(tag_put(o, tag), rw, td * 16384 + (lane >> 5) * 8192 + (lane & 31) * 16, so);
      }
      sig(FL + F_XP);
    }
    TS(5);
#pragma unroll
    for (int m = 0; m < 4; ++m)
#pragma unroll
      for (int i = 0; i < 8; ++i) acc[m][i >> 2][i & 3] = phi_f<true>(acc[m][i >> 2][i & 3]);
    asm volatile("" : "+v"(acc[0][0]), "+v"(acc[1][0]), "+v"(acc[2][0]), "+v"(acc[3][0]),
                      "+v"(acc[0][1]), "+v"(acc[1][1]), "+v"(acc[2][1]), "+v"(acc[3][1]));
    u32x4 fq[4];
    {
      LAS f32x4* zl = (LAS f32x4*)(sm + L_Z + hd * 128);
      if (mh == 1) {
        u32x4 za, zb;
#pragma unroll 1
        for (int it = 0; it < (1 << 17); ++it) {
          CBAR();
          za = ld16(rw, lane * 16, WS_ZP + head * 2048);
          zb = ld16(rw, 1024 + lane * 16, WS_ZP + head * 2048);
          if (__all(((tag_get(za) ^ tag) | (tag_get(zb) ^ tag)) == 0u)) break;
          __builtin_amdgcn_s_sleep(PSLEEP);
        }
        const f32x4 fa_ = tag_val(za), fb_ = tag_val(zb);
        f32x4 z;
#pragma unroll
        for (int i = 0; i < 4; ++i) { float s = fa_[i] + fb_[i]; asm("" : "+v"(s)); z[i] = sum32(sum16(sum8(s))); }
        if (lane < 8) zl[lane] = z;
        sig(FL + F_Z + hd);
      } else {
        waitge(FL + F_Z + hd, 1);
      }
      TS(6);
      const f32x4 z0 = zl[g4], z1 = zl[4 + g4];
#pragma unroll
      for (int m = 0; m < 4; ++m) {
        float d0 = acc[m][0][0] * z0[0], d1 = acc[m][1][0] * z1[0];
#pragma unroll
        for (int i = 1; i < 4; ++i) { d0 = __builtin_fmaf(acc[m][0][i], z0[i], d0); d1 = __builtin_fmaf(acc[m][1][i], z1[i], d1); }
        const float den = sum32(sum16(d0 + d1));
        const float rc = __builtin_amdgcn_rcpf(den + 1e-6f);
        float e[8];
#pragma unroll
        for (int i = 0; i < 8; ++i) { e[i] = acc[m][i >> 2][i & 3] * rc; asm("" : "+v"(e[i])); }
        fq[m] = (u32x4){pk_bf16(e[0], e[1]), pk_bf16(e[2], e[3]), pk_bf16(e[4], e[5]), pk_bf16(e[6], e[7])};
      }
    }
    asm volatile("" : "+v"(fq[0]), "+v"(fq[1]), "+v"(fq[2]), "+v"(fq[3]));
    TS(13);
    {
      u32x4 k[4];
      const unsigned t0 = tag & 0xffffu, t1 = tag >> 16;
#pragma unroll 1
      for (int it = 0; it < (1 << 17); ++it) {
        CBAR();
#pragma unroll
        for (int i = 0; i < 4; ++i) k[i] = ld16(rw, i * 1024 + lane * 16, WS_KVR + head * 4096);
        unsigned bad = 0;
#pragma unroll
        for (int i = 0; i < 4; ++i) bad |= (k[i][0] ^ t0) | (k[i][1] ^ t1) | (k[i][2] ^ t0) | (k[i][3] ^ t1);
        if (__all((bad & 0xffffu) == 0u)) break;
        __builtin_amdgcn_s_sleep(PSLEEP);
      }
      TS(14);
      u32x4 ka[2];
#pragma unroll
      for (int te = 0; te < 2; ++te)
#pragma unroll
        for (int i = 0; i < 4; ++i) ka[te][i] = __builtin_amdgcn_perm(k[2 * te + (i >> 1)][2 * (i & 1) + 1], k[2 * te + (i >> 1)][2 * (i & 1)], 0x07060302u);
#pragma unroll
      for (int m = 0; m < 4; ++m) {
        const f32x4 z4 = {0.f, 0.f, 0.f, 0.f};
        const f32x4 d0 = MFMA16(asbf(ka[0]), asbf(fq[m]), z4), d1 = MFMA16(asbf(ka[1]), asbf(fq[m]), z4);
        const u32x4 ov = {pk_bf16(d0[0], d0[1]), pk_bf16(d0[2], d0[3]), pk_bf16(d1[0], d1[1]), pk_bf16(d1[2], d1[3])};
        st16(ov, rw, (m & 1) * 1024 + lane * 16, WS_O + ((mb * 4 + mh * 2 + (m >> 1)) * 32 + head) * 2048);
      }
    }
    TS(7);
    WAITVM(0);
    gflag(ws, WS_FP + ((mb * 2 + mh) * 32 + head) * 128, tag, lane);
    TS(8);
    gpoll(ws + WS_FP + (((mb2 * 2 + (wave >> 1)) * 32 + (lane & 31)) << 7), tag);
    TS(9);
    {
      const int pqs = WS_O + (mb2 * 4 + wave) * 65536;
      u32x4 pb[NRING][2];
#define LDQ(i_) do { const int so_ = pqs + ((i_) << 11);                                             \
        pb[(i_) % NRING][0] = ld16(rw, lane * 16, so_); pb[(i_) % NRING][1] = ld16(rw, 1024 + lane * 16, so_); } while (0)
#pragma unroll
      for (int i = 0; i < NRING; ++i) LDQ(i);
      f32x4 oc[2][4];
#pragma unroll
      for (int s = 0; s < 2; ++s)
#pragma unroll
        for (int t = 0; t < 4; ++t) oc[s][t] = (f32x4){0.f, 0.f, 0.f, 0.f};
      asm volatile("" : "+v"(oc[0][0]), "+v"(oc[0][1]), "+v"(oc[0][2]), "+v"(oc[0][3]),
                   "+v"(oc[1][0]), "+v"(oc[1][1]), "+v"(oc[1][2]), "+v"(oc[1][3]));
      LAS char* mp0 = sm + lane * 16;
      bf16x8 ma[4];
      waitge(FL + F_LW, 4);
      if (bid == 0 && wave == 0) gflag(ws, WS_EPOCH, tag, lane);
      TS(10); TC(2);
#pragma unroll
      for (int t = 0; t < 4; ++t) ma[t] = *(LAS bf16x8*)(mp0 + t * 1024);
#pragma unroll
      for (int i = 0; i < 32; ++i) {
        SB();
        const u32x4 b0_ = pb[i % NRING][0], b1_ = pb[i % NRING][1];
#pragma unroll
        for (int t = 0; t < 4; ++t) {
          AMFMA16(oc[0][t], ma[t], b0_);
          AMFMA16(oc[1][t], ma[t], b1_);
          if (i < 31) ma[t] = *(LAS bf16x8*)(mp0 + (i + 1) * 4096 + t * 1024);
          SB();
        }
        if (i + NRING < 32) LDQ(i + NRING);
      }
#undef LDQ
      asm volatile("s_nop 7\n\ts_nop 3" : "+v"(oc[0][0]), "+v"(oc[0][1]), "+v"(oc[0][2]), "+v"(oc[0][3]),
                   "+v"(oc[1][0]), "+v"(oc[1][1]), "+v"(oc[1][2]), "+v"(oc[1][3]));
      TS(11); TC(3);
#pragma unroll
      for (int s = 0; s < 2; ++s)
#pragma unroll
        for (int j = 0; j < 2; ++j) {
          u32x4 w = {pk_bf16(oc[s][2 * j][0], oc[s][2 * j][1]), pk_bf16(oc[s][2 * j][2], oc[s][2 * j][3]),
                     pk_bf16(oc[s][2 * j + 1][0], oc[s][2 * j + 1][1]), pk_bf16(oc[s][2 * j + 1][2], oc[s][2 * j + 1][3])};
          ost16(out + (size_t)((mb2 * 4 + wave) * 32 + s * 16 + c16) * 2048 + nb * 64 + 32 * j + 8 * g4, w);
        }
    }
    TS(12);
    TSDUMP();
    }
    return;
  }

  const int cg = bid >> 4, tt16 = bid & 15;
  const int rg = lane >> 4;
  BAR();
  bf16x8 F[16];
  unsigned fu;
  {
    {
      const f32x4 oa = *(LAS f32x4*)(kc + lane * 16), ob = *(LAS f32x4*)(kc + 1024 + lane * 16), oc = *(LAS f32x4*)(kc + 2048 + lane * 16);
#pragma unroll
      for (int i = 0; i < 4; ++i) {
        float x0 = ta[i] + oa[i], x1 = tb[i] + ob[i], x2 = tc_[i] + oc[i];
        asm("" : "+v"(x0)); asm("" : "+v"(x1)); asm("" : "+v"(x2));
        ta[i] = x0; tb[i] = x1; tc_[i] = x2;
      }
      CBAR();
    }
    unsigned wd[8];
    {
      const char* wp = (const char*)wo + (size_t)(((tt16 * 2 + (c >> 1)) * 32 + 4 * g4) * 2048 + cg * 128 + (c & 1) * 64 + c16 * 4);
#pragma unroll
      for (int i = 0; i < 8; ++i) wd[i] = *(const unsigned*)(wp + ((i & 3) + (i >> 2) * 16) * 2048);
      CBAR();
    }
#pragma unroll
    for (int i = 0; i < 4; ++i) {
      const int f1 = ((lane >> 4) & 1) | ((i & 1) << 1);
      GLDS((const char*)u + (size_t)((tt16 * 128 + c * 32 + i * 8 + (lane >> 3)) * 2048 + cg * 128 + (((lane & 7) ^ (f1 << 1)) << 4)),
           kc + i * 1024);
    }
#define ROL1(v) (unsigned)__builtin_amdgcn_update_dpp(0, (int)(v), 0x134, 0xf, 0xf, false)
#define MIX(lo, hi) __builtin_amdgcn_perm((hi), (lo), 0x07060100u)
    const bool l0 = (lane == 0);
    const u32x2 w0 = {pk_bf16(tc_[0] + (l0 ? v_dsk : 0.f), tc_[1]), pk_bf16(tc_[2], tc_[3])};
    const u32x2 w1 = {pk_bf16(tb[0], tb[0]), pk_bf16(tb[1], tb[2])};
    const unsigned pa0 = pk_bf16(ta[0], ta[0]), pa1 = pk_bf16(ta[1], ta[1]), pa2 = pk_bf16(ta[2], ta[2]), pa3 = pk_bf16(ta[3], ta[3]);
    const unsigned pe = pk_bf16(tb[3], tb[3]);
    const unsigned n0 = ROL1(l0 ? pa2 : pa0), n2 = ROL1(l0 ? pa3 : pa2), n3 = ROL1(l0 ? pe : pa3);
    const u32x4 w2 = {pa0, MIX(pa0, pa1), pa1, n0};
    const u32x4 w3 = {pa2, pa2, (lane & 1) ? n2 : MIX(pa2, n2), n2};
    const u32x4 w4 = {pa3, pa3, (lane & 1) ? n3 : MIX(pa3, n3), n3};
#undef ROL1
#undef MIX
    LAS char* K = ub + 32;
    const int k01 = 2 * (128 * (rg >> 1) + 8 * c16 + 4 * (rg & 1));
    BAR();
    *(LAS u32x2*)(K + k01) = w0;
    *(LAS u32x2*)(K + 512 + k01) = w1;
    BAR();
    *(LAS u32x4*)(K + 1024 + lane * 16) = w2;
    BAR();
    *(LAS u32x4*)(K + 2048 + lane * 16) = w3;
    BAR();
    *(LAS u32x4*)(K + 3072 + lane * 16) = w4;
    asm volatile("s_waitcnt vmcnt(0)" : "+v"(wd[0]), "+v"(wd[1]), "+v"(wd[2]), "+v"(wd[3]), "+v"(wd[4]), "+v"(wd[5]), "+v"(wd[6]), "+v"(wd[7]) :: "memory");
    {
      u32x4 t0, t1;
      const unsigned sel = (lane & 1) ? 0x07060302u : 0x05040100u;
#define QP(v, ctl) (unsigned)__builtin_amdgcn_update_dpp(0, (int)(v), ctl, 0xf, 0xf, false)
#pragma unroll
      for (int i = 0; i < 4; ++i) {
        t0[i] = __builtin_amdgcn_perm(QP(wd[2 * i + 1], 0x50), QP(wd[2 * i], 0x50), sel);
        t1[i] = __builtin_amdgcn_perm(QP(wd[2 * i + 1], 0xFA), QP(wd[2 * i], 0xFA), sel);
      }
#undef QP
      const int so = WS_WT + ((cg * 32 + tt16 * 2 + (c >> 1)) * 4 + (c & 1) * 2) * 1024;
      st16(t0, rw, lane * 16, so);
      st16(t1, rw, 1024 + lane * 16, so);
    }
    BAR();
    TS(4);
    if (lane < 8) *(LAS unsigned*)(ub + lane * 4) = 0u;
    {
      const int f1 = (q4 >> 1) | ((g4 & 1) << 1);
      const int po = (8 * g4 + q4) * 128 + (((2 * c + (p4 >> 1)) ^ (f1 << 1)) << 4) + (p4 & 1) * 8;
#pragma unroll
      for (int j = 0; j < 4; ++j) {
        LAS char* pp = ((j < 2) ? sm + 3 * SLOT + j * 4096 : sm + L_X + (j - 2) * 4096) + po;
        u32x4 v = tr_pair(pp, pp + 512);
        st16(v, rw, (16 * c + (lane & 15)) * 4096 + (32 * j + 8 * g4) * 2, WS_UT + cg * 64 * 4096 + tt16 * 256);
      }
    }
    BAR();
    LAS char* fbp = ub + 2 * (16 - 12 + 32 * rg + 4 * (q4 + p4));
#pragma unroll
    for (int s = 0; s < 16; ++s) F[s] = tr_frag(fbp + 256 * s, fbp + 256 * s + 32);
    CBAR(); __builtin_amdgcn_s_waitcnt(0xC07F); CBAR();
    BAR();
    TS(3);
    *(LAS unsigned*)(ub + lane * 4) = 0u;
    bool pend = true;
#define TRYF(force_) do { if (pend && ((force_) || (__builtin_amdgcn_s_getreg((31 << 11) | 7) & 0xC0000Fu) == 0u)) {      \
      WAITVM(0); gflag(ws, WS_FU + (bid * 4 + c) * 128, tag, lane); pend = false; TS(5); } } while (0)
    TRYF(false);
    BAR();
    TRYF(false);
    BAR();
    TRYF(false);
    BAR();
    TRYF(false);
    BAR();
    TRYF(true);
#undef TRYF
    char* fup = ws + WS_FU + (((g4 & 1) ? (nb * 16 + c16) * 4 + c : (cg * 16 + c16) * 4 + (tt16 >> 2)) << 7);
    fu = __hip_atomic_load((unsigned*)fup, __ATOMIC_RELAXED, __HIP_MEMORY_SCOPE_AGENT);
    BAR();
    BAR();
    if (!__all(fu == tag)) gpoll(fup, tag);
    TS(6);
#pragma unroll
    for (int i = 0; i < 4; ++i)
      __builtin_amdgcn_global_load_lds((const GAS unsigned*)(ws + WS_UT + d * 4096 + i * 1024 + lane * 16), (LAS unsigned*)(ub + 256 + i * 1024), 16, 0, SC1);
    BAR();
    BAR();
    BAR();
    waitge(FL + F_XP, 4);
    WAITVM(0);
  }
  TS(8); TC(2);
  unsigned yp[16];
  {
    LAS char* gb = ub + 2 * (128 + 16 * p4 + 12 - 32 * rg - 4 * q4);
    float pc1 = 0.f, pc2 = 0.f, pc3 = 0.f, yprev = 0.f;
    const bool l0 = (lane == 0);
#define ROR1(v) __int_as_float(__builtin_amdgcn_update_dpp(0, __float_as_int(v), 0x13C, 0xf, 0xf, false))
#define FIN(i)                                                                       \
  do {                                                                               \
    float r3 = ROR1(ca[i][3]);                                                       \
    float s2 = ca[i][2] + (l0 ? pc3 : r3);                                           \
    float r2 = ROR1(s2);                                                             \
    float s1 = ca[i][1] + (l0 ? pc2 : r2);                                           \
    float r1 = ROR1(s1);                                                             \
    float y = ca[i][0] + (l0 ? pc1 : r1);                                            \
    pc1 = r1; pc2 = r2; pc3 = r3;                                                    \
    if ((i) & 1) yp[8 * pass + ((i) >> 1)] = pk_bf16(yprev, y); else yprev = y;      \
  } while (0)
#pragma unroll
    for (int pass = 0; pass < 2; ++pass) {
      f32x4 ca[16];
#pragma unroll
      for (int i = 0; i < 16; ++i) ca[i] = (f32x4){0, 0, 0, 0};
      bf16x8 G = tr_frag(gb, gb - 32);
#pragma unroll
      for (int v = 0; v < 16 * pass + 16; ++v) {
        bf16x8 Gn = G;
        if (v + 1 < 16 * pass + 16) Gn = tr_frag(gb + 128 * (v + 1), gb + 128 * (v + 1) - 32);
        SB();
#pragma unroll
        for (int i = 0; i < 16; ++i) {
          const int T = 16 * pass + i;
          if (T >= v && ((T - v) & 1) == 0) ca[i] = MFMA16(G, F[(T - v) >> 1], ca[i]);
        }
        if (v - 1 >= 16 * pass) FIN(v - 1 - 16 * pass);
        SB();
        G = Gn;
      }
      FIN(15);
      CBAR();
      SB();
    }
#undef FIN
#undef ROR1
  }
  CBAR();
  TS(9); TC(3);
#pragma unroll
  for (int i = 0; i < 16; ++i) {
    ((LAS u16*)ub)[128 * i + lane] = (u16)yp[i];
    ((LAS u16*)ub)[128 * i + 64 + lane] = (u16)(yp[i] >> 16);
  }
  CBAR();
#pragma unroll
  for (int i = 0; i < 4; ++i) st16(*(LAS u32x4*)(ub + i * 1024 + lane * 16), rw, i * 1024 + lane * 16, WS_YT + d * 4096);
  WAITVM(0);
  if (sig_ret(FL + F_CY) == 3u) {
    gflag(ws, WS_FY + (cg * 16 + tt16) * 128, tag, lane);
    TS(10);
    gpoll(ws + WS_FY + ((cg * 16 + (lane & 15)) << 7), tag);
    sig(FL + F_Y);
  } else {
    TS(10);
    waitge(FL + F_Y, 1);
  }
  TS(11);
  {
    int ln2 = lane;
    asm volatile("" : "+v"(ln2));
    const int lane = ln2, q4 = (lane & 15) >> 2, p4 = lane & 3, g4 = lane >> 4;
    LAS char* tb = sm + L_UB;
    u32x4 yr[4];
#pragma unroll
    for (int i = 0; i < 4; ++i)
      yr[i] = ld16(rw, (16 * c + 4 * i + g4) * 4096 + (lane & 15) * 16, WS_YT + cg * 64 * 4096 + tt16 * 256);
#pragma unroll
    for (int i = 0; i < 4; ++i) {
      const int row = 16 * c + 4 * i + g4;
      const int f = (row & 3) | (((row >> 3) & 1) << 2);
      *(LAS u32x4*)(tb + row * 256 + (((lane & 15) ^ (f << 1)) << 4)) = yr[i];
    }
    sig(FL + F_T2);
    waitge(FL + F_T2, 4);
    const int f = q4 | ((g4 & 1) << 2);
#pragma unroll
    for (int j = 0; j < 4; ++j) {
      const int tbk = 2 * c + (j >> 1), hf = j & 1;
      LAS char* pp = tb + (32 * hf + 8 * g4 + q4) * 256 + (((2 * tbk + (p4 >> 1)) ^ (f << 1)) << 4) + (p4 & 1) * 8;
      u32x4 v = tr_pair(pp, pp + 1024);
      ost16(out + (size_t)(tt16 * 128 + 16 * tbk + (lane & 15)) * 2048 + 1024 + cg * 64 + 32 * hf + 8 * g4, v);
    }
  }
  TS(12);
  TSDUMP();
}

extern "C" void kernel_launch(void* const* d_in, const int* in_sizes, int n_in, void* d_out, int out_size,
                              void* d_ws, size_t ws_size, hipStream_t stream) {
  fused_kernel<<<256, 768, 0, stream>>>((const u16*)d_in[0], (const u16*)d_in[1], (const u16*)d_in[2], (const u16*)d_in[3],
                                        (const u16*)d_in[4], (const u16*)d_in[5], (const u16*)d_in[6], (const u16*)d_in[7],
                                        (const u16*)d_in[8], (const u16*)d_in[9], (u16*)d_out, (char*)d_ws);
}
```

```cpp
#include <hip/hip_runtime.h>
#include <stdint.h>

#define LAS __attribute__((address_space(3)))
#define GAS __attribute__((address_space(1)))
#define DI __device__ __forceinline__

typedef unsigned short u16;
typedef short s16x4 __attribute__((ext_vector_type(4)));
typedef __bf16 bf16x2 __attribute__((ext_vector_type(2)));
typedef __bf16 bf16x8 __attribute__((ext_vector_type(8)));
typedef _Float16 f16x2 __attribute__((ext_vector_type(2)));
typedef _Float16 f16x8 __attribute__((ext_vector_type(8)));
typedef float f32x2 __attribute__((ext_vector_type(2)));
typedef float f32x4 __attribute__((ext_vector_type(4)));
typedef float f32x16 __attribute__((ext_vector_type(16)));
typedef unsigned u32x2 __attribute__((ext_vector_type(2)));
typedef unsigned u32x4 __attribute__((ext_vector_type(4)));
typedef __amdgpu_buffer_rsrc_t rsrc_t;

#define CBAR() asm volatile("" ::: "memory")
#define SB() __builtin_amdgcn_sched_barrier(0)
#define WAITVM(n) asm volatile("s_waitcnt vmcnt(" #n ")" ::: "memory")
#define WAITVMX(n) WAITVM(n)
#define WAITVL(n) asm volatile("s_waitcnt vmcnt(" #n ") lgkmcnt(0)" ::: "memory")
#define BAR() asm volatile("s_barrier" ::: "memory")
#define LBAR() do { CBAR(); __builtin_amdgcn_s_waitcnt(0xC07F); asm volatile("s_barrier" ::: "memory"); } while (0)
#define TSDECL
#define TS(i)
#define TC(i)
#define TSDUMP()
#define KOFF ((ml & 3) * 3 + 4 * (xcd >> 1) + 8 * (xcd & 1))
#define KXOFF (4 * (xcd >> 1) + 8 * (xcd & 1))
#define PRIO_C 2
#define PRIO_L 3
#define PSLEEP 3
#define CODE_LEN 33920u
#define PFM 15
#define EBAR
#define WT 2
#define NRING 12

DI void ost16(void* p, u32x4 v) { asm volatile("global_store_dwordx4 %0, %1, off sc1\n\ts_nop 1" :: "v"(p), "v"(v) : "memory"); }
DI float sum16(float x) {
  auto r = __builtin_amdgcn_permlane16_swap(__float_as_uint(x), __float_as_uint(x), false, false);
  float s = __uint_as_float(r[0]) + __uint_as_float(r[1]);
  asm("" : "+v"(s));
  return s;
}
DI float sum32(float x) {
  auto r = __builtin_amdgcn_permlane32_swap(__float_as_uint(x), __float_as_uint(x), false, false);
  float s = __uint_as_float(r[0]) + __uint_as_float(r[1]);
  asm("" : "+v"(s));
  return s;
}
DI float sum8(float x) {
  return x + __int_as_float(__builtin_amdgcn_update_dpp(0, __float_as_int(x), 0x128, 0xf, 0xf, false));
}
DI float wave_max(float x) {
#define DPPMAX(ctl) x = __builtin_fmaxf(x, __int_as_float(__builtin_amdgcn_update_dpp(0, __float_as_int(x), ctl, 0xf, 0xf, false)))
  DPPMAX(0xB1); DPPMAX(0x4E); DPPMAX(0x141); DPPMAX(0x140);
#undef DPPMAX
  auto r = __builtin_amdgcn_permlane16_swap(__float_as_uint(x), __float_as_uint(x), false, false);
  x = __builtin_fmaxf(__uint_as_float(r[0]), __uint_as_float(r[1]));
  r = __builtin_amdgcn_permlane32_swap(__float_as_uint(x), __float_as_uint(x), false, false);
  return __builtin_fmaxf(__uint_as_float(r[0]), __uint_as_float(r[1]));
}
DI int lane_id() {
  int l = __builtin_amdgcn_mbcnt_hi(~0u, __builtin_amdgcn_mbcnt_lo(~0u, 0u));
  asm volatile("" : "+v"(l));
  return l;
}
DI float bf2f(u16 v) { return __uint_as_float((unsigned)v << 16); }
DI unsigned pk_bf16(float a, float b) {
  f32x2 f = {a, b};
  return __builtin_bit_cast(unsigned, __builtin_convertvector(f, bf16x2));
}
DI unsigned pk_f16(float a, float b) {
  f32x2 f = {a, b};
  return __builtin_bit_cast(unsigned, __builtin_convertvector(f, f16x2));
}
DI float rbf(float a) { return __uint_as_float(pk_bf16(a, 0.f) << 16); }
DI u32x2 tr_read(LAS char* p) {
  s16x4 v = __builtin_amdgcn_ds_read_tr16_b64_v4i16((LAS s16x4*)p);
  return __builtin_bit_cast(u32x2, v);
}
DI u32x4 join(u32x2 a, u32x2 b) { u32x4 r = {a.x, a.y, b.x, b.y}; return r; }
DI u32x4 tr_pair(LAS char* p0, LAS char* p1) { return join(tr_read(p0), tr_read(p1)); }
DI bf16x8 asbf(u32x4 v) { return __builtin_bit_cast(bf16x8, v); }
DI bf16x8 tr_frag(LAS char* p0, LAS char* p1) { return asbf(tr_pair(p0, p1)); }
#define MFMA32(a, b, c) __builtin_amdgcn_mfma_f32_32x32x16_bf16((a), (b), (c), 0, 0, 0)
#define MFMA16(a, b, c) __builtin_amdgcn_mfma_f32_16x16x32_bf16((a), (b), (c), 0, 0, 0)
#define MFMA16H(a, b, c) __builtin_amdgcn_mfma_f32_16x16x32_f16((a), (b), (c), 0, 0, 0)
#define AMFMA16(c, a, b) asm volatile("v_mfma_f32_16x16x32_bf16 %0, %1, %2, %0" : "+v"(c) : "v"(a), "v"(b))
#define GLDS(src, dst) __builtin_amdgcn_global_load_lds((const GAS unsigned*)(src), (LAS unsigned*)(dst), 16, 0, 0)
#define XAUX 0
#define WAUX 0
#define UAUX 2
#define GLDSA(src, dst, aux) __builtin_amdgcn_global_load_lds((const GAS unsigned*)(src), (LAS unsigned*)(dst), 16, 0, aux)
#define SC1 16
DI u32x4 ld16(rsrc_t r, int voff, int soff) { return __builtin_amdgcn_raw_buffer_load_b128(r, voff, soff, SC1); }
DI void st16(u32x4 v, rsrc_t r, int voff, int soff) {
  __builtin_amdgcn_raw_buffer_store_b128(v, r, voff, soff, SC1);
  asm volatile("s_nop 1" :: "v"(v) : "memory");
}

constexpr int WS_EPOCH = 0;
constexpr int WS_FY = 65536, WS_FU = 131072, WS_FP = WS_FU + 131072;
constexpr int WS_O = 524288;
constexpr int WS_UT = WS_O + (4 << 20);
constexpr int WS_YT = WS_UT + (4 << 20);
constexpr int WS_WT = WS_YT + (4 << 20);
constexpr int WS_KVP = WS_WT + (2 << 20);
constexpr int WS_ZP = WS_KVP + 16 * 16 * 16 * 512;
constexpr int WS_KVR = WS_ZP + 32 * 16 * 128;
constexpr int WS_END = WS_KVR + 32 * 4096;

constexpr int SLOT = 40960;
constexpr int L_UB = 131072, UB_CH = 4352;
constexpr int L_X = L_UB + 4 * UB_CH;
constexpr int L_Z = L_X + 4 * 2176;
constexpr int L_FLAG = L_Z + 512;
constexpr int LDS_BYTES = L_FLAG + 256;
enum { F_XE = 8, F_LW = 11, F_CY, F_T2, F_XP, F_Z = 24, F_Y = 26 };

typedef f32x2 cplx;
DI cplx cmul(cplx a, cplx b) {
  cplx t, r;
  asm("v_pk_mul_f32 %0, %1, %2 op_sel:[1,1] op_sel_hi:[1,0] neg_lo:[1,0]" : "=v"(t) : "v"(a), "v"(b));
  asm("v_pk_fma_f32 %0, %1, %2, %3 op_sel:[0,0,0] op_sel_hi:[0,1,1]" : "=v"(r) : "v"(a), "v"(b), "v"(t));
  return r;
}
DI cplx zpow(float arl2, float ai, float p) {
  const float INV2PI = 0.15915494309189535f;
  const float INV2PI_LO = (float)(0.15915494309189533576888 - (double)0.15915494309189535f);
  float e = __builtin_amdgcn_exp2f(arl2 * p);
  float r = ai * p;
  float hi = r * INV2PI;
  float lo = __builtin_fmaf(r, INV2PI_LO, __builtin_fmaf(r, INV2PI, -hi));
  float f = (hi - __builtin_rintf(hi)) + lo;
  cplx z = {e * __builtin_amdgcn_cosf(f), e * __builtin_amdgcn_sinf(f)};
  return z;
}
template <bool NOPK> DI float phi_f(float t) {
  float a = t * 1.4426950408889634f, b = t + 1.f;
  if (NOPK) { asm("" : "+v"(a)); asm("" : "+v"(b)); }
  float r = __builtin_amdgcn_fmed3f(__builtin_amdgcn_exp2f(a), b, 1.f) + 1e-6f;
  if (NOPK) asm("" : "+v"(r));
  return r;
}
DI u32x4 tag_put(f32x4 v, unsigned tag) {
  u32x4 b = __builtin_bit_cast(u32x4, v), r;
  r[0] = __builtin_amdgcn_perm(b[0], tag, 0x07060500u);
  r[1] = __builtin_amdgcn_perm(b[1], tag, 0x07060501u);
  r[2] = __builtin_amdgcn_perm(b[2], tag, 0x07060502u);
  r[3] = __builtin_amdgcn_perm(b[3], tag, 0x07060503u);
  return r;
}
DI unsigned tag_get(u32x4 b) {
  unsigned lo = __builtin_amdgcn_perm(b[1], b[0], 0x0c0c0400u), hi = __builtin_amdgcn_perm(b[3], b[2], 0x04000c0cu);
  return lo | hi;
}
DI f32x4 tag_val(u32x4 b) { return __builtin_bit_cast(f32x4, b & 0xffffff00u); }

DI void sig(LAS unsigned* f) {
  asm volatile("s_mov_b64 exec, 1\n\tds_add_u32 %0, %1\n\ts_mov_b64 exec, -1" :: "v"((unsigned)(size_t)f), "v"(1u) : "memory");
}
DI unsigned sig_ret(LAS unsigned* f) {
  unsigned r;
  asm volatile("s_mov_b64 exec, 1\n\tds_add_rtn_u32 %0, %1, %2\n\ts_mov_b64 exec, -1\n\ts_waitcnt lgkmcnt(0)"
               : "=&v"(r) : "v"((unsigned)(size_t)f), "v"(1u) : "memory");
  return __builtin_amdgcn_readfirstlane(r);
}
DI unsigned ldsword(LAS unsigned* f) { return __builtin_amdgcn_readfirstlane(*(volatile LAS unsigned*)f); }
DI void waitge(LAS unsigned* f, unsigned n) {
  CBAR();
#pragma unroll 1
  for (int it = 0; it < (1 << 22); ++it) {
    if (ldsword(f) >= n) break;
    __builtin_amdgcn_s_sleep(1);
  }
  CBAR();
}
DI void gflag(char* ws, int off, unsigned tag, int lane) {
  CBAR();
  if (lane == 0) __hip_atomic_store((unsigned*)(ws + off), tag, __ATOMIC_RELAXED, __HIP_MEMORY_SCOPE_AGENT);
  CBAR();
}
DI void gpoll(char* p, unsigned tag) {
  CBAR();
  unsigned v = ~tag;
#pragma unroll 1
  for (int it = 0; it < (1 << 18); ++it) {
    if (v != tag) v = __hip_atomic_load((unsigned*)p, __ATOMIC_RELAXED, __HIP_MEMORY_SCOPE_AGENT);
    if (__all(v == tag)) break;
    __builtin_amdgcn_s_sleep(PSLEEP);
  }
  CBAR();
}

__global__ __launch_bounds__(768, 3) void fused_kernel(const u16* __restrict__ x, const u16* __restrict__ wqkv,
                                                       const u16* __restrict__ wo, const u16* __restrict__ u,
                                                       const u16* __restrict__ lar, const u16* __restrict__ aim,
                                                       const u16* __restrict__ cre, const u16* __restrict__ cim,
                                                       const u16* __restrict__ ldt, const u16* __restrict__ dsk,
                                                       u16* __restrict__ out, char* __restrict__ ws) {
  __shared__ __attribute__((aligned(16))) char smem[LDS_BYTES];
  LAS char* sm = (LAS char*)smem;
  LAS unsigned* FL = (LAS unsigned*)(sm + L_FLAG);
  const int tid = threadIdx.x, lane = tid & 63;
  const int wave = __builtin_amdgcn_readfirstlane(tid >> 6);
  const int bid = blockIdx.x, xcd = bid & 7, idx = bid >> 3;
  const int ml = idx & 7, hl = idx >> 3;
  const int mb = (xcd & 1) * 8 + ml, hp = (xcd >> 1) * 4 + hl;
  const int mb2 = (xcd & 3) * 4 + (idx & 3), nb = (xcd >> 2) * 8 + (idx >> 2);
  const rsrc_t rw = __builtin_amdgcn_make_buffer_rsrc((void*)ws, 0, WS_END, 0x00020000);
  TSDECL; TS(0);
  unsigned pf = 0u;
  {
    const unsigned po = (unsigned)(tid * 64 + (idx & PFM) * 4096) | (tid >= 64 ? 0x40000000u : 0u);
    if (po < CODE_LEN) asm volatile("global_load_dword %0, %1, %2" : "+v"(pf) : "v"(po), "s"(__builtin_amdgcn_s_getpc()) : "memory");
  }
#define PFDONE() asm volatile("" :: "v"(pf))
  asm volatile("" :: "s"(x), "s"(wqkv), "s"(wo), "s"(u), "s"(lar), "s"(aim), "s"(cre), "s"(cim), "s"(ldt), "s"(dsk), "s"(out), "s"(ws));
  unsigned tag;
#define TAGLD() asm volatile("s_load_dword %0, %1, 0x0" : "=s"(tag) : "s"(ws) : "memory")
#define TAGRD() do { asm volatile("s_waitcnt lgkmcnt(0)" : "+s"(tag) :: "memory"); tag = tag * 2654435761u + 0x9E3779B1u; } while (0)
  const int q4 = (lane & 15) >> 2, p4 = lane & 3, g4 = lane >> 4, c16 = lane & 15;

#define LROLE 2
  if ((wave >> 2) == LROLE) {
    __builtin_amdgcn_s_setprio(PRIO_L);
    const int hw = wave & 3;
    const int koff = KOFF;
    const unsigned vx0 = (unsigned)((mb * 128 + hw * 32 + (lane >> 3)) * 2048) + (((lane & 7) ^ (lane >> 4)) << 4), vx1 = vx0 ^ 64;
    const unsigned vw0 = (unsigned)((lane >> 3) * 6144 + hp * 128 + (((lane & 7) ^ (((lane >> 4) & 1) << 1)) << 4)), vw1 = vw0 ^ 64;
#define ISSUE(tt_, slot_)                                                                                  \
  do {                                                                                                     \
    const int k_ = ((tt_) + koff) & 15;                                                                    \
    const char* xa_ = (const char*)x + k_ * 128;                                                           \
    const char* wa_ = (const char*)wqkv + (size_t)k_ * 393216;                                             \
    LAS char* sb_ = sm + (slot_) * SLOT;                                                                   \
    _Pragma("unroll") for (int i = 0; i < 4; ++i)                                                          \
        GLDSA(xa_ + i * 16384 + ((i & 1) ? vx1 : vx0), sb_ + (hw * 4 + i) * 1024, XAUX);                   \
    _Pragma("unroll") for (int i = 0; i < 6; ++i) {                                                        \
      const int qq_ = hw * 6 + i;                                                                          \
      GLDSA(wa_ + ((qq_ & 7) * 49152 + (qq_ >> 3) * 2048) + ((i & 1) ? vw1 : vw0), sb_ + 16384 + qq_ * 1024, WAUX); \
    }                                                                                                      \
  } while (0)
    BAR();
    const int kx = KXOFF;
    const unsigned l24 = idx * 24 + lane, l6 = idx * 24 + hw * 6 + lane;
    const unsigned t24 = (l24 >> 2) * 2048 + (l24 & 3) * 128 + (xcd >> 1) * 512, t6 = (l6 >> 2) * 2048 + (l6 & 3) * 128 + (xcd >> 1) * 512;
#define TOUCH(j_, n_, o_) do { if (lane < (n_)) __builtin_amdgcn_global_load_lds((const GAS unsigned*)((const char*)wqkv + (size_t)(((j_) + kx) & 15) * 393216 + (o_)), (LAS unsigned*)(sm + L_X + 8192), 4, 0, 0); } while (0)
    TOUCH(hw, 24, t24);
    ISSUE(0, 0);
    TS(13);
    ISSUE(1, 1);
    TOUCH(4 + hw, 24, t24);
    ISSUE(2, 2);
    TOUCH(8 + hw, 24, t24);
    TS(14);
    TAGLD();
    WAITVM(22);
    PFDONE();
    TAGRD();
    TS(1);
    BAR();
    {
      int sf = 0;
#pragma unroll 1
      for (int t = 0; t < 13; ++t) {
        WAITVM(12);
        BAR();
        ISSUE(t + 3, sf);
        TOUCH(t + (WT == 1 ? 9 : 12), 6, t6);
        sf = (sf == 2) ? 0 : sf + 1;
      }
    }
#define W13 12
#define W14 11
#undef ISSUE
    TS(2);
    {
      const char* src = ws + WS_WT + nb * 131072 + hw * 1024 + lane * 16;
#define WOP(s_) __builtin_amdgcn_global_load_lds((const GAS unsigned*)(src + (s_) * 4096), (LAS unsigned*)(sm + ((s_) * 4 + hw) * 1024), 16, 0, SC1)
      WAITVMX(W13);
      BAR();
#pragma unroll
      for (int i = 10; i < 20; ++i) WOP(i);
      WAITVMX(W14);
      BAR();
#pragma unroll
      for (int i = 20; i < 32; ++i) WOP(i);
      BAR();
#pragma unroll
      for (int i = 0; i < 10; ++i) WOP(i);
#undef WOP
      TS(3);
      WAITVM(0);
      sig(FL + F_LW);
    }
    TS(4);
    waitge(FL + F_XP, 4);
    {
      const int so = WS_KVP + (hp * 16 + mb) * 8192;
      const int vo = (lane >> 3) * 512 + (hw * 8 + (lane & 7)) * 16;
      u32x4 a = {0u, 0u, 0u, 0u}, b = a;
      bool na = true, nb_ = true;
#pragma unroll 1
      for (int it = 0; it < (1 << 17); ++it) {
        CBAR();
        if (na) a = ld16(rw, vo, so);
        if (nb_) b = ld16(rw, vo + 4096, so);
        na = tag_get(a) != tag; nb_ = tag_get(b) != tag;
        if (!__any(na || nb_)) break;
        __builtin_amdgcn_s_sleep(PSLEEP);
      }
      TS(5);
      const f32x4 fa = tag_val(a), fb = tag_val(b);
      float s[4];
#pragma unroll
      for (int i = 0; i < 4; ++i) { s[i] = fa[i] + fb[i]; asm("" : "+v"(s[i])); }
#pragma unroll
      for (int i = 0; i < 4; ++i) s[i] = sum32(sum16(sum8(s[i])));
      const unsigned p0 = pk_bf16(s[0], s[1]), p1 = pk_bf16(s[2], s[3]);
      const u32x4 w = {__builtin_amdgcn_perm(p0, tag, 0x05040100u), __builtin_amdgcn_perm(p0, tag, 0x07060302u),
                       __builtin_amdgcn_perm(p1, tag, 0x05040100u), __builtin_amdgcn_perm(p1, tag, 0x07060302u)};
      if (lane < 8) st16(w, rw, lane * 16, WS_KVR + hp * 8192 + mb * 512 + hw * 128);
      TS(6);
    }
    TS(9);
    TSDUMP();
    return;
  }

  const int c = wave & 3, d = bid * 4 + c;
  LAS char* ub = sm + L_UB + c * UB_CH;
  LAS char* kc = (c < 2) ? sm + 3 * SLOT + c * 4096 : sm + L_X + (c - 2) * 4096;
  f32x4 ta = {0, 0, 0, 0}, tb = {0, 0, 0, 0}, tc_ = {0, 0, 0, 0};
  float v_dsk = bf2f(dsk[d]);
  {
    const int pi = d * 128 + (wave < 4 ? 64 : 0) + lane;
    const float lr = bf2f(lar[pi]), aiv = bf2f(aim[pi]), cr = bf2f(cre[pi]), ci = bf2f(cim[pi]);
    const float dt = rbf(expf(bf2f(ldt[d])));
    LAS char* ks = (wave < 4) ? kc : ub;
    TAGLD();
    BAR();
    if (wave == 0 && lane < 32) FL[lane] = 0u;
    const float mx = wave_max(__builtin_fmaxf(__builtin_fabsf(cr), __builtin_fabsf(ci)));
    const int ex = (mx > 0.f && mx < 3.0e38f) ? __builtin_amdgcn_frexp_expf(mx) : 0;
    const float mult = __builtin_amdgcn_ldexpf(2.f * dt, ex);
    asm volatile("" : "+v"(v_dsk));
    PFDONE();
    TAGRD();
    TS(15);
    const float ar = -rbf(expf(lr)) * dt;
    const float ai = aiv * dt;
    const float arl2 = ar * 1.4426950408889634f;
    const cplx z1 = zpow(arl2, ai, 1.f), z8 = zpow(arl2, ai, 8.f), z128 = zpow(arl2, ai, 128.f);
    LAS char* wp = ks + lane * 64 + (((lane >> 1) & 3) << 4);
    const int h0 = g4 & 1;
    LAS char* rp0 = ks + (g4 * 256 + h0 * 128 + (q4 >> 1) * 64 + (((2 * (q4 & 1) + (p4 >> 1)) ^ ((2 * g4 + h0) & 3)) << 4) + (p4 & 1) * 8);
    LAS char* rp1 = ks + ((unsigned)(rp0 - ks) ^ 144u);
#define KW(v, sg)                                                                                                    \
  _Pragma("unroll") for (int hh = 0; hh < 2; ++hh) {                                                                 \
    u32x4 wr_, wi_;                                                                                                  \
    _Pragma("unroll") for (int i = 0; i < 4; ++i) {                                                                  \
      wr_[i] = pk_f16(v[8 * hh + 2 * i][0], v[8 * hh + 2 * i + 1][0]);                                               \
      wi_[i] = pk_f16(sg v[8 * hh + 2 * i][1], sg v[8 * hh + 2 * i + 1][1]);                                         \
    }                                                                                                                \
    *(LAS u32x4*)(ks + ((unsigned)(wp - ks) ^ (unsigned)(hh << 4))) = wr_;                                           \
    *(LAS u32x4*)(ks + ((unsigned)(wp - ks) ^ (unsigned)((2 + hh) << 4))) = wi_;                                     \
  }
#define KR(ks_) __builtin_bit_cast(f16x8, tr_pair(rp0 + (ks_) * 1024, rp1 + (ks_) * 1024))
    f16x8 bfr[4];
    cplx v[16], X[17], zp[9];
    v[0] = (cplx){1.f, 0.f};
    v[1] = z8;
    v[2] = cmul(z8, z8);
#pragma unroll
    for (int k = 3; k < 16; ++k) v[k] = cmul(v[k - 2], v[2]);
    KW(v, -);
    X[0] = (cplx){__builtin_amdgcn_ldexpf(cr, -ex), __builtin_amdgcn_ldexpf(ci, -ex)};
    X[1] = cmul(X[0], z128);
    {
      const cplx z256 = cmul(z128, z128);
#pragma unroll
      for (int r = 2; r < 17; ++r) X[r] = cmul(X[r - 2], z256);
    }
    zp[1] = z1; zp[8] = z8;
    zp[2] = cmul(z1, z1); zp[3] = cmul(zp[2], z1); zp[4] = cmul(zp[2], zp[2]);
    zp[5] = cmul(zp[4], z1); zp[6] = cmul(zp[4], zp[2]); zp[7] = cmul(zp[4], zp[3]);
#pragma unroll
    for (int k = 0; k < 4; ++k) bfr[k] = KR(k);
#pragma unroll
    for (int g = 0; g < 4; ++g) { v[4 * g] = X[4 + g]; v[4 * g + 1] = cmul(X[4 + g], zp[4]); v[4 * g + 2] = X[8 + g]; v[4 * g + 3] = X[12 + g]; }
    KW(v, );
#pragma unroll
    for (int j = 0; j < 16; ++j) v[j] = (j & 7) ? cmul(X[j >> 3], zp[j & 7]) : X[j >> 3];
    {
      f16x8 a[4];
#pragma unroll
      for (int k = 0; k < 4; ++k) a[k] = KR(k);
      KW(v, );
#pragma unroll
      for (int k = 0; k < 4; ++k) ta = MFMA16H(a[k], bfr[k], ta);
    }
#pragma unroll
    for (int a = 0; a < 2; ++a) {
      v[8 * a] = X[2 + a];
      v[8 * a + 1] = cmul(X[2 + a], zp[2]);
      v[8 * a + 2] = v[8 * a + 4] = cmul(X[2 + a], zp[4]);
      v[8 * a + 5] = cmul(X[2 + a], zp[6]);
      v[8 * a + 6] = cmul(X[2 + a], zp[8]);
      v[8 * a + 3] = v[8 * a + 7] = X[16];
    }
    {
      f16x8 a[4];
#pragma unroll
      for (int k = 0; k < 4; ++k) a[k] = KR(k);
      KW(v, );
#pragma unroll
      for (int k = 0; k < 4; ++k) tc_ = MFMA16H(a[k], bfr[k], tc_);
    }
#pragma unroll
    for (int k = 0; k < 4; ++k) { f16x8 a = KR(k); tb = MFMA16H(a, bfr[k], tb); }
#undef KW
#undef KR
    ta *= mult; tb *= mult; tc_ *= mult;
    TS(2);
  }

  if (wave < 4) {
    *(LAS f32x4*)(kc + lane * 16) = ta;
    *(LAS f32x4*)(kc + 1024 + lane * 16) = tb;
    *(LAS f32x4*)(kc + 2048 + lane * 16) = tc_;
    __builtin_amdgcn_s_setprio(PRIO_C);
    const int mh = wave & 1, hd = wave >> 1;
    const int a0 = (mh * 64 + c16) * 128 + ((g4 ^ (c16 >> 1)) << 4);
    const int b0 = 16384 + (8 * g4 + q4) * 128 + (((hd * 4 + (p4 >> 1)) ^ (((q4 >> 1) | ((g4 & 1) << 1)) << 1)) << 4) + (p4 & 1) * 8;
    f32x4 acc[4][6];
#pragma unroll
    for (int m = 0; m < 4; ++m)
#pragma unroll
      for (int g = 0; g < 6; ++g) acc[m][g] = (f32x4){0.f, 0.f, 0.f, 0.f};
#pragma unroll
    for (int m = 0; m < 4; ++m)
      asm volatile("" : "+v"(acc[m][0]), "+v"(acc[m][1]), "+v"(acc[m][2]), "+v"(acc[m][3]), "+v"(acc[m][4]), "+v"(acc[m][5]));
    bf16x8 fa[2][4];
    u32x2 bl[6], bh[6];
#define RA(set, m, pa) fa[set][m] = *(LAS bf16x8*)((pa) + (m) * 2048)
#define RL(j, pe, po) bl[(j) % 6] = tr_read((((j) & 1) ? (po) : (pe)) + (((j) % 6) >> 1) * 8192 + ((j) / 6) * 4096)
#define RH(j, pe, po) bh[(j) % 6] = tr_read((((j) & 1) ? (po) : (pe)) + (((j) % 6) >> 1) * 8192 + ((j) / 6) * 4096 + 512)
#define FB(j) asbf(join(bl[(j) % 6], bh[(j) % 6]))
#define MM(j, m) acc[m][(j) % 6] = ((j) % 6 < 2) ? MFMA16(FB(j), fa[(j) / 6][m], acc[m][(j) % 6]) : MFMA16(fa[(j) / 6][m], FB(j), acc[m][(j) % 6])
#define GRP(j, X0, X1, X2, X3)                \
  do {                                        \
    MM(j, 0); X0; SB(); MM(j, 1); X1; SB();   \
    MM(j, 2); X2; SB(); MM(j, 3); X3; SB();   \
  } while (0)
    LAS char* pa = sm + a0;
    LAS char* pa1 = sm + (a0 ^ 64);
    LAS char* pe = sm + b0;
    LAS char* po = sm + (b0 ^ 32);
    LBAR();
    TS(3); TC(0);
    RA(0, 0, pa); RL(0, pe, po); RH(0, pe, po); RA(0, 1, pa); RA(0, 2, pa); RA(0, 3, pa);
    RL(1, pe, po); RH(1, pe, po); RL(2, pe, po); RH(2, pe, po);
    {
      int slot = 0;
#pragma unroll 1
      for (int t = 0; t < 16; ++t) {
        const int dl = (slot == 2) ? -2 * SLOT : SLOT;
        slot = (slot == 2) ? 0 : slot + 1;
        SB();
        GRP(0, RL(3, pe, po), RH(3, pe, po), , );
        GRP(1, RL(4, pe, po), RH(4, pe, po), , );
        GRP(2, RL(5, pe, po), RH(5, pe, po), RA(1, 0, pa1), RA(1, 1, pa1));
        GRP(3, RL(6, pe, po), RH(6, pe, po), RA(1, 2, pa1), RA(1, 3, pa1));
        GRP(4, RL(7, pe, po), RH(7, pe, po), RL(8, pe, po), RH(8, pe, po));
        GRP(5, RL(9, pe, po), RH(9, pe, po), RL(10, pe, po), RH(10, pe, po));
        GRP(6, RL(11, pe, po), RH(11, pe, po), , );
        GRP(7, , , , );
        GRP(8, pa += dl, pa1 += dl, pe += dl, po += dl);
        asm volatile("" : "+v"(pa), "+v"(pa1), "+v"(pe), "+v"(po));
        LBAR();
        SB();
        GRP(9, RA(0, 0, pa), RL(0, pe, po), RH(0, pe, po), RA(0, 1, pa));
        GRP(10, RA(0, 2, pa), RA(0, 3, pa), RL(1, pe, po), RH(1, pe, po));
        GRP(11, RL(2, pe, po), RH(2, pe, po), , );
      }
    }
#undef RA
#undef RL
#undef RH
#undef FB
#undef MM
#undef GRP
    TS(4); TC(1);
    {
    const int lane = lane_id(), g4 = lane >> 4, c16 = lane & 15;
    const int head = hp * 2 + hd;
    {
      f32x4 kvt[2][2], zt[2];
#pragma unroll
      for (int i = 0; i < 2; ++i) { kvt[i][0] = kvt[i][1] = zt[i] = (f32x4){0.f, 0.f, 0.f, 0.f}; }
      const u32x4 ones = {0x3F803F80u, 0x3F803F80u, 0x3F803F80u, 0x3F803F80u};
#pragma unroll
      for (int j = 0; j < 2; ++j) {
        u32x4 fk[2], fv[2];
#pragma unroll
        for (int n = 0; n < 2; ++n)
#pragma unroll
          for (int i = 0; i < 4; ++i) {
            const f32x4 kk = acc[2 * j + (i >> 1)][2 + n], vv = acc[2 * j + (i >> 1)][4 + n];
            fk[n][i] = pk_bf16(phi_f<false>(kk[2 * (i & 1)]), phi_f<false>(kk[2 * (i & 1) + 1]));
            fv[n][i] = pk_bf16(vv[2 * (i & 1)], vv[2 * (i & 1) + 1]);
          }
#pragma unroll
        for (int td = 0; td < 2; ++td) {
          kvt[0][td] = MFMA16(asbf(fk[td]), asbf(fv[0]), kvt[0][td]);
          kvt[1][td] = MFMA16(asbf(fk[td]), asbf(fv[1]), kvt[1][td]);
          zt[td] = MFMA16(asbf(ones), asbf(fk[td]), zt[td]);
        }
      }
      LAS char* xo = sm + L_X + wave * 2176;
      LAS char* xi = sm + L_X + (wave ^ 1) * 2176;
      f32x4 keep[2];
#pragma unroll
      for (int td = 0; td < 2; ++td) {
        f32x4 ka = kvt[0][td], kb = kvt[1][td];
        asm("" : "+v"(ka), "+v"(kb));
        *(LAS f32x4*)(xo + td * 1024 + lane * 16) = mh ? ka : kb;
        keep[td] = mh ? kb : ka;
      }
      *(LAS float*)(xo + 2048 + (lane & 31) * 4) = (g4 & 1) ? zt[1][0] : zt[0][0];
      sig(FL + F_XE + hd);
      waitge(FL + F_XE + hd, 2);
      if (mh == 0) {
        f32x4 zz = *(LAS f32x4*)(xo + 2048 + (lane & 7) * 16) + *(LAS f32x4*)(xi + 2048 + (lane & 7) * 16);
        if (lane < 8) st16(tag_put(zz, tag), rw, lane * 16, WS_ZP + (head * 16 + mb) * 128);
      }
      const int so = WS_KVP + (hp * 16 + hd * 8 + mh * 4) * 8192 + mb * 512;
#pragma unroll
      for (int td = 0; td < 2; ++td) {
        f32x4 o = *(LAS f32x4*)(xi + td * 1024 + lane * 16) + keep[td];
        st16(tag_put(o, tag), rw, td * 16384 + (lane >> 5) * 8192 + (lane & 31) * 16, so);
      }
      sig(FL + F_XP);
    }
    TS(5);
#pragma unroll
    for (int m = 0; m < 4; ++m)
#pragma unroll
      for (int i = 0; i < 8; ++i) acc[m][i >> 2][i & 3] = phi_f<true>(acc[m][i >> 2][i & 3]);
    asm volatile("" : "+v"(acc[0][0]), "+v"(acc[1][0]), "+v"(acc[2][0]), "+v"(acc[3][0]),
                      "+v"(acc[0][1]), "+v"(acc[1][1]), "+v"(acc[2][1]), "+v"(acc[3][1]));
    u32x4 fq[4];
    {
      LAS f32x4* zl = (LAS f32x4*)(sm + L_Z + hd * 128);
      if (mh == 1) {
        u32x4 za = {0u, 0u, 0u, 0u}, zb = za;
        bool na = true, nb_ = true;
#pragma unroll 1
        for (int it = 0; it < (1 << 17); ++it) {
          CBAR();
          if (na) za = ld16(rw, lane * 16, WS_ZP + head * 2048);
          if (nb_) zb = ld16(rw, 1024 + lane * 16, WS_ZP + head * 2048);
          na = tag_get(za) != tag; nb_ = tag_get(zb) != tag;
          if (!__any(na || nb_)) break;
          __builtin_amdgcn_s_sleep(PSLEEP);
        }
        const f32x4 fa_ = tag_val(za), fb_ = tag_val(zb);
        f32x4 z;
#pragma unroll
        for (int i = 0; i < 4; ++i) { float s = fa_[i] + fb_[i]; asm("" : "+v"(s)); z[i] = sum32(sum16(sum8(s))); }
        if (lane < 8) zl[lane] = z;
        sig(FL + F_Z + hd);
      } else {
        waitge(FL + F_Z + hd, 1);
      }
      TS(6);
      const f32x4 z0 = zl[g4], z1 = zl[4 + g4];
#pragma unroll
      for (int m = 0; m < 4; ++m) {
        float d0 = acc[m][0][0] * z0[0], d1 = acc[m][1][0] * z1[0];
#pragma unroll
        for (int i = 1; i < 4; ++i) { d0 = __builtin_fmaf(acc[m][0][i], z0[i], d0); d1 = __builtin_fmaf(acc[m][1][i], z1[i], d1); }
        const float den = sum32(sum16(d0 + d1));
        const float rc = __builtin_amdgcn_rcpf(den + 1e-6f);
        float e[8];
#pragma unroll
        for (int i = 0; i < 8; ++i) { e[i] = acc[m][i >> 2][i & 3] * rc; asm("" : "+v"(e[i])); }
        fq[m] = (u32x4){pk_bf16(e[0], e[1]), pk_bf16(e[2], e[3]), pk_bf16(e[4], e[5]), pk_bf16(e[6], e[7])};
      }
    }
    asm volatile("" : "+v"(fq[0]), "+v"(fq[1]), "+v"(fq[2]), "+v"(fq[3]));
    TS(13);
    {
      u32x4 k[4];
      bool nk[4];
#pragma unroll
      for (int i = 0; i < 4; ++i) { k[i] = (u32x4){0u, 0u, 0u, 0u}; nk[i] = true; }
      const unsigned t0 = tag & 0xffffu, t1 = tag >> 16;
#pragma unroll 1
      for (int it = 0; it < (1 << 17); ++it) {
        CBAR();
#pragma unroll
        for (int i = 0; i < 4; ++i) if (nk[i]) k[i] = ld16(rw, i * 1024 + lane * 16, WS_KVR + head * 4096);
#pragma unroll
        for (int i = 0; i < 4; ++i) nk[i] = (((k[i][0] ^ t0) | (k[i][1] ^ t1) | (k[i][2] ^ t0) | (k[i][3] ^ t1)) & 0xffffu) != 0u;
        if (!__any(nk[0] || nk[1] || nk[2] || nk[3])) break;
        __builtin_amdgcn_s_sleep(PSLEEP);
      }
      TS(14);
      u32x4 ka[2];
#pragma unroll
      for (int te = 0; te < 2; ++te)
#pragma unroll
        for (int i = 0; i < 4; ++i) ka[te][i] = __builtin_amdgcn_perm(k[2 * te + (i >> 1)][2 * (i & 1) + 1], k[2 * te + (i >> 1)][2 * (i & 1)], 0x07060302u);
#pragma unroll
      for (int m = 0; m < 4; ++m) {
        const f32x4 z4 = {0.f, 0.f, 0.f, 0.f};
        const f32x4 d0 = MFMA16(asbf(ka[0]), asbf(fq[m]), z4), d1 = MFMA16(asbf(ka[1]), asbf(fq[m]), z4);
        const u32x4 ov = {pk_bf16(d0[0], d0[1]), pk_bf16(d0[2], d0[3]), pk_bf16(d1[0], d1[1]), pk_bf16(d1[2], d1[3])};
        st16(ov, rw, (m & 1) * 1024 + lane * 16, WS_O + ((mb * 4 + mh * 2 + (m >> 1)) * 32 + head) * 2048);
      }
    }
    TS(7);
    WAITVM(0);
    gflag(ws, WS_FP + ((mb * 2 + mh) * 32 + head) * 128, tag, lane);
    TS(8);
    gpoll(ws + WS_FP + (((mb2 * 2 + (wave >> 1)) * 32 + (lane & 31)) << 7), tag);
    TS(9);
    {
      const int pqs = WS_O + (mb2 * 4 + wave) * 65536;
      u32x4 pb[NRING][2];
#define LDQ(i_) do { const int so_ = pqs + ((i_) << 11);                                             \
        pb[(i_) % NRING][0] = ld16(rw, lane * 16, so_); pb[(i_) % NRING][1] = ld16(rw, 1024 + lane * 16, so_); } while (0)
#pragma unroll
      for (int i = 0; i < NRING; ++i) LDQ(i);
      f32x4 oc[2][4];
#pragma unroll
      for (int s = 0; s < 2; ++s)
#pragma unroll
        for (int t = 0; t < 4; ++t) oc[s][t] = (f32x4){0.f, 0.f, 0.f, 0.f};
      asm volatile("" : "+v"(oc[0][0]), "+v"(oc[0][1]), "+v"(oc[0][2]), "+v"(oc[0][3]),
                   "+v"(oc[1][0]), "+v"(oc[1][1]), "+v"(oc[1][2]), "+v"(oc[1][3]));
      LAS char* mp0 = sm + lane * 16;
      bf16x8 ma[4];
      waitge(FL + F_LW, 4);
      if (bid == 0 && wave == 0) gflag(ws, WS_EPOCH, tag, lane);
      TS(10); TC(2);
#pragma unroll
      for (int t = 0; t < 4; ++t) ma[t] = *(LAS bf16x8*)(mp0 + t * 1024);
#pragma unroll
      for (int i = 0; i < 32; ++i) {
        SB();
        const u32x4 b0_ = pb[i % NRING][0], b1_ = pb[i % NRING][1];
#pragma unroll
        for (int t = 0; t < 4; ++t) {
          AMFMA16(oc[0][t], ma[t], b0_);
          AMFMA16(oc[1][t], ma[t], b1_);
          if (i < 31) ma[t] = *(LAS bf16x8*)(mp0 + (i + 1) * 4096 + t * 1024);
          SB();
        }
        if (i + NRING < 32) LDQ(i + NRING);
      }
#undef LDQ
      asm volatile("s_nop 7\n\ts_nop 3" : "+v"(oc[0][0]), "+v"(oc[0][1]), "+v"(oc[0][2]), "+v"(oc[0][3]),
                   "+v"(oc[1][0]), "+v"(oc[1][1]), "+v"(oc[1][2]), "+v"(oc[1][3]));
      TS(11); TC(3);
#pragma unroll
      for (int s = 0; s < 2; ++s)
#pragma unroll
        for (int j = 0; j < 2; ++j) {
          u32x4 w = {pk_bf16(oc[s][2 * j][0], oc[s][2 * j][1]), pk_bf16(oc[s][2 * j][2], oc[s][2 * j][3]),
                     pk_bf16(oc[s][2 * j + 1][0], oc[s][2 * j + 1][1]), pk_bf16(oc[s][2 * j + 1][2], oc[s][2 * j + 1][3])};
          ost16(out + (size_t)((mb2 * 4 + wave) * 32 + s * 16 + c16) * 2048 + nb * 64 + 32 * j + 8 * g4, w);
        }
    }
    TS(12);
    TSDUMP();
    }
    return;
  }

  const int cg = bid >> 4, tt16 = bid & 15;
  const int rg = lane >> 4;
  BAR();
  bf16x8 F[16];
  unsigned fu;
  {
    {
      const f32x4 oa = *(LAS f32x4*)(kc + lane * 16), ob = *(LAS f32x4*)(kc + 1024 + lane * 16), oc = *(LAS f32x4*)(kc + 2048 + lane * 16);
#pragma unroll
      for (int i = 0; i < 4; ++i) {
        float x0 = ta[i] + oa[i], x1 = tb[i] + ob[i], x2 = tc_[i] + oc[i];
        asm("" : "+v"(x0)); asm("" : "+v"(x1)); asm("" : "+v"(x2));
        ta[i] = x0; tb[i] = x1; tc_[i] = x2;
      }
      CBAR();
    }
    unsigned wd[8];
    {
      const char* wp = (const char*)wo + (size_t)(((tt16 * 2 + (c >> 1)) * 32 + 4 * g4) * 2048 + cg * 128 + (c & 1) * 64 + c16 * 4);
#pragma unroll
      for (int i = 0; i < 8; ++i) wd[i] = *(const unsigned*)(wp + ((i & 3) + (i >> 2) * 16) * 2048);
      CBAR();
    }
#pragma unroll
    for (int i = 0; i < 4; ++i) {
      const int f1 = ((lane >> 4) & 1) | ((i & 1) << 1);
      GLDSA((const char*)u + (size_t)((tt16 * 128 + c * 32 + i * 8 + (lane >> 3)) * 2048 + cg * 128 + (((lane & 7) ^ (f1 << 1)) << 4)),
           kc + i * 1024, UAUX);
    }
#define ROL1(v) (unsigned)__builtin_amdgcn_update_dpp(0, (int)(v), 0x134, 0xf, 0xf, false)
#define MIX(lo, hi) __builtin_amdgcn_perm((hi), (lo), 0x07060100u)
    const bool l0 = (lane == 0);
    const u32x2 w0 = {pk_bf16(tc_[0] + (l0 ? v_dsk : 0.f), tc_[1]), pk_bf16(tc_[2], tc_[3])};
    const u32x2 w1 = {pk_bf16(tb[0], tb[0]), pk_bf16(tb[1], tb[2])};
    const unsigned pa0 = pk_bf16(ta[0], ta[0]), pa1 = pk_bf16(ta[1], ta[1]), pa2 = pk_bf16(ta[2], ta[2]), pa3 = pk_bf16(ta[3], ta[3]);
    const unsigned pe = pk_bf16(tb[3], tb[3]);
    const unsigned n0 = ROL1(l0 ? pa2 : pa0), n2 = ROL1(l0 ? pa3 : pa2), n3 = ROL1(l0 ? pe : pa3);
    const u32x4 w2 = {pa0, MIX(pa0, pa1), pa1, n0};
    const u32x4 w3 = {pa2, pa2, (lane & 1) ? n2 : MIX(pa2, n2), n2};
    const u32x4 w4 = {pa3, pa3, (lane & 1) ? n3 : MIX(pa3, n3), n3};
#undef ROL1
#undef MIX
    LAS char* K = ub + 32;
    const int k01 = 2 * (128 * (rg >> 1) + 8 * c16 + 4 * (rg & 1));
    BAR();
    *(LAS u32x2*)(K + k01) = w0;
    *(LAS u32x2*)(K + 512 + k01) = w1;
    BAR();
    *(LAS u32x4*)(K + 1024 + lane * 16) = w2;
    BAR();
    *(LAS u32x4*)(K + 2048 + lane * 16) = w3;
    BAR();
    *(LAS u32x4*)(K + 3072 + lane * 16) = w4;
    asm volatile("s_waitcnt vmcnt(0)" : "+v"(wd[0]), "+v"(wd[1]), "+v"(wd[2]), "+v"(wd[3]), "+v"(wd[4]), "+v"(wd[5]), "+v"(wd[6]), "+v"(wd[7]) :: "memory");
    {
      u32x4 t0, t1;
      const unsigned sel = (lane & 1) ? 0x07060302u : 0x05040100u;
#define QP(v, ctl) (unsigned)__builtin_amdgcn_update_dpp(0, (int)(v), ctl, 0xf, 0xf, false)
#pragma unroll
      for (int i = 0; i < 4; ++i) {
        t0[i] = __builtin_amdgcn_perm(QP(wd[2 * i + 1], 0x50), QP(wd[2 * i], 0x50), sel);
        t1[i] = __builtin_amdgcn_perm(QP(wd[2 * i + 1], 0xFA), QP(wd[2 * i], 0xFA), sel);
      }
#undef QP
      const int so = WS_WT + ((cg * 32 + tt16 * 2 + (c >> 1)) * 4 + (c & 1) * 2) * 1024;
      st16(t0, rw, lane * 16, so);
      st16(t1, rw, 1024 + lane * 16, so);
    }
    BAR();
    TS(4);
    if (lane < 8) *(LAS unsigned*)(ub + lane * 4) = 0u;
    {
      const int f1 = (q4 >> 1) | ((g4 & 1) << 1);
      const int po = (8 * g4 + q4) * 128 + (((2 * c + (p4 >> 1)) ^ (f1 << 1)) << 4) + (p4 & 1) * 8;
#pragma unroll
      for (int j = 0; j < 4; ++j) {
        LAS char* pp = ((j < 2) ? sm + 3 * SLOT + j * 4096 : sm + L_X + (j - 2) * 4096) + po;
        u32x4 v = tr_pair(pp, pp + 512);
        st16(v, rw, (16 * c + (lane & 15)) * 4096 + (32 * j + 8 * g4) * 2, WS_UT + cg * 64 * 4096 + tt16 * 256);
      }
    }
    BAR();
    LAS char* fbp = ub + 2 * (16 - 12 + 32 * rg + 4 * (q4 + p4));
#pragma unroll
    for (int s = 0; s < 16; ++s) F[s] = tr_frag(fbp + 256 * s, fbp + 256 * s + 32);
    CBAR(); __builtin_amdgcn_s_waitcnt(0xC07F); CBAR();
    BAR();
    TS(3);
    *(LAS unsigned*)(ub + lane * 4) = 0u;
    bool pend = true;
#define TRYF(force_) do { if (pend && ((force_) || (__builtin_amdgcn_s_getreg((31 << 11) | 7) & 0xC0000Fu) == 0u)) {      \
      WAITVM(0); gflag(ws, WS_FU + (bid * 4 + c) * 128, tag, lane); pend = false; TS(5); } } while (0)
    TRYF(false);
    BAR();
    TRYF(false);
    BAR();
    TRYF(false);
    BAR();
    TRYF(false);
    BAR();
    TRYF(true);
#undef TRYF
    char* fup = ws + WS_FU + (((g4 & 1) ? (nb * 16 + c16) * 4 + c : (cg * 16 + c16) * 4 + (tt16 >> 2)) << 7);
    fu = __hip_atomic_load((unsigned*)fup, __ATOMIC_RELAXED, __HIP_MEMORY_SCOPE_AGENT);
    BAR();
    BAR();
    if (!__all(fu == tag)) gpoll(fup, tag);
    TS(6);
#pragma unroll
    for (int i = 0; i < 4; ++i)
      __builtin_amdgcn_global_load_lds((const GAS unsigned*)(ws + WS_UT + d * 4096 + i * 1024 + lane * 16), (LAS unsigned*)(ub + 256 + i * 1024), 16, 0, SC1);
    BAR();
    BAR();
    BAR();
    waitge(FL + F_XP, 4);
    WAITVM(0);
  }
  TS(8); TC(2);
  unsigned yp[16];
  {
    LAS char* gb = ub + 2 * (128 + 16 * p4 + 12 - 32 * rg - 4 * q4);
    float pc1 = 0.f, pc2 = 0.f, pc3 = 0.f, yprev = 0.f;
    const bool l0 = (lane == 0);
#define ROR1(v) __int_as_float(__builtin_amdgcn_update_dpp(0, __float_as_int(v), 0x13C, 0xf, 0xf, false))
#define FIN(i)                                                                       \
  do {                                                                               \
    float r3 = ROR1(ca[i][3]);                                                       \
    float s2 = ca[i][2] + (l0 ? pc3 : r3);                                           \
    float r2 = ROR1(s2);                                                             \
    float s1 = ca[i][1] + (l0 ? pc2 : r2);                                           \
    float r1 = ROR1(s1);                                                             \
    float y = ca[i][0] + (l0 ? pc1 : r1);                                            \
    pc1 = r1; pc2 = r2; pc3 = r3;                                                    \
    if ((i) & 1) yp[8 * pass + ((i) >> 1)] = pk_bf16(yprev, y); else yprev = y;      \
  } while (0)
#pragma unroll
    for (int pass = 0; pass < 2; ++pass) {
      f32x4 ca[16];
#pragma unroll
      for (int i = 0; i < 16; ++i) ca[i] = (f32x4){0, 0, 0, 0};
      bf16x8 G = tr_frag(gb, gb - 32);
#pragma unroll
      for (int v = 0; v < 16 * pass + 16; ++v) {
        bf16x8 Gn = G;
        if (v + 1 < 16 * pass + 16) Gn = tr_frag(gb + 128 * (v + 1), gb + 128 * (v + 1) - 32);
        SB();
#pragma unroll
        for (int i = 0; i < 16; ++i) {
          const int T = 16 * pass + i;
          if (T >= v && ((T - v) & 1) == 0) ca[i] = MFMA16(G, F[(T - v) >> 1], ca[i]);
        }
        if (v - 1 >= 16 * pass) FIN(v - 1 - 16 * pass);
        SB();
        G = Gn;
      }
      FIN(15);
      CBAR();
      SB();
    }
#undef FIN
#undef ROR1
  }
  CBAR();
  TS(9); TC(3);
#pragma unroll
  for (int i = 0; i < 16; ++i) {
    ((LAS u16*)ub)[128 * i + lane] = (u16)yp[i];
    ((LAS u16*)ub)[128 * i + 64 + lane] = (u16)(yp[i] >> 16);
  }
  CBAR();
#pragma unroll
  for (int i = 0; i < 4; ++i) st16(*(LAS u32x4*)(ub + i * 1024 + lane * 16), rw, i * 1024 + lane * 16, WS_YT + d * 4096);
  WAITVM(0);
  if (sig_ret(FL + F_CY) == 3u) {
    gflag(ws, WS_FY + (cg * 16 + tt16) * 128, tag, lane);
    TS(10);
    gpoll(ws + WS_FY + ((cg * 16 + (lane & 15)) << 7), tag);
    sig(FL + F_Y);
  } else {
    TS(10);
    waitge(FL + F_Y, 1);
  }
  TS(11);
  {
    int ln2 = lane;
    asm volatile("" : "+v"(ln2));
    const int lane = ln2, q4 = (lane & 15) >> 2, p4 = lane & 3, g4 = lane >> 4;
    LAS char* tb = sm + L_UB;
    u32x4 yr[4];
#pragma unroll
    for (int i = 0; i < 4; ++i)
      yr[i] = ld16(rw, (16 * c + 4 * i + g4) * 4096 + (lane & 15) * 16, WS_YT + cg * 64 * 4096 + tt16 * 256);
#pragma unroll
    for (int i = 0; i < 4; ++i) {
      const int row = 16 * c + 4 * i + g4;
      const int f = (row & 3) | (((row >> 3) & 1) << 2);
      *(LAS u32x4*)(tb + row * 256 + (((lane & 15) ^ (f << 1)) << 4)) = yr[i];
    }
    sig(FL + F_T2);
    waitge(FL + F_T2, 4);
    const int f = q4 | ((g4 & 1) << 2);
#pragma unroll
    for (int j = 0; j < 4; ++j) {
      const int tbk = 2 * c + (j >> 1), hf = j & 1;
      LAS char* pp = tb + (32 * hf + 8 * g4 + q4) * 256 + (((2 * tbk + (p4 >> 1)) ^ (f << 1)) << 4) + (p4 & 1) * 8;
      u32x4 v = tr_pair(pp, pp + 1024);
      ost16(out + (size_t)(tt16 * 128 + 16 * tbk + (lane & 15)) * 2048 + 1024 + cg * 64 + 32 * hf + 8 * g4, v);
    }
  }
  TS(12);
  TSDUMP();
}

extern "C" void kernel_launch(void* const* d_in, const int* in_sizes, int n_in, void* d_out, int out_size,
                              void* d_ws, size_t ws_size, hipStream_t stream) {
  fused_kernel<<<256, 768, 0, stream>>>((const u16*)d_in[0], (const u16*)d_in[1], (const u16*)d_in[2], (const u16*)d_in[3],
                                        (const u16*)d_in[4], (const u16*)d_in[5], (const u16*)d_in[6], (const u16*)d_in[7],
                                        (const u16*)d_in[8], (const u16*)d_in[9], (u16*)d_out, (char*)d_ws);
}
```

```cpp
#include <hip/hip_runtime.h>
#include <stdint.h>

#define LAS __attribute__((address_space(3)))
#define GAS __attribute__((address_space(1)))
#define DI __device__ __forceinline__

typedef unsigned short u16;
typedef short s16x4 __attribute__((ext_vector_type(4)));
typedef __bf16 bf16x2 __attribute__((ext_vector_type(2)));
typedef __bf16 bf16x8 __attribute__((ext_vector_type(8)));
typedef _Float16 f16x2 __attribute__((ext_vector_type(2)));
typedef _Float16 f16x8 __attribute__((ext_vector_type(8)));
typedef float f32x2 __attribute__((ext_vector_type(2)));
typedef float f32x4 __attribute__((ext_vector_type(4)));
typedef float f32x16 __attribute__((ext_vector_type(16)));
typedef unsigned u32x2 __attribute__((ext_vector_type(2)));
typedef unsigned u32x4 __attribute__((ext_vector_type(4)));
typedef __amdgpu_buffer_rsrc_t rsrc_t;

#define CBAR() asm volatile("" ::: "memory")
#define SB() __builtin_amdgcn_sched_barrier(0)
#define WAITVM(n) asm volatile("s_waitcnt vmcnt(" #n ")" ::: "memory")
#define WAITVMX(n) WAITVM(n)
#define WAITVL(n) asm volatile("s_waitcnt vmcnt(" #n ") lgkmcnt(0)" ::: "memory")
#define BAR() asm volatile("s_barrier" ::: "memory")
#define LBAR() do { CBAR(); __builtin_amdgcn_s_waitcnt(0xC07F); asm volatile("s_barrier" ::: "memory"); } while (0)
#define TSDECL
#define TS(i)
#define TC(i)
#define TSDUMP()
#define KOFF (4 * (xcd >> 1) + 8 * (xcd & 1))
#define KXOFF (4 * (xcd >> 1) + 8 * (xcd & 1))
#define PRIO_C 2
#define PRIO_L 3
#define PSLEEP 3
#define CODE_LEN 30208u
#define PFM 15
#define EBAR
#define WT 2
#define NRING 10
#define PRE 3
#define PD 0
#define SB1 0
#define ORD 6
#define NWB 2
#define RT0 (6 + NWB)
#define UE 1
#define PRIO_SH 3
#define FSPLIT 0
#define FUB 11
#define PT 4
#define HQX 1
#define KL 1
#define KD 0
#define FB1 1
#define PAUX 0
#define YW 0
#define OT 0
#define OD 0
#define OAUX SC1
#define QAUX SC1
#define QINV 0

DI void ost16(void* p, u32x4 v) { asm volatile("global_store_dwordx4 %0, %1, off sc1\n\ts_nop 1" :: "v"(p), "v"(v) : "memory"); }
DI float sum16(float x) {
  auto r = __builtin_amdgcn_permlane16_swap(__float_as_uint(x), __float_as_uint(x), false, false);
  float s = __uint_as_float(r[0]) + __uint_as_float(r[1]);
  asm("" : "+v"(s));
  return s;
}
DI float sum32(float x) {
  auto r = __builtin_amdgcn_permlane32_swap(__float_as_uint(x), __float_as_uint(x), false, false);
  float s = __uint_as_float(r[0]) + __uint_as_float(r[1]);
  asm("" : "+v"(s));
  return s;
}
DI float sum8(float x) {
  return x + __int_as_float(__builtin_amdgcn_update_dpp(0, __float_as_int(x), 0x128, 0xf, 0xf, false));
}
DI float wave_max(float x) {
#define DPPMAX(ctl) x = __builtin_fmaxf(x, __int_as_float(__builtin_amdgcn_update_dpp(0, __float_as_int(x), ctl, 0xf, 0xf, false)))
  DPPMAX(0xB1); DPPMAX(0x4E); DPPMAX(0x141); DPPMAX(0x140);
#undef DPPMAX
  auto r = __builtin_amdgcn_permlane16_swap(__float_as_uint(x), __float_as_uint(x), false, false);
  x = __builtin_fmaxf(__uint_as_float(r[0]), __uint_as_float(r[1]));
  r = __builtin_amdgcn_permlane32_swap(__float_as_uint(x), __float_as_uint(x), false, false);
  return __builtin_fmaxf(__uint_as_float(r[0]), __uint_as_float(r[1]));
}
DI int lane_id() {
  int l = __builtin_amdgcn_mbcnt_hi(~0u, __builtin_amdgcn_mbcnt_lo(~0u, 0u));
  asm volatile("" : "+v"(l));
  __builtin_assume(l >= 0 && l < 64);
  return l;
}
DI float bf2f(u16 v) { return __uint_as_float((unsigned)v << 16); }
DI unsigned pk_bf16(float a, float b) {
  f32x2 f = {a, b};
  return __builtin_bit_cast(unsigned, __builtin_convertvector(f, bf16x2));
}
DI unsigned pk_f16(float a, float b) {
  f32x2 f = {a, b};
  return __builtin_bit_cast(unsigned, __builtin_convertvector(f, f16x2));
}
DI float rbf(float a) { return __uint_as_float(pk_bf16(a, 0.f) << 16); }
DI u32x2 tr_read(LAS char* p) {
  s16x4 v = __builtin_amdgcn_ds_read_tr16_b64_v4i16((LAS s16x4*)p);
  return __builtin_bit_cast(u32x2, v);
}
DI u32x4 join(u32x2 a, u32x2 b) { u32x4 r = {a.x, a.y, b.x, b.y}; return r; }
DI u32x4 tr_pair(LAS char* p0, LAS char* p1) { return join(tr_read(p0), tr_read(p1)); }
DI bf16x8 asbf(u32x4 v) { return __builtin_bit_cast(bf16x8, v); }
DI bf16x8 tr_frag(LAS char* p0, LAS char* p1) { return asbf(tr_pair(p0, p1)); }
#define MFMA32(a, b, c) __builtin_amdgcn_mfma_f32_32x32x16_bf16((a), (b), (c), 0, 0, 0)
#define MFMA16(a, b, c) __builtin_amdgcn_mfma_f32_16x16x32_bf16((a), (b), (c), 0, 0, 0)
#define MFMA16H(a, b, c) __builtin_amdgcn_mfma_f32_16x16x32_f16((a), (b), (c), 0, 0, 0)
#define AMFMA16(c, a, b) asm volatile("v_mfma_f32_16x16x32_bf16 %0, %1, %2, %0" : "+v"(c) : "v"(a), "v"(b))
#define GLDS(src, dst) __builtin_amdgcn_global_load_lds((const GAS unsigned*)(src), (LAS unsigned*)(dst), 16, 0, 0)
#define XAUX 0
#define WAUX 0
#define UAUX 2
#define GLDSA(src, dst, aux) __builtin_amdgcn_global_load_lds((const GAS unsigned*)(src), (LAS unsigned*)(dst), 16, 0, aux)
#define SC1 16
DI u32x4 ld16(rsrc_t r, int voff, int soff) { return __builtin_amdgcn_raw_buffer_load_b128(r, voff, soff, SC1); }
DI void st16(u32x4 v, rsrc_t r, int voff, int soff) {
  __builtin_amdgcn_raw_buffer_store_b128(v, r, voff, soff, SC1);
  asm volatile("s_nop 1" :: "v"(v) : "memory");
}

constexpr int WS_EPOCH = 0;
constexpr int WS_FY = 65536, WS_FU = 131072, WS_FP = WS_FU + 131072;
constexpr int WS_O = 524288;
constexpr int WS_UT = WS_O + (4 << 20);
constexpr int WS_YT = WS_UT + (4 << 20);
constexpr int WS_WT = WS_YT + (4 << 20);
constexpr int WS_KVP = WS_WT + (2 << 20);
constexpr int WS_ZP = WS_KVP + 16 * 16 * 16 * 512;
constexpr int WS_KVR = WS_ZP + 32 * 16 * 128;
constexpr int WS_END = WS_KVR + 32 * 4096;

constexpr int SLOT = 40960;
constexpr int L_UB = 131072, UB_CH = 4352;
constexpr int L_X = L_UB + 4 * UB_CH;
constexpr int L_Z = L_X + 4 * 2176;
constexpr int L_FLAG = L_Z + 512;
constexpr int L_DUM = L_FLAG + 256;
constexpr int L_FU = L_DUM + 256;
constexpr int LDS_BYTES = L_FU + 1024;
enum { F_KC = 0, F_XE = 8, F_LW = 11, F_CY, F_T2, F_XP, F_KV = 16, F_OD = 18, F_OS, F_Z = 24, F_Y = 26 };

typedef f32x2 cplx;
DI cplx cmul(cplx a, cplx b) {
  cplx t, r;
  asm("v_pk_mul_f32 %0, %1, %2 op_sel:[1,1] op_sel_hi:[1,0] neg_lo:[1,0]" : "=v"(t) : "v"(a), "v"(b));
  asm("v_pk_fma_f32 %0, %1, %2, %3 op_sel:[0,0,0] op_sel_hi:[0,1,1]" : "=v"(r) : "v"(a), "v"(b), "v"(t));
  return r;
}
DI cplx zpow(float arl2, float ai, float p) {
  const float INV2PI = 0.15915494309189535f;
  const float INV2PI_LO = (float)(0.15915494309189533576888 - (double)0.15915494309189535f);
  float e = __builtin_amdgcn_exp2f(arl2 * p);
  float r = ai * p;
  float hi = r * INV2PI;
  float lo = __builtin_fmaf(r, INV2PI_LO, __builtin_fmaf(r, INV2PI, -hi));
  float f = (hi - __builtin_rintf(hi)) + lo;
  cplx z = {e * __builtin_amdgcn_cosf(f), e * __builtin_amdgcn_sinf(f)};
  return z;
}
template <bool NOPK> DI float phi_f(float t) {
  float a = t * 1.4426950408889634f, b = t + 1.f;
  if (NOPK) { asm("" : "+v"(a)); asm("" : "+v"(b)); }
  float r = __builtin_amdgcn_fmed3f(__builtin_amdgcn_exp2f(a), b, 1.f) + 1e-6f;
  if (NOPK) asm("" : "+v"(r));
  return r;
}
DI u32x4 tag_put(f32x4 v, unsigned tag) {
  u32x4 b = __builtin_bit_cast(u32x4, v), r;
  r[0] = __builtin_amdgcn_perm(b[0], tag, 0x07060500u);
  r[1] = __builtin_amdgcn_perm(b[1], tag, 0x07060501u);
  r[2] = __builtin_amdgcn_perm(b[2], tag, 0x07060502u);
  r[3] = __builtin_amdgcn_perm(b[3], tag, 0x07060503u);
  return r;
}
DI unsigned tag_get(u32x4 b) {
  unsigned lo = __builtin_amdgcn_perm(b[1], b[0], 0x0c0c0400u), hi = __builtin_amdgcn_perm(b[3], b[2], 0x04000c0cu);
  return lo | hi;
}
DI f32x4 tag_val(u32x4 b) { return __builtin_bit_cast(f32x4, b & 0xffffff00u); }

DI void sig(LAS unsigned* f) {
  asm volatile("s_mov_b64 exec, 1\n\tds_add_u32 %0, %1\n\ts_mov_b64 exec, -1" :: "v"((unsigned)(size_t)f), "v"(1u) : "memory");
}
DI unsigned sig_ret(LAS unsigned* f) {
  unsigned r;
  asm volatile("s_mov_b64 exec, 1\n\tds_add_rtn_u32 %0, %1, %2\n\ts_mov_b64 exec, -1\n\ts_waitcnt lgkmcnt(0)"
               : "=&v"(r) : "v"((unsigned)(size_t)f), "v"(1u) : "memory");
  return __builtin_amdgcn_readfirstlane(r);
}
DI unsigned ldsword(LAS unsigned* f) { return __builtin_amdgcn_readfirstlane(*(volatile LAS unsigned*)f); }
DI void waitge(LAS unsigned* f, unsigned n) {
  CBAR();
#pragma unroll 1
  for (int it = 0; it < (1 << 22); ++it) {
    if (ldsword(f) >= n) break;
    __builtin_amdgcn_s_sleep(1);
  }
  CBAR();
}
DI void gflag(char* ws, int off, unsigned tag, int lane) {
  CBAR();
  if (lane < 32) __hip_atomic_store((unsigned*)(ws + off) + lane, tag, __ATOMIC_RELAXED, __HIP_MEMORY_SCOPE_AGENT);
  CBAR();
}
DI void gpoll(char* p, unsigned tag) {
  CBAR();
  unsigned v = ~tag;
#pragma unroll 1
  for (int it = 0; it < (1 << 18); ++it) {
    if (v != tag) v = __hip_atomic_load((unsigned*)p, __ATOMIC_RELAXED, __HIP_MEMORY_SCOPE_AGENT);
    if (__all(v == tag)) break;
    __builtin_amdgcn_s_sleep(PSLEEP);
  }
  CBAR();
}

__global__ __launch_bounds__(768, 3) void fused_kernel(const u16* __restrict__ x, const u16* __restrict__ wqkv,
                                                       const u16* __restrict__ wo, const u16* __restrict__ u,
                                                       const u16* __restrict__ lar, const u16* __restrict__ aim,
                                                       const u16* __restrict__ cre, const u16* __restrict__ cim,
                                                       const u16* __restrict__ ldt, const u16* __restrict__ dsk,
                                                       u16* __restrict__ out, char* __restrict__ ws) {
  __shared__ __attribute__((aligned(16))) char smem[LDS_BYTES];
  LAS char* sm = (LAS char*)smem;
  LAS unsigned* FL = (LAS unsigned*)(sm + L_FLAG);
  const int tid = threadIdx.x, lane = tid & 63;
  const int wave = __builtin_amdgcn_readfirstlane(tid >> 6);
  const int bid = blockIdx.x, xcd = bid & 7, idx = bid >> 3;
  const int ml = idx & 7, hl = idx >> 3;
  const int mb = (xcd & 1) * 8 + ml, hp = (xcd >> 1) * 4 + hl;
  const int hq = hp ^ HQX;
  const int mb2 = (xcd & 3) * 4 + (idx & 3), nb = (xcd >> 2) * 8 + (idx >> 2);
  unsigned pf = 0u;
  {
    const unsigned po = (unsigned)(tid * 64 + (idx & PFM) * 4096) | (tid >= 64 ? 0x40000000u : 0u);
    asm volatile("v_cmp_gt_u32 vcc, %3, %1\n\ts_mov_b64 exec, vcc\n\tglobal_load_dword %0, %1, %2\n\ts_mov_b64 exec, -1"
                 : "+v"(pf) : "v"(po), "s"(__builtin_amdgcn_s_getpc()), "s"(CODE_LEN) : "memory", "vcc");
  }
#define PFDONE() asm volatile("" :: "v"(pf))
  asm volatile("ds_write_b32 %0, %1" :: "v"((unsigned)(size_t)FL + lane_id() * 4), "v"(0u) : "memory");
  SB();
  TSDECL;
  const rsrc_t rw = __builtin_amdgcn_make_buffer_rsrc((void*)ws, 0, WS_END, 0x00020000);
  asm volatile("" :: "s"(x), "s"(wqkv), "s"(wo), "s"(u), "s"(lar), "s"(aim), "s"(cre), "s"(cim), "s"(ldt), "s"(dsk), "s"(out), "s"(ws));
  asm volatile("s_waitcnt lgkmcnt(0)" ::: "memory");
  unsigned tag;
#define TAGLD() asm volatile("s_load_dword %0, %1, 0x0" : "=s"(tag) : "s"(ws) : "memory")
#define TAGRD() do { asm volatile("s_waitcnt lgkmcnt(0)" : "+s"(tag) :: "memory"); tag = tag * 2654435761u + 0x9E3779B1u; } while (0)
  const int q4 = (lane & 15) >> 2, p4 = lane & 3, g4 = lane >> 4, c16 = lane & 15;

#define LROLE 2
  if ((wave >> 2) == LROLE) {
    __builtin_amdgcn_s_setprio(PRIO_L);
    const int hw = wave & 3;
    const int lane = lane_id(), q4 = (lane & 15) >> 2, p4 = lane & 3, g4 = lane >> 4, c16 = lane & 15;
    const int koff = KOFF;
    const unsigned vx0 = (unsigned)((mb * 128 + hw * 32 + (lane >> 3)) * 2048) + (((lane & 7) ^ (lane >> 4)) << 4), vx1 = vx0 ^ 64;
    const unsigned vw0 = (unsigned)((lane >> 3) * 6144 + (((lane & 7) ^ (((lane >> 4) & 1) << 1)) << 4)), vw1 = vw0 ^ 64;
#define ISSUE(tt_, slot_)                                                                                  \
  do {                                                                                                     \
    const int k_ = ((tt_) + koff) & 15;                                                                    \
    const char* xa_ = (const char*)x + k_ * 128;                                                           \
    const char* wa_ = (const char*)wqkv + (size_t)k_ * 393216;                                             \
    LAS char* sb_ = sm + (slot_) * SLOT;                                                                   \
    _Pragma("unroll") for (int i = 0; i < 4; ++i)                                                          \
        GLDSA(xa_ + i * 16384 + ((i & 1) ? vx1 : vx0), sb_ + (hw * 4 + i) * 1024, XAUX);                   \
    _Pragma("unroll") for (int i = 0; i < 6; ++i) {                                                        \
      const int qq_ = hw * 6 + i;                                                                          \
      GLDSA(wa_ + ((qq_ & 7) * 49152 + (qq_ >> 3) * 2048 + (qq_ < 8 ? hq : hp) * 128) + ((i & 1) ? vw1 : vw0), sb_ + 16384 + qq_ * 1024, WAUX); \
    }                                                                                                      \
  } while (0)
    BAR();
    const int kx = KXOFF;
    const int cg = bid >> 4, tt16 = bid & 15;
    const unsigned xo = (unsigned)((mb * 128 + hl * 32 + hw * 8 + (lane & 7)) * 2048);
#define TOUCH(xt_, nx_, wt_, nw_) do {                                                                                     \
      const unsigned m_ = (unsigned)(lane - 8 * (nx_)), q_ = (m_ * 43u) >> 8, l_ = idx * 24 + hw * 6 + (m_ - 6u * q_);     \
      const unsigned ow_ = (((wt_) + kx + q_) & 15u) * 393216u + (l_ >> 2) * 2048u + (l_ & 3u) * 128u + (xcd >> 1) * 512;  \
      const unsigned ox_ = (((xt_) + koff + (lane >> 3)) & 15u) * 128u + xo;                                               \
      const char* p_ = (lane < 8 * (nx_)) ? (const char*)x + ox_ : (const char*)wqkv + ow_;                                \
      if (lane < 8 * (nx_) + 6 * (nw_)) __builtin_amdgcn_global_load_lds((const GAS unsigned*)p_, (LAS unsigned*)(sm + L_DUM), 4, 0, 0); } while (0)
    LAS char* uc = (hw < 2) ? sm + 3 * SLOT + hw * 4096 : sm + L_X + (hw - 2) * 4096;
    unsigned wd[8];
#define ULD() do {                                                                                                         \
      const unsigned uo0 = (unsigned)((tt16 * 128 + hw * 32 + (lane >> 3)) * 2048 + cg * 128 + (((lane & 7) ^ (((lane >> 4) & 1) << 1)) << 4)), uo1 = (uo0 ^ 64) + 16384; \
      GLDSA((const char*)u + uo0, uc, UAUX); GLDSA((const char*)u + uo1, uc + 1024, UAUX);                                 \
      GLDSA((const char*)u + 32768 + uo0, uc + 2048, UAUX); GLDSA((const char*)u + 32768 + uo1, uc + 3072, UAUX);          \
      const unsigned wo0 = (unsigned)(((tt16 * 2 + (hw >> 1)) * 32 + 4 * g4) * 2048 + cg * 128 + (hw & 1) * 64 + c16 * 4); \
      asm volatile("global_load_dword %0, %8, %10\n\tglobal_load_dword %1, %8, %10 offset:2048\n\t"                        \
                   "global_load_dword %2, %9, %10\n\tglobal_load_dword %3, %9, %10 offset:2048\n\t"                        \
                   "global_load_dword %4, %8, %11\n\tglobal_load_dword %5, %8, %11 offset:2048\n\t"                        \
                   "global_load_dword %6, %9, %11\n\tglobal_load_dword %7, %9, %11 offset:2048"                            \
                   : "=&v"(wd[0]), "=&v"(wd[1]), "=&v"(wd[2]), "=&v"(wd[3]), "=&v"(wd[4]), "=&v"(wd[5]), "=&v"(wd[6]), "=&v"(wd[7]) \
                   : "v"(wo0), "v"(wo0 + 4096u), "s"(wo), "s"((const char*)wo + 32768) : "memory"); } while (0)
    ISSUE(0, 0);
    TS(13);
    TOUCH(1, 3, 1, 6);
    ISSUE(1, 1);
    TOUCH(0, 0, 7, NWB - 1);
    ULD();
#define WM1 24
#define W00 23
#define AO 1
    TS(14);
    TAGLD();
    WAITVMX(WM1);
    TAGRD();
    TS(1);
    BAR();
    ISSUE(2, 2);
#undef ULD
    u32x4 ut[4], wt[2];
    const u32x4 nt = {~tag, ~tag, ~tag, ~tag};
    const int f1 = (q4 >> 1) | ((g4 & 1) << 1);
    const unsigned up = (unsigned)(size_t)uc + (8 * g4 + q4) * 128 + (((p4 >> 1) ^ (f1 << 1)) << 4) + (p4 & 1) * 8;
    const int uo = c16 * 4096 + 16 * g4, us = WS_UT + cg * 64 * 4096 + tt16 * 256 + hw * 64;
    const int ws_ = WS_WT + ((cg * 32 + tt16 * 2 + (hw >> 1)) * 4 + (hw & 1) * 2) * 1024;
    const int ps = WS_KVP + (hp * 16 + hw * 4) * 8192 + mb * 512, po0 = (lane >> 5) * 8192 + (lane & 31) * 16;
    int po1;
    {
      const int a0_ = WS_KVR + hp * 8192 + mb * 512 + hw * 128, a1_ = WS_ZP + ((hp * 2 + (hw & 1)) * 16 + mb) * 128;
      const int a2_ = FB1 ? WS_FP + (mb * 16 + hq) * 128 : WS_FP + ((mb * 2 + (hw & 1)) * 32 + hq * 2 + (hw >> 1)) * 128, a3_ = WS_FY + bid * 128;
      const int g8 = (lane >> 3) & 3;
      int av = (g8 & 1) ? a1_ : a0_, aw = (g8 & 1) ? a3_ : a2_;
      asm("" : "+v"(av), "+v"(aw));
      po1 = ((g8 & 2) ? aw : av) + (lane & 7) * 16;
    }
    const unsigned fuo = WS_FU + ((((g4 & 1) ? nb : cg) * 16 + c16) << 7);
    WAITVMX(W00);
    {
      int sf = 0;
#pragma unroll 1
      for (int t = 0;; ++t) {
        BAR();
        ISSUE(t + 3, sf);
        if (t == 12) break;
        TOUCH(0, 0, t + RT0, 1);
        sf = (sf == 2) ? 0 : sf + 1;
        if (t == AO) {
          u32x2 lo[4], hi[4];
          asm volatile("ds_read_b64_tr_b16 %0, %8\n\tds_read_b64_tr_b16 %1, %8 offset:512\n\t"
                       "ds_read_b64_tr_b16 %2, %9\n\tds_read_b64_tr_b16 %3, %9 offset:512\n\t"
                       "ds_read_b64_tr_b16 %4, %10\n\tds_read_b64_tr_b16 %5, %10 offset:512\n\t"
                       "ds_read_b64_tr_b16 %6, %11\n\tds_read_b64_tr_b16 %7, %11 offset:512\n\t"
                       "s_waitcnt lgkmcnt(0)"
                       : "=&v"(lo[0]), "=&v"(hi[0]), "=&v"(lo[1]), "=&v"(hi[1]), "=&v"(lo[2]), "=&v"(hi[2]), "=&v"(lo[3]), "=&v"(hi[3])
                       : "v"(up), "v"(up ^ 32u), "v"(up ^ 64u), "v"(up ^ 96u) : "memory");
#pragma unroll
          for (int j = 0; j < 4; ++j) ut[j] = join(lo[j], hi[j]);
          st16(ut[0], rw, uo, us);
          asm volatile("" : "+v"(wd[0]), "+v"(wd[1]), "+v"(wd[2]), "+v"(wd[3]), "+v"(wd[4]), "+v"(wd[5]), "+v"(wd[6]), "+v"(wd[7]));
          const unsigned sel = (lane & 1) ? 0x07060302u : 0x05040100u;
#define QP(v, ctl) (unsigned)__builtin_amdgcn_update_dpp(0, (int)(v), ctl, 0xf, 0xf, false)
#pragma unroll
          for (int i = 0; i < 4; ++i) {
            wt[0][i] = __builtin_amdgcn_perm(QP(wd[2 * i + 1], 0x50), QP(wd[2 * i], 0x50), sel);
            wt[1][i] = __builtin_amdgcn_perm(QP(wd[2 * i + 1], 0xFA), QP(wd[2 * i], 0xFA), sel);
          }
#undef QP
        }
        else if (t == AO + 1) st16(ut[1], rw, uo, us + 65536);
        else if (t == AO + 2) st16(ut[2], rw, uo, us + 131072);
        else if (t == AO + 3) st16(ut[3], rw, uo, us + 196608);
        else if (t == AO + 4) st16(wt[0], rw, lane * 16, ws_);
        else if (t == AO + 5) st16(wt[1], rw, lane * 16, ws_ + 1024);
        else if (t == AO + 6) st16(nt, rw, po0, ps);
        else if (t == AO + 7) st16(~nt, rw, (lane & 7) * 16, WS_FU + bid * 128);
        else if (t == AO + 8) st16(nt, rw, po0, ps + 16384);
        else if (t == 11) __builtin_amdgcn_global_load_lds((const GAS unsigned*)(ws + fuo), (LAS unsigned*)(sm + L_FU + hw * 256), 4, 0, SC1);
        else st16(nt, rw, po1, 0);
        WAITVM(12);
      }
    }
#undef TOUCH
#undef ISSUE
    TS(2);
    {
      unsigned fu;
      asm volatile("s_waitcnt vmcnt(10)\n\tds_read_b32 %0, %1\n\ts_waitcnt lgkmcnt(0)" : "=v"(fu) : "v"((unsigned)(size_t)sm + L_FU + hw * 256 + lane * 4) : "memory");
      if (!__all(fu == tag)) gpoll(ws + fuo, tag);
      const char* utp = ws + WS_UT + (bid * 4 + hw) * 4096 + lane * 16;
#pragma unroll
      for (int i = 0; i < 4; ++i)
        __builtin_amdgcn_global_load_lds((const GAS unsigned*)(utp + i * 1024), (LAS unsigned*)(sm + L_UB + hw * UB_CH + 256 + i * 1024), 16, 0, SC1);
      const char* src = ws + WS_WT + nb * 131072 + hw * 1024 + lane * 16;
#define WOP(s_) __builtin_amdgcn_global_load_lds((const GAS unsigned*)(src + (s_) * 4096), (LAS unsigned*)(sm + ((s_) * 4 + hw) * 1024), 16, 0, SC1)
      BAR();
#pragma unroll
      for (int i = 10; i < 20; ++i) WOP(i);
      WAITVM(14);
      BAR();
#pragma unroll
      for (int i = 20; i < 32; ++i) WOP(i);
      WAITVM(22);
      BAR();
#pragma unroll
      for (int i = 0; i < 10; ++i) WOP(i);
#undef WOP
      TS(3);
      WAITVM(0);
      sig(FL + F_LW);
    }
    TS(4);
    waitge(FL + F_XP, 4);
    {
      const int so = WS_KVP + (hp * 16 + mb) * 8192;
      const int vo = (lane >> 3) * 512 + (hw * 8 + (lane & 7)) * 16;
      u32x4 a = {0u, 0u, 0u, 0u}, b = a;
      bool na = true, nb_ = true;
#pragma unroll 1
      for (int it = 0; it < (1 << 17); ++it) {
        CBAR();
        if (na) a = ld16(rw, vo, so);
        if (nb_) b = ld16(rw, vo + 4096, so);
        na = tag_get(a) != tag; nb_ = tag_get(b) != tag;
        if (!__any(na || nb_)) break;
        __builtin_amdgcn_s_sleep(PSLEEP);
      }
      TS(5);
      const f32x4 fa = tag_val(a), fb = tag_val(b);
      float s[4];
#pragma unroll
      for (int i = 0; i < 4; ++i) { s[i] = fa[i] + fb[i]; asm("" : "+v"(s[i])); }
#pragma unroll
      for (int i = 0; i < 4; ++i) s[i] = sum32(sum16(sum8(s[i])));
      const unsigned p0 = pk_bf16(s[0], s[1]), p1 = pk_bf16(s[2], s[3]);
      const u32x4 w = {__builtin_amdgcn_perm(p0, tag, 0x05040100u), __builtin_amdgcn_perm(p0, tag, 0x07060302u),
                       __builtin_amdgcn_perm(p1, tag, 0x05040100u), __builtin_amdgcn_perm(p1, tag, 0x07060302u)};
      if (lane < 8) st16(w, rw, lane * 16, WS_KVR + hp * 8192 + mb * 512 + hw * 128);
      TS(6);
    }
    {
      u32x4 a = {0u, 0u, 0u, 0u}, b = a;
      bool na = true, nb_ = true;
      const unsigned t0 = tag & 0xffffu, t1 = tag >> 16;
#pragma unroll 1
      for (int it = 0; it < (1 << 17); ++it) {
        CBAR();
        if (na) a = ld16(rw, lane * 16, WS_KVR + hq * 8192 + hw * 2048);
        if (nb_) b = ld16(rw, 1024 + lane * 16, WS_KVR + hq * 8192 + hw * 2048);
        na = (((a[0] ^ t0) | (a[1] ^ t1) | (a[2] ^ t0) | (a[3] ^ t1)) & 0xffffu) != 0u;
        nb_ = (((b[0] ^ t0) | (b[1] ^ t1) | (b[2] ^ t0) | (b[3] ^ t1)) & 0xffffu) != 0u;
        if (!__any(na || nb_)) break;
        __builtin_amdgcn_s_sleep(PSLEEP);
      }
      *(LAS u32x4*)(sm + L_X + hw * 1024 + lane * 16) =
          (u32x4){__builtin_amdgcn_perm(a[1], a[0], 0x07060302u), __builtin_amdgcn_perm(a[3], a[2], 0x07060302u),
                  __builtin_amdgcn_perm(b[1], b[0], 0x07060302u), __builtin_amdgcn_perm(b[3], b[2], 0x07060302u)};
      sig(FL + F_KV + (hw >> 1));
    }
    TS(9);
    TSDUMP();
    return;
  }

  const int c = wave & 3, d = bid * 4 + c;
  LAS char* ub = sm + L_UB + c * UB_CH;
  LAS char* kc = sm + 2 * SLOT + c * 4096;
  f32x4 ta = {0, 0, 0, 0}, tb = {0, 0, 0, 0}, tc_ = {0, 0, 0, 0};
  float v_dsk = bf2f(dsk[d]);
  {
    const int pi = d * 128 + (wave < 4 ? 64 : 0) + lane;
    const float lr = bf2f(lar[pi]), aiv = bf2f(aim[pi]), cr = bf2f(cre[pi]), ci = bf2f(cim[pi]);
    const float dt = rbf(expf(bf2f(ldt[d])));
    LAS char* ks = (wave < 4) ? kc : ub;
    TAGLD();
    BAR();
    const float mx = wave_max(__builtin_fmaxf(__builtin_fabsf(cr), __builtin_fabsf(ci)));
    const int ex = (mx > 0.f && mx < 3.0e38f) ? __builtin_amdgcn_frexp_expf(mx) : 0;
    const float mult = __builtin_amdgcn_ldexpf(2.f * dt, ex);
    asm volatile("" : "+v"(v_dsk));
    PFDONE();
    TAGRD();
    TS(15);
    const float ar = -rbf(expf(lr)) * dt;
    const float ai = aiv * dt;
    const float arl2 = ar * 1.4426950408889634f;
    const cplx z1 = zpow(arl2, ai, 1.f), z8 = zpow(arl2, ai, 8.f), z128 = zpow(arl2, ai, 128.f);
    LAS char* wp = ks + lane * 64 + (((lane >> 1) & 3) << 4);
    const int h0 = g4 & 1;
    LAS char* rp0 = ks + (g4 * 256 + h0 * 128 + (q4 >> 1) * 64 + (((2 * (q4 & 1) + (p4 >> 1)) ^ ((2 * g4 + h0) & 3)) << 4) + (p4 & 1) * 8);
    LAS char* rp1 = ks + ((unsigned)(rp0 - ks) ^ 144u);
#define KW(v, sg)                                                                                                    \
  _Pragma("unroll") for (int hh = 0; hh < 2; ++hh) {                                                                 \
    u32x4 wr_, wi_;                                                                                                  \
    _Pragma("unroll") for (int i = 0; i < 4; ++i) {                                                                  \
      wr_[i] = pk_f16(v[8 * hh + 2 * i][0], v[8 * hh + 2 * i + 1][0]);                                               \
      wi_[i] = pk_f16(sg v[8 * hh + 2 * i][1], sg v[8 * hh + 2 * i + 1][1]);                                         \
    }                                                                                                                \
    *(LAS u32x4*)(ks + ((unsigned)(wp - ks) ^ (unsigned)(hh << 4))) = wr_;                                           \
    *(LAS u32x4*)(ks + ((unsigned)(wp - ks) ^ (unsigned)((2 + hh) << 4))) = wi_;                                     \
  }
#define KR(ks_) __builtin_bit_cast(f16x8, tr_pair(rp0 + (ks_) * 1024, rp1 + (ks_) * 1024))
    f16x8 bfr[4];
    cplx v[16], X[17], zp[9];
    v[0] = (cplx){1.f, 0.f};
    v[1] = z8;
    v[2] = cmul(z8, z8);
#pragma unroll
    for (int k = 3; k < 16; ++k) v[k] = cmul(v[k - 2], v[2]);
    KW(v, -);
    X[0] = (cplx){__builtin_amdgcn_ldexpf(cr, -ex), __builtin_amdgcn_ldexpf(ci, -ex)};
    X[1] = cmul(X[0], z128);
    {
      const cplx z256 = cmul(z128, z128);
#pragma unroll
      for (int r = 2; r < 17; ++r) X[r] = cmul(X[r - 2], z256);
    }
    zp[1] = z1; zp[8] = z8;
    zp[2] = cmul(z1, z1); zp[3] = cmul(zp[2], z1); zp[4] = cmul(zp[2], zp[2]);
    zp[5] = cmul(zp[4], z1); zp[6] = cmul(zp[4], zp[2]); zp[7] = cmul(zp[4], zp[3]);
#pragma unroll
    for (int k = 0; k < 4; ++k) bfr[k] = KR(k);
#pragma unroll
    for (int g = 0; g < 4; ++g) { v[4 * g] = X[4 + g]; v[4 * g + 1] = cmul(X[4 + g], zp[4]); v[4 * g + 2] = X[8 + g]; v[4 * g + 3] = X[12 + g]; }
    KW(v, );
#pragma unroll
    for (int j = 0; j < 16; ++j) v[j] = (j & 7) ? cmul(X[j >> 3], zp[j & 7]) : X[j >> 3];
    {
      f16x8 a[4];
#pragma unroll
      for (int k = 0; k < 4; ++k) a[k] = KR(k);
      KW(v, );
#pragma unroll
      for (int k = 0; k < 4; ++k) ta = MFMA16H(a[k], bfr[k], ta);
    }
#pragma unroll
    for (int a = 0; a < 2; ++a) {
      v[8 * a] = X[2 + a];
      v[8 * a + 1] = cmul(X[2 + a], zp[2]);
      v[8 * a + 2] = v[8 * a + 4] = cmul(X[2 + a], zp[4]);
      v[8 * a + 5] = cmul(X[2 + a], zp[6]);
      v[8 * a + 6] = cmul(X[2 + a], zp[8]);
      v[8 * a + 3] = v[8 * a + 7] = X[16];
    }
    {
      f16x8 a[4];
#pragma unroll
      for (int k = 0; k < 4; ++k) a[k] = KR(k);
      KW(v, );
#pragma unroll
      for (int k = 0; k < 4; ++k) tc_ = MFMA16H(a[k], bfr[k], tc_);
    }
#pragma unroll
    for (int k = 0; k < 4; ++k) { f16x8 a = KR(k); tb = MFMA16H(a, bfr[k], tb); }
#undef KW
#undef KR
    ta *= mult; tb *= mult; tc_ *= mult;
    TS(2);
  }

  if (wave < 4) {
    *(LAS f32x4*)(kc + lane * 16) = ta;
    *(LAS f32x4*)(kc + 1024 + lane * 16) = tb;
    *(LAS f32x4*)(kc + 2048 + lane * 16) = tc_;
    sig(FL + F_KC + c);
    __builtin_amdgcn_s_setprio(PRIO_C);
    const int lane = lane_id(), q4 = (lane & 15) >> 2, p4 = lane & 3, g4 = lane >> 4, c16 = lane & 15;
    const int mh = wave & 1, hd = wave >> 1;
    const int a0 = (mh * 64 + c16) * 128 + ((g4 ^ (c16 >> 1)) << 4);
    const int b0 = 16384 + (8 * g4 + q4) * 128 + (((hd * 4 + (p4 >> 1)) ^ (((q4 >> 1) | ((g4 & 1) << 1)) << 1)) << 4) + (p4 & 1) * 8;
    f32x4 acc[4][6];
#pragma unroll
    for (int m = 0; m < 4; ++m)
#pragma unroll
      for (int g = 0; g < 6; ++g) acc[m][g] = (f32x4){0.f, 0.f, 0.f, 0.f};
#pragma unroll
    for (int m = 0; m < 4; ++m)
      asm volatile("" : "+v"(acc[m][0]), "+v"(acc[m][1]), "+v"(acc[m][2]), "+v"(acc[m][3]), "+v"(acc[m][4]), "+v"(acc[m][5]));
    bf16x8 fa[2][4];
    u32x2 bl[6], bh[6];
#define RA(set, m, pa) fa[set][m] = *(LAS bf16x8*)((pa) + (m) * 2048)
#define RL(j, pe, po) bl[(j) % 6] = tr_read((((j) & 1) ? (po) : (pe)) + (((j) % 6) >> 1) * 8192 + ((j) / 6) * 4096)
#define RH(j, pe, po) bh[(j) % 6] = tr_read((((j) & 1) ? (po) : (pe)) + (((j) % 6) >> 1) * 8192 + ((j) / 6) * 4096 + 512)
#define FB(j) asbf(join(bl[(j) % 6], bh[(j) % 6]))
#define MM(j, m) acc[m][(j) % 6] = ((j) % 6 < 2) ? MFMA16(FB(j), fa[(j) / 6][m], acc[m][(j) % 6]) : MFMA16(fa[(j) / 6][m], FB(j), acc[m][(j) % 6])
#define GRP(j, X0, X1, X2, X3)                \
  do {                                        \
    MM(j, 0); X0; SB(); MM(j, 1); X1; SB();   \
    MM(j, 2); X2; SB(); MM(j, 3); X3; SB();   \
  } while (0)
    LAS char* pa = sm + a0;
    LAS char* pa1 = sm + (a0 ^ 64);
    LAS char* pe = sm + b0;
    LAS char* po = sm + (b0 ^ 32);
    LBAR();
    TS(3); TC(0);
    RA(0, 0, pa); RL(0, pe, po); RH(0, pe, po); RA(0, 1, pa); RA(0, 2, pa); RA(0, 3, pa);
    RL(1, pe, po); RH(1, pe, po); RL(2, pe, po); RH(2, pe, po);
    {
      int slot = 0;
#pragma unroll 1
      for (int t = 0; t < 16; ++t) {
        const int dl = (slot == 2) ? -2 * SLOT : SLOT;
        slot = (slot == 2) ? 0 : slot + 1;
        SB();
        GRP(0, RL(3, pe, po), RH(3, pe, po), , );
        GRP(1, RL(4, pe, po), RH(4, pe, po), , );
        GRP(2, RL(5, pe, po), RH(5, pe, po), RA(1, 0, pa1), RA(1, 1, pa1));
        GRP(3, RL(6, pe, po), RH(6, pe, po), RA(1, 2, pa1), RA(1, 3, pa1));
        GRP(4, RL(7, pe, po), RH(7, pe, po), RL(8, pe, po), RH(8, pe, po));
        GRP(5, RL(9, pe, po), RH(9, pe, po), RL(10, pe, po), RH(10, pe, po));
        GRP(6, RL(11, pe, po), RH(11, pe, po), , );
        GRP(7, , , , );
        GRP(8, pa += dl, pa1 += dl, pe += dl, po += dl);
        asm volatile("" : "+v"(pa), "+v"(pa1), "+v"(pe), "+v"(po));
        LBAR();
        SB();
        GRP(9, RA(0, 0, pa), RL(0, pe, po), RH(0, pe, po), RA(0, 1, pa));
        GRP(10, RA(0, 2, pa), RA(0, 3, pa), RL(1, pe, po), RH(1, pe, po));
        GRP(11, RL(2, pe, po), RH(2, pe, po), , );
      }
    }
#undef RA
#undef RL
#undef RH
#undef FB
#undef MM
#undef GRP
    TS(4); TC(1);
    {
    const int lane = lane_id(), g4 = lane >> 4, c16 = lane & 15;
    const int head = hp * 2 + hd, headq = hq * 2 + hd;
    {
      f32x4 kvt[2][2], zt[2];
#pragma unroll
      for (int i = 0; i < 2; ++i) { kvt[i][0] = kvt[i][1] = zt[i] = (f32x4){0.f, 0.f, 0.f, 0.f}; }
      const u32x4 ones = {0x3F803F80u, 0x3F803F80u, 0x3F803F80u, 0x3F803F80u};
#pragma unroll
      for (int j = 0; j < 2; ++j) {
        u32x4 fk[2], fv[2];
#pragma unroll
        for (int n = 0; n < 2; ++n)
#pragma unroll
          for (int i = 0; i < 4; ++i) {
            const f32x4 kk = acc[2 * j + (i >> 1)][2 + n], vv = acc[2 * j + (i >> 1)][4 + n];
            fk[n][i] = pk_bf16(phi_f<false>(kk[2 * (i & 1)]), phi_f<false>(kk[2 * (i & 1) + 1]));
            fv[n][i] = pk_bf16(vv[2 * (i & 1)], vv[2 * (i & 1) + 1]);
          }
#pragma unroll
        for (int td = 0; td < 2; ++td) {
          kvt[0][td] = MFMA16(asbf(fk[td]), asbf(fv[0]), kvt[0][td]);
          kvt[1][td] = MFMA16(asbf(fk[td]), asbf(fv[1]), kvt[1][td]);
          zt[td] = MFMA16(asbf(ones), asbf(fk[td]), zt[td]);
        }
      }
      LAS char* xo = sm + L_X + wave * 2176;
      LAS char* xi = sm + L_X + (wave ^ 1) * 2176;
      f32x4 keep[2];
#pragma unroll
      for (int td = 0; td < 2; ++td) {
        f32x4 ka = kvt[0][td], kb = kvt[1][td];
        asm("" : "+v"(ka), "+v"(kb));
        *(LAS f32x4*)(xo + td * 1024 + lane * 16) = mh ? ka : kb;
        keep[td] = mh ? kb : ka;
      }
      *(LAS float*)(xo + 2048 + (lane & 31) * 4) = (g4 & 1) ? zt[1][0] : zt[0][0];
      sig(FL + F_XE + hd);
      waitge(FL + F_XE + hd, 2);
      if (mh == 0) {
        f32x4 zz = *(LAS f32x4*)(xo + 2048 + (lane & 7) * 16) + *(LAS f32x4*)(xi + 2048 + (lane & 7) * 16);
        if (lane < 8) st16(tag_put(zz, tag), rw, lane * 16, WS_ZP + (head * 16 + mb) * 128);
      }
      const int so = WS_KVP + (hp * 16 + hd * 8 + mh * 4) * 8192 + mb * 512;
#pragma unroll
      for (int td = 0; td < 2; ++td) {
        f32x4 o = *(LAS f32x4*)(xi + td * 1024 + lane * 16) + keep[td];
        st16(tag_put(o, tag), rw, td * 16384 + (lane >> 5) * 8192 + (lane & 31) * 16, so);
      }
      sig(FL + F_XP);
    }
    TS(5);
#pragma unroll
    for (int m = 0; m < 4; ++m)
#pragma unroll
      for (int i = 0; i < 8; ++i) acc[m][i >> 2][i & 3] = phi_f<true>(acc[m][i >> 2][i & 3]);
    asm volatile("" : "+v"(acc[0][0]), "+v"(acc[1][0]), "+v"(acc[2][0]), "+v"(acc[3][0]),
                      "+v"(acc[0][1]), "+v"(acc[1][1]), "+v"(acc[2][1]), "+v"(acc[3][1]));
    u32x4 fq[4];
    {
      LAS f32x4* zl = (LAS f32x4*)(sm + L_Z + hd * 128);
      if (mh == 1) {
        u32x4 za = {0u, 0u, 0u, 0u}, zb = za;
        bool na = true, nb_ = true;
#pragma unroll 1
        for (int it = 0; it < (1 << 17); ++it) {
          CBAR();
          if (na) za = ld16(rw, lane * 16, WS_ZP + headq * 2048);
          if (nb_) zb = ld16(rw, 1024 + lane * 16, WS_ZP + headq * 2048);
          na = tag_get(za) != tag; nb_ = tag_get(zb) != tag;
          if (!__any(na || nb_)) break;
          __builtin_amdgcn_s_sleep(PSLEEP);
        }
        const f32x4 fa_ = tag_val(za), fb_ = tag_val(zb);
        f32x4 z;
#pragma unroll
        for (int i = 0; i < 4; ++i) { float s = fa_[i] + fb_[i]; asm("" : "+v"(s)); z[i] = sum32(sum16(sum8(s))); }
        if (lane < 8) zl[lane] = z;
        sig(FL + F_Z + hd);
      } else {
        waitge(FL + F_Z + hd, 1);
      }
      TS(6);
      const f32x4 z0 = zl[g4], z1 = zl[4 + g4];
#pragma unroll
      for (int m = 0; m < 4; ++m) {
        float d0 = acc[m][0][0] * z0[0], d1 = acc[m][1][0] * z1[0];
#pragma unroll
        for (int i = 1; i < 4; ++i) { d0 = __builtin_fmaf(acc[m][0][i], z0[i], d0); d1 = __builtin_fmaf(acc[m][1][i], z1[i], d1); }
        const float den = sum32(sum16(d0 + d1));
        const float rc = __builtin_amdgcn_rcpf(den + 1e-6f);
        float e[8];
#pragma unroll
        for (int i = 0; i < 8; ++i) { e[i] = acc[m][i >> 2][i & 3] * rc; asm("" : "+v"(e[i])); }
        fq[m] = (u32x4){pk_bf16(e[0], e[1]), pk_bf16(e[2], e[3]), pk_bf16(e[4], e[5]), pk_bf16(e[6], e[7])};
      }
    }
    asm volatile("" : "+v"(fq[0]), "+v"(fq[1]), "+v"(fq[2]), "+v"(fq[3]));
    TS(13);
    {
      waitge(FL + F_KV + hd, 2);
      TS(14);
      u32x4 ka[2];
#pragma unroll
      for (int te = 0; te < 2; ++te) ka[te] = *(LAS u32x4*)(sm + L_X + (hd * 2 + te) * 1024 + lane * 16);
#pragma unroll
      for (int m = 0; m < 4; ++m) {
        const f32x4 z4 = {0.f, 0.f, 0.f, 0.f};
        const f32x4 d0 = MFMA16(asbf(ka[0]), asbf(fq[m]), z4), d1 = MFMA16(asbf(ka[1]), asbf(fq[m]), z4);
        const u32x4 ov = {pk_bf16(d0[0], d0[1]), pk_bf16(d0[2], d0[3]), pk_bf16(d1[0], d1[1]), pk_bf16(d1[2], d1[3])};
        st16(ov, rw, (m & 1) * 1024 + lane * 16, WS_O + ((mb * 4 + mh * 2 + (m >> 1)) * 32 + headq) * 2048);
      }
    }
    TS(7);
    WAITVM(0);
    if (sig_ret(FL + F_OD) == 3u) {
      gflag(ws, WS_FP + (mb * 16 + hq) * 128, tag, lane);
      TS(8);
      gpoll(ws + WS_FP + ((mb2 * 16 + c16) << 7), tag);
      sig(FL + F_OS);
    } else {
      TS(8);
      waitge(FL + F_OS, 1);
    }
    TS(9);
    {
      const int pqs = WS_O + (mb2 * 4 + wave) * 65536;
      u32x4 pb[NRING][2];
#define LDQ(i_) do { const int so_ = pqs + ((i_) << 11);                                             \
        pb[(i_) % NRING][0] = __builtin_amdgcn_raw_buffer_load_b128(rw, lane * 16, so_, QAUX);         \
        pb[(i_) % NRING][1] = __builtin_amdgcn_raw_buffer_load_b128(rw, 1024 + lane * 16, so_, QAUX); } while (0)
#pragma unroll
      for (int i = 0; i < NRING; ++i) LDQ(i);
      f32x4 oc[2][4];
#pragma unroll
      for (int s = 0; s < 2; ++s)
#pragma unroll
        for (int t = 0; t < 4; ++t) oc[s][t] = (f32x4){0.f, 0.f, 0.f, 0.f};
      asm volatile("" : "+v"(oc[0][0]), "+v"(oc[0][1]), "+v"(oc[0][2]), "+v"(oc[0][3]),
                   "+v"(oc[1][0]), "+v"(oc[1][1]), "+v"(oc[1][2]), "+v"(oc[1][3]));
      LAS char* mp0 = sm + lane * 16;
      bf16x8 ma[4];
      waitge(FL + F_LW, 4);
      if (bid == 0 && wave == 0) gflag(ws, WS_EPOCH, tag, lane);
      TS(10); TC(2);
#pragma unroll
      for (int t = 0; t < 4; ++t) ma[t] = *(LAS bf16x8*)(mp0 + t * 1024);
#define SPL 1
#define LDH(i_, h_) pb[(i_) % NRING][h_] = __builtin_amdgcn_raw_buffer_load_b128(rw, (h_) * 1024 + lane * 16, pqs + ((i_) << 11), QAUX)
#pragma unroll
      for (int i = 0; i < 32; ++i) {
        SB();
        const u32x4 b0_ = pb[i % NRING][0], b1_ = pb[i % NRING][1];
#pragma unroll
        for (int t = 0; t < 4; ++t) {
          AMFMA16(oc[0][t], ma[t], b0_);
          if (i > 0 && i - 1 + NRING < 32 && (t & 1) == (SPL - 1)) LDH(i - 1 + NRING, t >> 1);
          SB();
          AMFMA16(oc[1][t], ma[t], b1_);
          if (i < 31) ma[t] = *(LAS bf16x8*)(mp0 + (i + 1) * 4096 + t * 1024);
          SB();
        }
      }
#undef LDH
#undef LDQ
      asm volatile("s_nop 7\n\ts_nop 3" : "+v"(oc[0][0]), "+v"(oc[0][1]), "+v"(oc[0][2]), "+v"(oc[0][3]),
                   "+v"(oc[1][0]), "+v"(oc[1][1]), "+v"(oc[1][2]), "+v"(oc[1][3]));
      TS(11); TC(3);
#pragma unroll
      for (int s = 0; s < 2; ++s)
#pragma unroll
        for (int j = 0; j < 2; ++j) {
          u32x4 w = {pk_bf16(oc[s][2 * j][0], oc[s][2 * j][1]), pk_bf16(oc[s][2 * j][2], oc[s][2 * j][3]),
                     pk_bf16(oc[s][2 * j + 1][0], oc[s][2 * j + 1][1]), pk_bf16(oc[s][2 * j + 1][2], oc[s][2 * j + 1][3])};
          ost16(out + (size_t)((mb2 * 4 + wave) * 32 + s * 16 + c16) * 2048 + nb * 64 + 32 * j + 8 * g4, w);
        }
    }
    TS(12);
    TSDUMP();
    }
    return;
  }

  const int cg = bid >> 4, tt16 = bid & 15;
  const int rg = lane >> 4;
  bf16x8 F[16];
  {
    waitge(FL + F_KC + c, 1);
    {
      const f32x4 oa = *(LAS f32x4*)(kc + lane * 16), ob = *(LAS f32x4*)(kc + 1024 + lane * 16), oc = *(LAS f32x4*)(kc + 2048 + lane * 16);
      LBAR();
      TS(1);
#pragma unroll
      for (int i = 0; i < 4; ++i) {
        float x0 = ta[i] + oa[i], x1 = tb[i] + ob[i], x2 = tc_[i] + oc[i];
        asm volatile("" : "+v"(x0)); asm volatile("" : "+v"(x1)); asm volatile("" : "+v"(x2));
        ta[i] = x0; tb[i] = x1; tc_[i] = x2;
      }
    }
#define ROL1(v) (unsigned)__builtin_amdgcn_update_dpp(0, (int)(v), 0x134, 0xf, 0xf, false)
#define MIX(lo, hi) __builtin_amdgcn_perm((hi), (lo), 0x07060100u)
    const bool l0 = (lane == 0);
    const u32x2 w0 = {pk_bf16(tc_[0] + (l0 ? v_dsk : 0.f), tc_[1]), pk_bf16(tc_[2], tc_[3])};
    const u32x2 w1 = {pk_bf16(tb[0], tb[0]), pk_bf16(tb[1], tb[2])};
    const unsigned pa0 = pk_bf16(ta[0], ta[0]), pa1 = pk_bf16(ta[1], ta[1]), pa2 = pk_bf16(ta[2], ta[2]), pa3 = pk_bf16(ta[3], ta[3]);
    const unsigned pe = pk_bf16(tb[3], tb[3]);
    const unsigned n0 = ROL1(l0 ? pa2 : pa0), n2 = ROL1(l0 ? pa3 : pa2), n3 = ROL1(l0 ? pe : pa3);
    const u32x4 w2 = {pa0, MIX(pa0, pa1), pa1, n0};
    const u32x4 w3 = {pa2, pa2, (lane & 1) ? n2 : MIX(pa2, n2), n2};
    const u32x4 w4 = {pa3, pa3, (lane & 1) ? n3 : MIX(pa3, n3), n3};
#undef ROL1
#undef MIX
    LAS char* K = ub + 32;
    const int k01 = 2 * (128 * (rg >> 1) + 8 * c16 + 4 * (rg & 1));
    BAR();
    TS(4);
    *(LAS u32x2*)(K + k01) = w0;
    *(LAS u32x2*)(K + 512 + k01) = w1;
    BAR();
    TS(5);
    *(LAS u32x4*)(K + 1024 + lane * 16) = w2;
    BAR();
    TS(6);
    *(LAS u32x4*)(K + 2048 + lane * 16) = w3;
    BAR();
    TS(7);
    *(LAS u32x4*)(K + 3072 + lane * 16) = w4;
    BAR();
    TS(13);
    if (lane < 8) *(LAS unsigned*)(ub + lane * 4) = 0u;
    LAS char* fbp = ub + 2 * (16 - 12 + 32 * rg + 4 * (q4 + p4));
#pragma unroll
    for (int s = 0; s < 16; ++s) {
      if ((s & 3) == 0) { BAR(); TS(s == 0 ? 14 : 15 + (s >> 2)); }
      F[s] = tr_frag(fbp + 256 * s, fbp + 256 * s + 32);
    }
    LBAR();
    TS(3);
    *(LAS unsigned*)(ub + lane * 4) = 0u;
    BAR();
    TS(19);
    BAR();
    TS(20);
    BAR();
    TS(21);
    BAR();
    TS(22);
    BAR();
    BAR();
    waitge(FL + F_XP, 4);
  }
  TS(8); TC(2);
  unsigned yp[16];
  {
    LAS char* gb = ub + 2 * (128 + 16 * p4 + 12 - 32 * rg - 4 * q4);
    float pc1 = 0.f, pc2 = 0.f, pc3 = 0.f, yprev = 0.f;
    const bool l0 = (lane == 0);
#define ROR1(v) __int_as_float(__builtin_amdgcn_update_dpp(0, __float_as_int(v), 0x13C, 0xf, 0xf, false))
#define FIN(i)                                                                       \
  do {                                                                               \
    float r3 = ROR1(ca[i][3]);                                                       \
    float s2 = ca[i][2] + (l0 ? pc3 : r3);                                           \
    float r2 = ROR1(s2);                                                             \
    float s1 = ca[i][1] + (l0 ? pc2 : r2);                                           \
    float r1 = ROR1(s1);                                                             \
    float y = ca[i][0] + (l0 ? pc1 : r1);                                            \
    pc1 = r1; pc2 = r2; pc3 = r3;                                                    \
    if ((i) & 1) yp[8 * pass + ((i) >> 1)] = pk_bf16(yprev, y); else yprev = y;      \
  } while (0)
#pragma unroll
    for (int pass = 0; pass < 2; ++pass) {
      f32x4 ca[16];
#pragma unroll
      for (int i = 0; i < 16; ++i) ca[i] = (f32x4){0, 0, 0, 0};
      bf16x8 G = tr_frag(gb, gb - 32);
#pragma unroll
      for (int v = 0; v < 16 * pass + 16; ++v) {
        bf16x8 Gn = G;
        if (v + 1 < 16 * pass + 16) Gn = tr_frag(gb + 128 * (v + 1), gb + 128 * (v + 1) - 32);
        SB();
#pragma unroll
        for (int i = 0; i < 16; ++i) {
          const int T = 16 * pass + i;
          if (T >= v && ((T - v) & 1) == 0) ca[i] = MFMA16(G, F[(T - v) >> 1], ca[i]);
        }
        if (v - 1 >= 16 * pass) FIN(v - 1 - 16 * pass);
        SB();
        G = Gn;
      }
      FIN(15);
      CBAR();
      SB();
    }
#undef FIN
#undef ROR1
  }
  CBAR();
  TS(9); TC(3);
  {
  const int lane = lane_id();
#pragma unroll
  for (int i = 0; i < 16; ++i) {
    ((LAS u16*)ub)[128 * i + lane] = (u16)yp[i];
    ((LAS u16*)ub)[128 * i + 64 + lane] = (u16)(yp[i] >> 16);
  }
  CBAR();
  {
    u32x4 yv[4];
#pragma unroll
    for (int i = 0; i < 4; ++i) yv[i] = *(LAS u32x4*)(ub + i * 1024 + lane * 16);
#pragma unroll
    for (int i = 0; i < 4; ++i) st16(yv[i], rw, i * 1024 + lane * 16, WS_YT + d * 4096);
  }
  WAITVM(0);
  if (sig_ret(FL + F_CY) == 3u) {
    gflag(ws, WS_FY + (cg * 16 + tt16) * 128, tag, lane);
    TS(10);
    gpoll(ws + WS_FY + ((cg * 16 + (lane & 15)) << 7), tag);
    sig(FL + F_Y);
  } else {
    TS(10);
    waitge(FL + F_Y, 1);
  }
  TS(11);
  {
    const int q4 = (lane & 15) >> 2, p4 = lane & 3, g4 = lane >> 4;
    LAS char* tb = sm + L_UB;
    u32x4 yr[4];
#pragma unroll
    for (int i = 0; i < 4; ++i)
      yr[i] = ld16(rw, (16 * c + 4 * i + g4) * 4096 + (lane & 15) * 16, WS_YT + cg * 64 * 4096 + tt16 * 256);
#pragma unroll
    for (int i = 0; i < 4; ++i) {
      const int row = 16 * c + 4 * i + g4;
      const int f = (row & 3) | (((row >> 3) & 1) << 2);
      *(LAS u32x4*)(tb + row * 256 + (((lane & 15) ^ (f << 1)) << 4)) = yr[i];
    }
    sig(FL + F_T2);
    waitge(FL + F_T2, 4);
    const int f = q4 | ((g4 & 1) << 2);
#pragma unroll
    for (int j = 0; j < 4; ++j) {
      const int tbk = 2 * c + (j >> 1), hf = j & 1;
      LAS char* pp = tb + (32 * hf + 8 * g4 + q4) * 256 + (((2 * tbk + (p4 >> 1)) ^ (f << 1)) << 4) + (p4 & 1) * 8;
      u32x4 v = tr_pair(pp, pp + 1024);
      ost16(out + (size_t)(tt16 * 128 + 16 * tbk + (lane & 15)) * 2048 + 1024 + cg * 64 + 32 * hf + 8 * g4, v);
    }
  }
  TS(12);
  TSDUMP();
  }
}

extern "C" void kernel_launch(void* const* d_in, const int* in_sizes, int n_in, void* d_out, int out_size,
                              void* d_ws, size_t ws_size, hipStream_t stream) {
  fused_kernel<<<256, 768, 0, stream>>>((const u16*)d_in[0], (const u16*)d_in[1], (const u16*)d_in[2], (const u16*)d_in[3],
                                        (const u16*)d_in[4], (const u16*)d_in[5], (const u16*)d_in[6], (const u16*)d_in[7],
                                        (const u16*)d_in[8], (const u16*)d_in[9], (u16*)d_out, (char*)d_ws);
}
```

```cpp
#include <hip/hip_runtime.h>
#include <stdint.h>
#pragma clang diagnostic ignored "-Winline-asm"

#define LAS __attribute__((address_space(3)))
#define GAS __attribute__((address_space(1)))
#define DI __device__ __forceinline__

typedef unsigned short u16;
typedef short s16x4 __attribute__((ext_vector_type(4)));
typedef __bf16 bf16x2 __attribute__((ext_vector_type(2)));
typedef __bf16 bf16x8 __attribute__((ext_vector_type(8)));
typedef _Float16 f16x2 __attribute__((ext_vector_type(2)));
typedef _Float16 f16x8 __attribute__((ext_vector_type(8)));
typedef float f32x2 __attribute__((ext_vector_type(2)));
typedef float f32x4 __attribute__((ext_vector_type(4)));
typedef float f32x16 __attribute__((ext_vector_type(16)));
typedef unsigned u32x2 __attribute__((ext_vector_type(2)));
typedef unsigned u32x4 __attribute__((ext_vector_type(4)));
typedef __amdgpu_buffer_rsrc_t rsrc_t;

#define CBAR() asm volatile("" ::: "memory")
#define SB() __builtin_amdgcn_sched_barrier(0)
#define WAITVM(n) asm volatile("s_waitcnt vmcnt(" #n ")" ::: "memory")
#define WAITVMX(n) WAITVM(n)
#define WAITVL(n) asm volatile("s_waitcnt vmcnt(" #n ") lgkmcnt(0)" ::: "memory")
#define BAR() asm volatile("s_barrier" ::: "memory")
#define LBAR() do { CBAR(); __builtin_amdgcn_s_waitcnt(0xC07F); asm volatile("s_barrier" ::: "memory"); } while (0)
#define TSDECL
#define TS(i)
#define TC(i)
#define TSDUMP()
#define KOFF (4 * (xcd >> 1) + 8 * (xcd & 1))
#define KXOFF (4 * (xcd >> 1) + 8 * (xcd & 1))
#define PRIO_C 2
#define PRIO_L 3
#define PSLEEP 3
#define CODE_LEN 30912u
#define PFM 15
#define EBAR
#define WT 2
#define NRING 10
#define PRE 3
#define PD 0
#define SB1 0
#define ORD 6
#define NWB 2
#define RT0 (6 + NWB)
#define UE 1
#define PRIO_SH 3
#define FSPLIT 0
#define FUB 11
#define PT 4
#define HQX 1
#define KL 0
#define KD 0
#define FB1 1
#define PAUX 0
#define TS8 1
#define EX 2
#define EW 2
#define LX 0
#define LT 1
#define XD 5
#define RLX 0
#define WL0 12
#define WL1 14
#define KPW 5
#define XUT 0
#define XWT 0
#define NP 5
#define NP4 20
#define LRF 1
#define EARLY 1
#define PW 0
#define PWAIT 0
#define YW 0
#define OT 0
#define OD 0
#define OAUX SC1
#define QAUX SC1
#define QINV 0

DI void ost16(void* p, u32x4 v) { asm volatile("global_store_dwordx4 %0, %1, off sc1\n\ts_nop 1" :: "v"(p), "v"(v) : "memory"); }
DI float sum16(float x) {
  auto r = __builtin_amdgcn_permlane16_swap(__float_as_uint(x), __float_as_uint(x), false, false);
  float s = __uint_as_float(r[0]) + __uint_as_float(r[1]);
  asm("" : "+v"(s));
  return s;
}
DI float sum32(float x) {
  auto r = __builtin_amdgcn_permlane32_swap(__float_as_uint(x), __float_as_uint(x), false, false);
  float s = __uint_as_float(r[0]) + __uint_as_float(r[1]);
  asm("" : "+v"(s));
  return s;
}
DI float sum8(float x) {
  return x + __int_as_float(__builtin_amdgcn_update_dpp(0, __float_as_int(x), 0x128, 0xf, 0xf, false));
}
DI float wave_max(float x) {
#define DPPMAX(ctl) x = __builtin_fmaxf(x, __int_as_float(__builtin_amdgcn_update_dpp(0, __float_as_int(x), ctl, 0xf, 0xf, false)))
  DPPMAX(0xB1); DPPMAX(0x4E); DPPMAX(0x141); DPPMAX(0x140);
#undef DPPMAX
  auto r = __builtin_amdgcn_permlane16_swap(__float_as_uint(x), __float_as_uint(x), false, false);
  x = __builtin_fmaxf(__uint_as_float(r[0]), __uint_as_float(r[1]));
  r = __builtin_amdgcn_permlane32_swap(__float_as_uint(x), __float_as_uint(x), false, false);
  return __builtin_fmaxf(__uint_as_float(r[0]), __uint_as_float(r[1]));
}
DI int lane_id() {
  int l = __builtin_amdgcn_mbcnt_hi(~0u, __builtin_amdgcn_mbcnt_lo(~0u, 0u));
  asm volatile("" : "+v"(l));
  __builtin_assume(l >= 0 && l < 64);
  return l;
}
DI float bf2f(u16 v) { return __uint_as_float((unsigned)v << 16); }
DI unsigned pk_bf16(float a, float b) {
  f32x2 f = {a, b};
  return __builtin_bit_cast(unsigned, __builtin_convertvector(f, bf16x2));
}
DI unsigned pk_f16(float a, float b) {
  f32x2 f = {a, b};
  return __builtin_bit_cast(unsigned, __builtin_convertvector(f, f16x2));
}
DI float rbf(float a) { return __uint_as_float(pk_bf16(a, 0.f) << 16); }
DI u32x2 tr_read(LAS char* p) {
  s16x4 v = __builtin_amdgcn_ds_read_tr16_b64_v4i16((LAS s16x4*)p);
  return __builtin_bit_cast(u32x2, v);
}
DI u32x4 join(u32x2 a, u32x2 b) { u32x4 r = {a.x, a.y, b.x, b.y}; return r; }
DI u32x4 tr_pair(LAS char* p0, LAS char* p1) { return join(tr_read(p0), tr_read(p1)); }
DI bf16x8 asbf(u32x4 v) { return __builtin_bit_cast(bf16x8, v); }
DI bf16x8 tr_frag(LAS char* p0, LAS char* p1) { return asbf(tr_pair(p0, p1)); }
#define MFMA32(a, b, c) __builtin_amdgcn_mfma_f32_32x32x16_bf16((a), (b), (c), 0, 0, 0)
#define MFMA16(a, b, c) __builtin_amdgcn_mfma_f32_16x16x32_bf16((a), (b), (c), 0, 0, 0)
#define MFMA16H(a, b, c) __builtin_amdgcn_mfma_f32_16x16x32_f16((a), (b), (c), 0, 0, 0)
#define AMFMA16(c, a, b) asm volatile("v_mfma_f32_16x16x32_bf16 %0, %1, %2, %0" : "+v"(c) : "v"(a), "v"(b))
#define GLDS(src, dst) __builtin_amdgcn_global_load_lds((const GAS unsigned*)(src), (LAS unsigned*)(dst), 16, 0, 0)
#define XAUX 0
#define WAUX 0
#define UAUX 2
#define GLDSA(src, dst, aux) __builtin_amdgcn_global_load_lds((const GAS unsigned*)(src), (LAS unsigned*)(dst), 16, 0, aux)
#define SC1 16
#define DMA16(voff_, sbase_, lds_, aux_) asm volatile("s_mov_b32 m0, %2\n\ts_nop 0\n\tglobal_load_lds_dwordx4 %0, %1" aux_ :: "v"(voff_), "s"(sbase_), "s"(lds_) : "memory", "m0")
DI u32x4 ld16(rsrc_t r, int voff, int soff) { return __builtin_amdgcn_raw_buffer_load_b128(r, voff, soff, SC1); }
DI void st16(u32x4 v, rsrc_t r, int voff, int soff) {
  __builtin_amdgcn_raw_buffer_store_b128(v, r, voff, soff, SC1);
  asm volatile("s_nop 1" :: "v"(v) : "memory");
}

constexpr int WS_EPOCH = 0;
constexpr int WS_FY = 65536, WS_FU = 131072, WS_FP = WS_FU + 131072;
constexpr int WS_O = 524288;
constexpr int WS_UT = WS_O + (4 << 20);
#define YU 0
constexpr int WS_YT = WS_UT + (YU ? 0 : (4 << 20));
constexpr int WS_WT = WS_UT + (8 << 20);
constexpr int WS_KVP = WS_WT + (2 << 20);
constexpr int WS_ZP = WS_KVP + 16 * 16 * 16 * 512;
constexpr int WS_KVR = WS_ZP + 32 * 16 * 128;
constexpr int WS_END = WS_KVR + 32 * 4096;

constexpr int SLOT = 40960;
constexpr int L_UB = 131072, UB_CH = 4352;
constexpr int L_X = L_UB + 4 * UB_CH;
constexpr int L_Z = L_X + 4 * 2176;
constexpr int L_FLAG = L_Z + 512;
constexpr int L_DUM = L_FLAG + 256;
constexpr int LDS_BYTES = L_DUM + 256;
enum { F_KC = 0, F_XE = 8, F_LW = 11, F_CY, F_T2, F_XP, F_KV = 16, F_OD = 18, F_OS, F_Z = 24, F_Y = 26, F_LIST = 32 };

typedef f32x2 cplx;
DI cplx cmul(cplx a, cplx b) {
  cplx t, r;
  asm("v_pk_mul_f32 %0, %1, %2 op_sel:[1,1] op_sel_hi:[1,0] neg_lo:[1,0]" : "=v"(t) : "v"(a), "v"(b));
  asm("v_pk_fma_f32 %0, %1, %2, %3 op_sel:[0,0,0] op_sel_hi:[0,1,1]" : "=v"(r) : "v"(a), "v"(b), "v"(t));
  return r;
}
DI cplx zpow(float arl2, float ai, float p) {
  const float INV2PI = 0.15915494309189535f;
  const float INV2PI_LO = (float)(0.15915494309189533576888 - (double)0.15915494309189535f);
  float e = __builtin_amdgcn_exp2f(arl2 * p);
  float r = ai * p;
  float hi = r * INV2PI;
  float lo = __builtin_fmaf(r, INV2PI_LO, __builtin_fmaf(r, INV2PI, -hi));
  float f = (hi - __builtin_rintf(hi)) + lo;
  cplx z = {e * __builtin_amdgcn_cosf(f), e * __builtin_amdgcn_sinf(f)};
  return z;
}
template <bool NOPK> DI float phi_f(float t) {
  float a = t * 1.4426950408889634f, b = t + 1.f;
  if (NOPK) { asm("" : "+v"(a)); asm("" : "+v"(b)); }
  float r = __builtin_amdgcn_fmed3f(__builtin_amdgcn_exp2f(a), b, 1.f) + 1e-6f;
  if (NOPK) asm("" : "+v"(r));
  return r;
}
DI u32x4 tag_put(f32x4 v, unsigned tag) {
  u32x4 b = __builtin_bit_cast(u32x4, v), r;
  r[0] = __builtin_amdgcn_perm(b[0], tag, 0x07060500u);
  r[1] = __builtin_amdgcn_perm(b[1], tag, 0x07060501u);
  r[2] = __builtin_amdgcn_perm(b[2], tag, 0x07060502u);
  r[3] = __builtin_amdgcn_perm(b[3], tag, 0x07060503u);
  return r;
}
DI unsigned tag_get(u32x4 b) {
  unsigned lo = __builtin_amdgcn_perm(b[1], b[0], 0x0c0c0400u), hi = __builtin_amdgcn_perm(b[3], b[2], 0x04000c0cu);
  return lo | hi;
}
DI f32x4 tag_val(u32x4 b) { return __builtin_bit_cast(f32x4, b & 0xffffff00u); }

DI void sig(LAS unsigned* f) {
  asm volatile("s_mov_b64 exec, 1\n\tds_add_u32 %0, %1\n\ts_mov_b64 exec, -1" :: "v"((unsigned)(size_t)f), "v"(1u) : "memory");
}
DI unsigned sig_ret(LAS unsigned* f) {
  unsigned r;
  asm volatile("s_mov_b64 exec, 1\n\tds_add_rtn_u32 %0, %1, %2\n\ts_mov_b64 exec, -1\n\ts_waitcnt lgkmcnt(0)"
               : "=&v"(r) : "v"((unsigned)(size_t)f), "v"(1u) : "memory");
  return __builtin_amdgcn_readfirstlane(r);
}
DI unsigned ldsword(LAS unsigned* f) { return __builtin_amdgcn_readfirstlane(*(volatile LAS unsigned*)f); }
DI void waitge(LAS unsigned* f, unsigned n) {
  CBAR();
#pragma unroll 1
  for (int it = 0; it < (1 << 22); ++it) {
    if (ldsword(f) >= n) break;
    __builtin_amdgcn_s_sleep(1);
  }
  CBAR();
}
DI void gflag(char* ws, int off, unsigned tag, int lane) {
  CBAR();
  if (lane < 32) __hip_atomic_store((unsigned*)(ws + off) + lane, tag, __ATOMIC_RELAXED, __HIP_MEMORY_SCOPE_AGENT);
  CBAR();
}
DI void gpoll(char* p, unsigned tag) {
  CBAR();
  unsigned v = ~tag;
#pragma unroll 1
  for (int it = 0; it < (1 << 18); ++it) {
    if (v != tag) v = __hip_atomic_load((unsigned*)p, __ATOMIC_RELAXED, __HIP_MEMORY_SCOPE_AGENT);
    if (__all(v == tag)) break;
    __builtin_amdgcn_s_sleep(PSLEEP);
  }
  CBAR();
}

__global__ __launch_bounds__(768, 3) void fused_kernel(const u16* __restrict__ x, const u16* __restrict__ wqkv,
                                                       const u16* __restrict__ wo, const u16* __restrict__ u,
                                                       const u16* __restrict__ lar, const u16* __restrict__ aim,
                                                       const u16* __restrict__ cre, const u16* __restrict__ cim,
                                                       const u16* __restrict__ ldt, const u16* __restrict__ dsk,
                                                       u16* __restrict__ out, char* __restrict__ ws) {
  __shared__ __attribute__((aligned(16))) char smem[LDS_BYTES];
  LAS char* sm = (LAS char*)smem;
  LAS unsigned* FL = (LAS unsigned*)(sm + L_FLAG);
  const int tid = threadIdx.x, lane = tid & 63;
  const int wave = __builtin_amdgcn_readfirstlane(tid >> 6);
  const int bid = blockIdx.x, xcd = bid & 7, idx = bid >> 3;
  const int ml = idx & 7, hl = idx >> 3;
  const int mb = (xcd & 1) * 8 + ml, hp = (xcd >> 1) * 4 + hl;
  const int hq = hp ^ HQX;
  const int mb2 = (xcd & 3) * 4 + (idx & 3), nb = (xcd >> 2) * 8 + (idx >> 2);
  unsigned pf = 0u;
  {
    const unsigned po = (unsigned)(tid * 64 + (idx & PFM) * 4096) | (tid >= 64 ? 0x40000000u : 0u);
    asm volatile("v_cmp_gt_u32 vcc, %3, %1\n\ts_mov_b64 exec, vcc\n\tglobal_load_dword %0, %1, %2\n\ts_mov_b64 exec, -1"
                 : "+v"(pf) : "v"(po), "s"(__builtin_amdgcn_s_getpc()), "s"(CODE_LEN) : "memory", "vcc");
  }
#define PFDONE() asm volatile("" :: "v"(pf))
  asm volatile("ds_write_b32 %0, %1" :: "v"((unsigned)(size_t)FL + lane_id() * 4), "v"(0u) : "memory");
  SB();
  TSDECL;
  const rsrc_t rw = __builtin_amdgcn_make_buffer_rsrc((void*)ws, 0, WS_END, 0x00020000);
  asm volatile("" :: "s"(x), "s"(wqkv), "s"(wo), "s"(u), "s"(lar), "s"(aim), "s"(cre), "s"(cim), "s"(ldt), "s"(dsk), "s"(out), "s"(ws));
  asm volatile("s_waitcnt lgkmcnt(0)" ::: "memory");
  unsigned tag;
#define TAGLD() asm volatile("s_load_dword %0, %1, 0x0" : "=s"(tag) : "s"(ws) : "memory")
#define TAGRD() do { asm volatile("s_waitcnt lgkmcnt(0)" : "+s"(tag) :: "memory"); tag = tag * 2654435761u + 0x9E3779B1u; } while (0)
  const int q4 = (lane & 15) >> 2, p4 = lane & 3, g4 = lane >> 4, c16 = lane & 15;

#define LROLE 2
  if ((wave >> 2) == LROLE) {
    __builtin_amdgcn_s_setprio(PRIO_L);
    const int hw = wave & 3;
    const int lane = lane_id(), q4 = (lane & 15) >> 2, p4 = lane & 3, g4 = lane >> 4, c16 = lane & 15;
    const int koff = KOFF;
    unsigned vo[10];
    {
      const unsigned vx0 = (unsigned)((mb * 128 + hw * 32 + (lane >> 3)) * 2048) + (((lane & 7) ^ (lane >> 4)) << 4);
      const unsigned vw0 = (unsigned)((lane >> 3) * 6144 + (((lane & 7) ^ (((lane >> 4) & 1) << 1)) << 4));
#pragma unroll
      for (int i = 0; i < 4; ++i) vo[i] = (vx0 ^ ((i & 1) * 64)) + i * 16384;
#pragma unroll
      for (int i = 0; i < 6; ++i) {
        const int qq = hw * 6 + i;
        vo[4 + i] = (vw0 ^ ((i & 1) * 64)) + ((qq & 7) * 49152 + (qq >> 3) * 2048 + (qq < 8 ? hq : hp) * 128);
      }
    }
    const unsigned lx = (unsigned)(size_t)sm + hw * 4096, lw = (unsigned)(size_t)sm + 16384 + hw * 6144;
#define DM(v_, s_) "s_nop 0\n\tglobal_load_lds_dwordx4 " v_ ", " s_ "\n\t"
#define M0A "s_add_u32 m0, m0, 0x400\n\t"
#define ISSUE_(xa_, wa_, lx_, lw_)                                                                                          \
    asm volatile("s_mov_b32 m0, %12\n\t" DM("%0", "%10") M0A DM("%1", "%10") M0A DM("%2", "%10") M0A DM("%3", "%10")          \
                 "s_mov_b32 m0, %13\n\t" DM("%4", "%11") M0A DM("%5", "%11") M0A DM("%6", "%11") M0A DM("%7", "%11") M0A      \
                 DM("%8", "%11") M0A DM("%9", "%11")                                                                        \
                 :: "v"(vo[0]), "v"(vo[1]), "v"(vo[2]), "v"(vo[3]), "v"(vo[4]), "v"(vo[5]), "v"(vo[6]), "v"(vo[7]), "v"(vo[8]), "v"(vo[9]), \
                    "s"(xa_), "s"(wa_), "s"(lx_), "s"(lw_) : "memory", "m0", "scc")
#define ISSUE(tt_, slot_) do {                                                                                              \
      const int k_ = ((tt_) + koff) & 15;                                                                                   \
      ISSUE_((const char*)x + k_ * 128, (const char*)wqkv + (size_t)k_ * 393216, lx + (slot_) * SLOT, lw + (slot_) * SLOT); } while (0)
    BAR();
    const int cg = bid >> 4, tt16 = bid & 15;
    const unsigned xo = (unsigned)((mb * 128 + hl * 32 + hw * 8 + (lane & 7)) * 2048);
    const unsigned l8 = idx * 32 + hw * 8 + (lane & 7);
    const unsigned wo8 = (l8 >> 2) * 2048u + (l8 & 3u) * 128u + (xcd >> 1) * 512;
#define TOUCH(xt_, nx_, wt_, nw_) do {                                                                                     \
      const unsigned ow_ = (((wt_) + koff + (lane >> 3) - (nx_)) & 15u) * 393216u + wo8;                                   \
      const unsigned ox_ = (((xt_) + koff + (lane >> 3)) & 15u) * 128u + xo;                                               \
      const char* p_ = (lane < 8 * (nx_) || idx >= 24) ? (const char*)x + ((lane < 8 * (nx_)) ? ox_ : xo + (koff & 15) * 128u) : (const char*)wqkv + ow_; \
      if (lane < 8 * (nx_) + 8 * (nw_)) __builtin_amdgcn_global_load_lds((const GAS unsigned*)p_, (LAS unsigned*)(sm + L_DUM), 4, 0, 0); } while (0)
#define TOUCHW(wt_, nw_) do {                                                                                              \
      const unsigned ow_ = (((wt_) + koff + (lane >> 3) - 2) & 15u) * 393216u + wo8;                                       \
      const char* p_ = (lane < 16 || idx >= 24) ? (const char*)wo + (unsigned)((tt16 * 64 + hw * 16 + c16) * 2048 + cg * 128) : (const char*)wqkv + ow_; \
      if (lane < 16 + 8 * (nw_)) __builtin_amdgcn_global_load_lds((const GAS unsigned*)p_, (LAS unsigned*)(sm + L_DUM), 4, 0, 0); } while (0)
#define ULD() do {                                                                                                         \
      LAS char* uc = (hw < 2) ? sm + 3 * SLOT + hw * 4096 : sm + L_X + (hw - 2) * 4096;                                    \
      const unsigned uo0 = (unsigned)((tt16 * 128 + hw * 32 + (lane >> 3)) * 2048 + cg * 128 + (((lane & 7) ^ (((lane >> 4) & 1) << 1)) << 4)), uo1 = (uo0 ^ 64) + 16384; \
      GLDSA((const char*)u + uo0, uc, UAUX); GLDSA((const char*)u + uo1, uc + 1024, UAUX);                                 \
      GLDSA((const char*)u + 32768 + uo0, uc + 2048, UAUX); GLDSA((const char*)u + 32768 + uo1, uc + 3072, UAUX); } while (0)
#define PRO ISSUE(0, 0); TS(13); TOUCH(1, 3, 1, 5); ISSUE(1, 1); TOUCHW(6, NWB); ULD()
#define WM1 16
#define POST ISSUE(2, 2)
#define W00 15
    PRO;
    TS(14);
    TAGLD();
    WAITVMX(WM1);
    TAGRD();
    TS(1);
    BAR();
    POST;
#undef ULD
#undef TOUCHW
#undef TOUCH
    {
      int k = (3 + koff) & 15;
      const char* xa = (const char*)x + k * 128;
      const char* wa = (const char*)wqkv + (size_t)k * 393216;
      unsigned l0 = lx, l1 = lw;
      asm volatile("" : "+s"(xa), "+s"(wa), "+s"(l0), "+s"(l1));
      WAITVMX(W00);
#pragma unroll 1
      for (int t = 0; t < 13; ++t) {
        BAR();
        ISSUE_(xa, wa, l0, l1);
        k = (k + 1) & 15;
        xa = (const char*)x + k * 128;
        wa = (const char*)wqkv + (size_t)k * 393216;
        const unsigned dl = (l0 == lx + 2 * SLOT) ? (unsigned)(-2 * SLOT) : (unsigned)SLOT;
        l0 += dl; l1 += dl;
        asm volatile("" : "+s"(xa), "+s"(wa), "+s"(l0), "+s"(l1));
        WAITVM(10);
      }
    }
#undef ISSUE
#undef ISSUE_
#undef M0A
#undef DM
    TS(2);
    {
      const char* src = ws + WS_WT + nb * 131072 + hw * 1024 + lane * 16;
#define WOP(s_) __builtin_amdgcn_global_load_lds((const GAS unsigned*)(src + (s_) * 4096), (LAS unsigned*)(sm + ((s_) * 4 + hw) * 1024), 16, 0, SC1)
      BAR();
#pragma unroll
      for (int i = 10; i < 20; ++i) WOP(i);
      WAITVM(10);
      BAR();
#pragma unroll
      for (int i = 20; i < 32; ++i) WOP(i);
      BAR();
#pragma unroll
      for (int i = 0; i < 10; ++i) WOP(i);
#undef WOP
      TS(3);
      WAITVM(0);
      sig(FL + F_LW);
    }
    TS(4);
    waitge(FL + F_XP, 4);
    {
      const int so = WS_KVP + (hp * 16 + mb) * 8192;
      const int vo = (lane >> 3) * 512 + (hw * 8 + (lane & 7)) * 16;
      u32x4 a = {0u, 0u, 0u, 0u}, b = a;
      bool na = true, nb_ = true;
#pragma unroll 1
      for (int it = 0; it < (1 << 17); ++it) {
        CBAR();
        if (na) a = ld16(rw, vo, so);
        if (nb_) b = ld16(rw, vo + 4096, so);
        na = tag_get(a) != tag; nb_ = tag_get(b) != tag;
        if (!__any(na || nb_)) break;
        __builtin_amdgcn_s_sleep(PSLEEP);
      }
      TS(5);
      const f32x4 fa = tag_val(a), fb = tag_val(b);
      float s[4];
#pragma unroll
      for (int i = 0; i < 4; ++i) { s[i] = fa[i] + fb[i]; asm("" : "+v"(s[i])); }
#pragma unroll
      for (int i = 0; i < 4; ++i) s[i] = sum32(sum16(sum8(s[i])));
      const unsigned p0 = pk_bf16(s[0], s[1]), p1 = pk_bf16(s[2], s[3]);
      const u32x4 w = {__builtin_amdgcn_perm(p0, tag, 0x05040100u), __builtin_amdgcn_perm(p0, tag, 0x07060302u),
                       __builtin_amdgcn_perm(p1, tag, 0x05040100u), __builtin_amdgcn_perm(p1, tag, 0x07060302u)};
      if (lane < 8) st16(w, rw, lane * 16, WS_KVR + hp * 8192 + mb * 512 + hw * 128);
      TS(6);
    }
    TS(9);
    if (hw == PW) {
      const char* fp = ws + WS_FP + ((mb2 * 16 + c16) << 7);
      unsigned v = (lane < 16) ? ~tag : tag, n = 0;
      CBAR();
#pragma unroll 1
      for (int it = 0; it < (1 << 18); ++it) {
        bool nw = false;
        if (v != tag) {
          v = __hip_atomic_load((unsigned*)fp, __ATOMIC_RELAXED, __HIP_MEMORY_SCOPE_AGENT);
          nw = (v == tag);
        }
        const unsigned m = (unsigned)__ballot(nw);
        if (nw) *(volatile LAS unsigned*)(FL + F_LIST + n + __builtin_amdgcn_mbcnt_lo(m, 0u)) = (unsigned)lane + 16u;
        n += __builtin_popcount(m);
        if (n >= 16u) break;
        __builtin_amdgcn_s_sleep(PSLEEP);
      }
      CBAR();
      TS(10);
    }
    TSDUMP();
    return;
  }

  const int c = wave & 3, d = bid * 4 + c;
  LAS char* ub = sm + L_UB + c * UB_CH;
  LAS char* kc = sm + 2 * SLOT + c * 4096;
  f32x4 ta = {0, 0, 0, 0}, tb = {0, 0, 0, 0}, tc_ = {0, 0, 0, 0};
  float v_dsk = bf2f(dsk[d]);
  {
    const int pi = d * 128 + (wave < 4 ? 64 : 0) + lane;
    const float lr = bf2f(lar[pi]), aiv = bf2f(aim[pi]), cr = bf2f(cre[pi]), ci = bf2f(cim[pi]);
    const float dt = rbf(expf(bf2f(ldt[d])));
    LAS char* ks = (wave < 4) ? kc : ub;
    TAGLD();
    BAR();
    const float mx = wave_max(__builtin_fmaxf(__builtin_fabsf(cr), __builtin_fabsf(ci)));
    const int ex = (mx > 0.f && mx < 3.0e38f) ? __builtin_amdgcn_frexp_expf(mx) : 0;
    const float mult = __builtin_amdgcn_ldexpf(2.f * dt, ex);
    asm volatile("" : "+v"(v_dsk));
    PFDONE();
    TAGRD();
    TS(15);
    const float ar = -rbf(expf(lr)) * dt;
    const float ai = aiv * dt;
    const float arl2 = ar * 1.4426950408889634f;
    const cplx z1 = zpow(arl2, ai, 1.f), z8 = zpow(arl2, ai, 8.f), z128 = zpow(arl2, ai, 128.f);
    LAS char* wp = ks + lane * 64 + (((lane >> 1) & 3) << 4);
    const int h0 = g4 & 1;
    LAS char* rp0 = ks + (g4 * 256 + h0 * 128 + (q4 >> 1) * 64 + (((2 * (q4 & 1) + (p4 >> 1)) ^ ((2 * g4 + h0) & 3)) << 4) + (p4 & 1) * 8);
    LAS char* rp1 = ks + ((unsigned)(rp0 - ks) ^ 144u);
#define KW(v, sg)                                                                                                    \
  _Pragma("unroll") for (int hh = 0; hh < 2; ++hh) {                                                                 \
    u32x4 wr_, wi_;                                                                                                  \
    _Pragma("unroll") for (int i = 0; i < 4; ++i) {                                                                  \
      wr_[i] = pk_f16(v[8 * hh + 2 * i][0], v[8 * hh + 2 * i + 1][0]);                                               \
      wi_[i] = pk_f16(sg v[8 * hh + 2 * i][1], sg v[8 * hh + 2 * i + 1][1]);                                         \
    }                                                                                                                \
    *(LAS u32x4*)(ks + ((unsigned)(wp - ks) ^ (unsigned)(hh << 4))) = wr_;                                           \
    *(LAS u32x4*)(ks + ((unsigned)(wp - ks) ^ (unsigned)((2 + hh) << 4))) = wi_;                                     \
  }
#define KR(ks_) __builtin_bit_cast(f16x8, tr_pair(rp0 + (ks_) * 1024, rp1 + (ks_) * 1024))
    f16x8 bfr[4];
    cplx v[16], X[17], zp[9];
    v[0] = (cplx){1.f, 0.f};
    v[1] = z8;
    v[2] = cmul(z8, z8);
#pragma unroll
    for (int k = 3; k < 16; ++k) v[k] = cmul(v[k - 2], v[2]);
    KW(v, -);
    X[0] = (cplx){__builtin_amdgcn_ldexpf(cr, -ex), __builtin_amdgcn_ldexpf(ci, -ex)};
    X[1] = cmul(X[0], z128);
    {
      const cplx z256 = cmul(z128, z128);
#pragma unroll
      for (int r = 2; r < 17; ++r) X[r] = cmul(X[r - 2], z256);
    }
    zp[1] = z1; zp[8] = z8;
    zp[2] = cmul(z1, z1); zp[3] = cmul(zp[2], z1); zp[4] = cmul(zp[2], zp[2]);
    zp[5] = cmul(zp[4], z1); zp[6] = cmul(zp[4], zp[2]); zp[7] = cmul(zp[4], zp[3]);
#pragma unroll
    for (int k = 0; k < 4; ++k) bfr[k] = KR(k);
#pragma unroll
    for (int g = 0; g < 4; ++g) { v[4 * g] = X[4 + g]; v[4 * g + 1] = cmul(X[4 + g], zp[4]); v[4 * g + 2] = X[8 + g]; v[4 * g + 3] = X[12 + g]; }
    KW(v, );
#pragma unroll
    for (int j = 0; j < 16; ++j) v[j] = (j & 7) ? cmul(X[j >> 3], zp[j & 7]) : X[j >> 3];
    {
      f16x8 a[4];
#pragma unroll
      for (int k = 0; k < 4; ++k) a[k] = KR(k);
      KW(v, );
#pragma unroll
      for (int k = 0; k < 4; ++k) ta = MFMA16H(a[k], bfr[k], ta);
    }
#pragma unroll
    for (int a = 0; a < 2; ++a) {
      v[8 * a] = X[2 + a];
      v[8 * a + 1] = cmul(X[2 + a], zp[2]);
      v[8 * a + 2] = v[8 * a + 4] = cmul(X[2 + a], zp[4]);
      v[8 * a + 5] = cmul(X[2 + a], zp[6]);
      v[8 * a + 6] = cmul(X[2 + a], zp[8]);
      v[8 * a + 3] = v[8 * a + 7] = X[16];
    }
    {
      f16x8 a[4];
#pragma unroll
      for (int k = 0; k < 4; ++k) a[k] = KR(k);
      KW(v, );
#pragma unroll
      for (int k = 0; k < 4; ++k) tc_ = MFMA16H(a[k], bfr[k], tc_);
    }
#pragma unroll
    for (int k = 0; k < 4; ++k) { f16x8 a = KR(k); tb = MFMA16H(a, bfr[k], tb); }
#undef KW
#undef KR
    ta *= mult; tb *= mult; tc_ *= mult;
    TS(2);
  }

  if (wave < 4) {
    *(LAS f32x4*)(kc + lane * 16) = ta;
    *(LAS f32x4*)(kc + 1024 + lane * 16) = tb;
    *(LAS f32x4*)(kc + 2048 + lane * 16) = tc_;
    sig(FL + F_KC + c);
    __builtin_amdgcn_s_setprio(PRIO_C);
    const int lane = lane_id(), q4 = (lane & 15) >> 2, p4 = lane & 3, g4 = lane >> 4, c16 = lane & 15;
    const int mh = wave & 1, hd = wave >> 1;
    const int a0 = (mh * 64 + c16) * 128 + ((g4 ^ (c16 >> 1)) << 4);
    const int b0 = 16384 + (8 * g4 + q4) * 128 + (((hd * 4 + (p4 >> 1)) ^ (((q4 >> 1) | ((g4 & 1) << 1)) << 1)) << 4) + (p4 & 1) * 8;
    f32x4 acc[4][6];
#pragma unroll
    for (int m = 0; m < 4; ++m)
#pragma unroll
      for (int g = 0; g < 6; ++g) acc[m][g] = (f32x4){0.f, 0.f, 0.f, 0.f};
#pragma unroll
    for (int m = 0; m < 4; ++m)
      asm volatile("" : "+v"(acc[m][0]), "+v"(acc[m][1]), "+v"(acc[m][2]), "+v"(acc[m][3]), "+v"(acc[m][4]), "+v"(acc[m][5]));
    bf16x8 fa[2][4];
    u32x2 bl[6], bh[6];
#define RA(set, m, pa) fa[set][m] = *(LAS bf16x8*)((pa) + (m) * 2048)
#define RL(j, pe, po) bl[(j) % 6] = tr_read((((j) & 1) ? (po) : (pe)) + (((j) % 6) >> 1) * 8192 + ((j) / 6) * 4096)
#define RH(j, pe, po) bh[(j) % 6] = tr_read((((j) & 1) ? (po) : (pe)) + (((j) % 6) >> 1) * 8192 + ((j) / 6) * 4096 + 512)
#define FB(j) asbf(join(bl[(j) % 6], bh[(j) % 6]))
#define MM(j, m) acc[m][(j) % 6] = ((j) % 6 < 2) ? MFMA16(FB(j), fa[(j) / 6][m], acc[m][(j) % 6]) : MFMA16(fa[(j) / 6][m], FB(j), acc[m][(j) % 6])
#define GRP(j, X0, X1, X2, X3)                \
  do {                                        \
    MM(j, 0); X0; SB(); MM(j, 1); X1; SB();   \
    MM(j, 2); X2; SB(); MM(j, 3); X3; SB();   \
  } while (0)
    LAS char* pa = sm + a0;
    LAS char* pa1 = sm + (a0 ^ 64);
    LAS char* pe = sm + b0;
    LAS char* po = sm + (b0 ^ 32);
    LBAR();
    TS(3); TC(0);
    RA(0, 0, pa); RL(0, pe, po); RH(0, pe, po); RA(0, 1, pa); RA(0, 2, pa); RA(0, 3, pa);
    RL(1, pe, po); RH(1, pe, po); RL(2, pe, po); RH(2, pe, po);
    {
      int slot = 0;
#pragma unroll 1
      for (int t = 0; t < 16; ++t) {
        const int dl = (slot == 2) ? -2 * SLOT : SLOT;
        slot = (slot == 2) ? 0 : slot + 1;
        SB();
        GRP(0, RL(3, pe, po), RH(3, pe, po), , );
        GRP(1, RL(4, pe, po), RH(4, pe, po), , );
        GRP(2, RL(5, pe, po), RH(5, pe, po), RA(1, 0, pa1), RA(1, 1, pa1));
        GRP(3, RL(6, pe, po), RH(6, pe, po), RA(1, 2, pa1), RA(1, 3, pa1));
        GRP(4, RL(7, pe, po), RH(7, pe, po), RL(8, pe, po), RH(8, pe, po));
        GRP(5, RL(9, pe, po), RH(9, pe, po), RL(10, pe, po), RH(10, pe, po));
        GRP(6, RL(11, pe, po), RH(11, pe, po), , );
        GRP(7, , , , );
        GRP(8, pa += dl, pa1 += dl, pe += dl, po += dl);
        asm volatile("" : "+v"(pa), "+v"(pa1), "+v"(pe), "+v"(po));
        LBAR();
        SB();
        GRP(9, RA(0, 0, pa), RL(0, pe, po), RH(0, pe, po), RA(0, 1, pa));
        GRP(10, RA(0, 2, pa), RA(0, 3, pa), RL(1, pe, po), RH(1, pe, po));
        GRP(11, RL(2, pe, po), RH(2, pe, po), , );
      }
    }
#undef RA
#undef RL
#undef RH
#undef FB
#undef MM
#undef GRP
    TS(4); TC(1);
    {
    const int lane = lane_id(), g4 = lane >> 4, c16 = lane & 15;
    const int head = hp * 2 + hd, headq = hq * 2 + hd;
    {
      f32x4 kvt[2][2], zt[2];
#pragma unroll
      for (int i = 0; i < 2; ++i) { kvt[i][0] = kvt[i][1] = zt[i] = (f32x4){0.f, 0.f, 0.f, 0.f}; }
      const u32x4 ones = {0x3F803F80u, 0x3F803F80u, 0x3F803F80u, 0x3F803F80u};
#pragma unroll
      for (int j = 0; j < 2; ++j) {
        u32x4 fk[2], fv[2];
#pragma unroll
        for (int n = 0; n < 2; ++n)
#pragma unroll
          for (int i = 0; i < 4; ++i) {
            const f32x4 kk = acc[2 * j + (i >> 1)][2 + n], vv = acc[2 * j + (i >> 1)][4 + n];
            fk[n][i] = pk_bf16(phi_f<false>(kk[2 * (i & 1)]), phi_f<false>(kk[2 * (i & 1) + 1]));
            fv[n][i] = pk_bf16(vv[2 * (i & 1)], vv[2 * (i & 1) + 1]);
          }
#pragma unroll
        for (int td = 0; td < 2; ++td) {
          kvt[0][td] = MFMA16(asbf(fk[td]), asbf(fv[0]), kvt[0][td]);
          kvt[1][td] = MFMA16(asbf(fk[td]), asbf(fv[1]), kvt[1][td]);
          zt[td] = MFMA16(asbf(ones), asbf(fk[td]), zt[td]);
        }
      }
      LAS char* xo = sm + L_X + wave * 2176;
      LAS char* xi = sm + L_X + (wave ^ 1) * 2176;
      f32x4 keep[2];
#pragma unroll
      for (int td = 0; td < 2; ++td) {
        f32x4 ka = kvt[0][td], kb = kvt[1][td];
        asm("" : "+v"(ka), "+v"(kb));
        *(LAS f32x4*)(xo + td * 1024 + lane * 16) = mh ? ka : kb;
        keep[td] = mh ? kb : ka;
      }
      *(LAS float*)(xo + 2048 + (lane & 31) * 4) = (g4 & 1) ? zt[1][0] : zt[0][0];
      sig(FL + F_XE + hd);
      waitge(FL + F_XE + hd, 2);
      if (mh == 0) {
        f32x4 zz = *(LAS f32x4*)(xo + 2048 + (lane & 7) * 16) + *(LAS f32x4*)(xi + 2048 + (lane & 7) * 16);
        if (lane < 8) st16(tag_put(zz, tag), rw, lane * 16, WS_ZP + (head * 16 + mb) * 128);
      }
      const int so = WS_KVP + (hp * 16 + hd * 8 + mh * 4) * 8192 + mb * 512;
#pragma unroll
      for (int td = 0; td < 2; ++td) {
        f32x4 o = *(LAS f32x4*)(xi + td * 1024 + lane * 16) + keep[td];
        st16(tag_put(o, tag), rw, td * 16384 + (lane >> 5) * 8192 + (lane & 31) * 16, so);
      }
      sig(FL + F_XP);
    }
    TS(5);
#pragma unroll
    for (int m = 0; m < 4; ++m)
#pragma unroll
      for (int i = 0; i < 8; ++i) acc[m][i >> 2][i & 3] = phi_f<true>(acc[m][i >> 2][i & 3]);
    asm volatile("" : "+v"(acc[0][0]), "+v"(acc[1][0]), "+v"(acc[2][0]), "+v"(acc[3][0]),
                      "+v"(acc[0][1]), "+v"(acc[1][1]), "+v"(acc[2][1]), "+v"(acc[3][1]));
    u32x4 fq[4];
    {
      LAS f32x4* zl = (LAS f32x4*)(sm + L_Z + hd * 128);
      if (mh == 1) {
        u32x4 za = {0u, 0u, 0u, 0u}, zb = za;
        bool na = true, nb_ = true;
#pragma unroll 1
        for (int it = 0; it < (1 << 17); ++it) {
          CBAR();
          if (na) za = ld16(rw, lane * 16, WS_ZP + headq * 2048);
          if (nb_) zb = ld16(rw, 1024 + lane * 16, WS_ZP + headq * 2048);
          na = tag_get(za) != tag; nb_ = tag_get(zb) != tag;
          if (!__any(na || nb_)) break;
          __builtin_amdgcn_s_sleep(PSLEEP);
        }
        const f32x4 fa_ = tag_val(za), fb_ = tag_val(zb);
        f32x4 z;
#pragma unroll
        for (int i = 0; i < 4; ++i) { float s = fa_[i] + fb_[i]; asm("" : "+v"(s)); z[i] = sum32(sum16(sum8(s))); }
        if (lane < 8) zl[lane] = z;
        sig(FL + F_Z + hd);
      } else {
        waitge(FL + F_Z + hd, 1);
      }
      TS(6);
      const f32x4 z0 = zl[g4], z1 = zl[4 + g4];
#pragma unroll
      for (int m = 0; m < 4; ++m) {
        float d0 = acc[m][0][0] * z0[0], d1 = acc[m][1][0] * z1[0];
#pragma unroll
        for (int i = 1; i < 4; ++i) { d0 = __builtin_fmaf(acc[m][0][i], z0[i], d0); d1 = __builtin_fmaf(acc[m][1][i], z1[i], d1); }
        const float den = sum32(sum16(d0 + d1));
        const float rc = __builtin_amdgcn_rcpf(den + 1e-6f);
        float e[8];
#pragma unroll
        for (int i = 0; i < 8; ++i) { e[i] = acc[m][i >> 2][i & 3] * rc; asm("" : "+v"(e[i])); }
        fq[m] = (u32x4){pk_bf16(e[0], e[1]), pk_bf16(e[2], e[3]), pk_bf16(e[4], e[5]), pk_bf16(e[6], e[7])};
      }
    }
    asm volatile("" : "+v"(fq[0]), "+v"(fq[1]), "+v"(fq[2]), "+v"(fq[3]));
    TS(13);
    {
      u32x4 k[4];
      bool nk[4];
#pragma unroll
      for (int i = 0; i < 4; ++i) { k[i] = (u32x4){0u, 0u, 0u, 0u}; nk[i] = true; }
      const unsigned t0 = tag & 0xffffu, t1 = tag >> 16;
#pragma unroll 1
      for (int it = 0; it < (1 << 17); ++it) {
        CBAR();
#pragma unroll
        for (int i = 0; i < 4; ++i) if (nk[i]) k[i] = ld16(rw, i * 1024 + lane * 16, WS_KVR + headq * 4096);
#pragma unroll
        for (int i = 0; i < 4; ++i) nk[i] = (((k[i][0] ^ t0) | (k[i][1] ^ t1) | (k[i][2] ^ t0) | (k[i][3] ^ t1)) & 0xffffu) != 0u;
        if (!__any(nk[0] || nk[1] || nk[2] || nk[3])) break;
        __builtin_amdgcn_s_sleep(PSLEEP);
      }
      TS(14);
      u32x4 ka[2];
#pragma unroll
      for (int te = 0; te < 2; ++te)
#pragma unroll
        for (int i = 0; i < 4; ++i) ka[te][i] = __builtin_amdgcn_perm(k[2 * te + (i >> 1)][2 * (i & 1) + 1], k[2 * te + (i >> 1)][2 * (i & 1)], 0x07060302u);
#pragma unroll
      for (int m = 0; m < 4; ++m) {
        const f32x4 z4 = {0.f, 0.f, 0.f, 0.f};
        const f32x4 d0 = MFMA16(asbf(ka[0]), asbf(fq[m]), z4), d1 = MFMA16(asbf(ka[1]), asbf(fq[m]), z4);
        const u32x4 ov = {pk_bf16(d0[0], d0[1]), pk_bf16(d0[2], d0[3]), pk_bf16(d1[0], d1[1]), pk_bf16(d1[2], d1[3])};
        st16(ov, rw, (m & 1) * 1024 + lane * 16, WS_O + ((mb * 4 + mh * 2 + (m >> 1)) * 32 + headq) * 2048);
      }
    }
    TS(7);
    {
      volatile LAS unsigned* lp = FL + F_LIST + c16;
      unsigned lst = *lp;
      const bool early = EARLY && __builtin_amdgcn_readlane(lst, NP - 1) != 0u;
      if (!early) {
        WAITVM(0);
        if (sig_ret(FL + F_OD) == 3u) gflag(ws, WS_FP + (mb * 16 + hq) * 128, tag, lane);
      }
      TS(8);
      const int pqs = WS_O + (mb2 * 4 + wave) * 65536 - 65536;
      u32x4 pb[2 * NP][2];
      unsigned e[16];
#define ENT(k_) do { e[k_] = __builtin_amdgcn_readlane(lst, k_);                                        \
        if (e[k_] == 0u) {                                                                              \
          int it_ = 0;                                                                                  \
          do { __builtin_amdgcn_s_sleep(1); lst = *lp; e[k_] = __builtin_amdgcn_readlane(lst, k_); } while (e[k_] == 0u && ++it_ < (1 << 22)); \
          e[k_] |= 16u;                                                                                 \
        } } while (0)
#define LDH(i_, h_) pb[(i_) % (2 * NP)][h_] = __builtin_amdgcn_raw_buffer_load_b128(rw, ((i_) & 1) * 2048 + (h_) * 1024 + lane * 16, pqs + (e[(i_) >> 1] << 12), QAUX)
#pragma unroll
      for (int k = 0; k < NP; ++k) { ENT(k); LDH(2 * k, 0); LDH(2 * k, 1); LDH(2 * k + 1, 0); LDH(2 * k + 1, 1); }
      if (early) {
        WAITVMX(NP4);
        if (sig_ret(FL + F_OD) == 3u) gflag(ws, WS_FP + (mb * 16 + hq) * 128, tag, lane);
      }
      TS(9);
      f32x4 oc[2][4];
#pragma unroll
      for (int s = 0; s < 2; ++s)
#pragma unroll
        for (int t = 0; t < 4; ++t) oc[s][t] = (f32x4){0.f, 0.f, 0.f, 0.f};
      asm volatile("" : "+v"(oc[0][0]), "+v"(oc[0][1]), "+v"(oc[0][2]), "+v"(oc[0][3]),
                   "+v"(oc[1][0]), "+v"(oc[1][1]), "+v"(oc[1][2]), "+v"(oc[1][3]));
      LAS char* mp0 = sm + lane * 16 - 131072;
      bf16x8 ma[4];
      waitge(FL + F_LW, 4);
      TS(10); TC(2);
#pragma unroll
      for (int t = 0; t < 4; ++t) ma[t] = *(LAS bf16x8*)(mp0 + (e[0] << 13) + t * 1024);
#pragma unroll
      for (int i = 0; i < 32; ++i) {
        const int j = i - 1 + 2 * NP;
        if (i > 0 && j < 32 && (j & 1) == 0) ENT(j >> 1);
        SB();
        const u32x4 b0_ = pb[i % (2 * NP)][0], b1_ = pb[i % (2 * NP)][1];
        LAS char* mp = mp0 + (e[((i + 1) >> 1) & 15] << 13) + ((i + 1) & 1) * 4096;
#pragma unroll
        for (int t = 0; t < 4; ++t) {
          AMFMA16(oc[0][t], ma[t], b0_);
          if (i > 0 && j < 32 && (t & 1) == 0) LDH(j, t >> 1);
          SB();
          AMFMA16(oc[1][t], ma[t], b1_);
          if (i < 31) ma[t] = *(LAS bf16x8*)(mp + t * 1024);
          if (LRF && t == 0 && (j & 1) && j < 31) lst = *lp;
          SB();
        }
      }
#undef LDH
#undef ENT
      if (bid == 0 && wave == 0) gflag(ws, WS_EPOCH, tag, lane);
      asm volatile("s_nop 7\n\ts_nop 3" : "+v"(oc[0][0]), "+v"(oc[0][1]), "+v"(oc[0][2]), "+v"(oc[0][3]),
                   "+v"(oc[1][0]), "+v"(oc[1][1]), "+v"(oc[1][2]), "+v"(oc[1][3]));
      TS(11); TC(3);
#pragma unroll
      for (int s = 0; s < 2; ++s)
#pragma unroll
        for (int j = 0; j < 2; ++j) {
          u32x4 w = {pk_bf16(oc[s][2 * j][0], oc[s][2 * j][1]), pk_bf16(oc[s][2 * j][2], oc[s][2 * j][3]),
                     pk_bf16(oc[s][2 * j + 1][0], oc[s][2 * j + 1][1]), pk_bf16(oc[s][2 * j + 1][2], oc[s][2 * j + 1][3])};
          ost16(out + (size_t)((mb2 * 4 + wave) * 32 + s * 16 + c16) * 2048 + nb * 64 + 32 * j + 8 * g4, w);
        }
    }
    TS(12);
    TSDUMP();
    }
    return;
  }

  const int cg = bid >> 4, tt16 = bid & 15;
  const int rg = lane >> 4;
  bf16x8 F[16];
  {
    waitge(FL + F_KC + c, 1);
    {
      const f32x4 oa = *(LAS f32x4*)(kc + lane * 16), ob = *(LAS f32x4*)(kc + 1024 + lane * 16), oc = *(LAS f32x4*)(kc + 2048 + lane * 16);
      LBAR();
      TS(1);
#pragma unroll
      for (int i = 0; i < 4; ++i) {
        float x0 = ta[i] + oa[i], x1 = tb[i] + ob[i], x2 = tc_[i] + oc[i];
        asm volatile("" : "+v"(x0)); asm volatile("" : "+v"(x1)); asm volatile("" : "+v"(x2));
        ta[i] = x0; tb[i] = x1; tc_[i] = x2;
      }
    }
#define ROL1(v) (unsigned)__builtin_amdgcn_update_dpp(0, (int)(v), 0x134, 0xf, 0xf, false)
#define MIX(lo, hi) __builtin_amdgcn_perm((hi), (lo), 0x07060100u)
    const bool l0 = (lane == 0);
    const u32x2 w0 = {pk_bf16(tc_[0] + (l0 ? v_dsk : 0.f), tc_[1]), pk_bf16(tc_[2], tc_[3])};
    const u32x2 w1 = {pk_bf16(tb[0], tb[0]), pk_bf16(tb[1], tb[2])};
    const unsigned pa0 = pk_bf16(ta[0], ta[0]), pa1 = pk_bf16(ta[1], ta[1]), pa2 = pk_bf16(ta[2], ta[2]), pa3 = pk_bf16(ta[3], ta[3]);
    const unsigned pe = pk_bf16(tb[3], tb[3]);
    const unsigned n0 = ROL1(l0 ? pa2 : pa0), n2 = ROL1(l0 ? pa3 : pa2), n3 = ROL1(l0 ? pe : pa3);
    const u32x4 w2 = {pa0, MIX(pa0, pa1), pa1, n0};
    const u32x4 w3 = {pa2, pa2, (lane & 1) ? n2 : MIX(pa2, n2), n2};
    const u32x4 w4 = {pa3, pa3, (lane & 1) ? n3 : MIX(pa3, n3), n3};
#undef ROL1
#undef MIX
    LAS char* K = ub + 32;
    const int k01 = 2 * (128 * (rg >> 1) + 8 * c16 + 4 * (rg & 1));
    LAS char* uc = (c < 2) ? sm + 3 * SLOT + c * 4096 : sm + L_X + (c - 2) * 4096;
    const int uo = c16 * 4096 + 16 * g4, us = WS_UT + cg * 64 * 4096 + tt16 * 256 + c * 64;
#define USTEP 65536
#define UST(v, o) st16(v, rw, uo, us + (o) * USTEP)
#define WST(v, o) st16(v, rw, lane * 16, ws_ + (o))
    u32x4 ut[4];
    st16((u32x4){~tag, ~tag, ~tag, ~tag}, rw, (lane >> 5) * 8192 + (lane & 31) * 16, WS_KVP + (hp * 16 + c * 4) * 8192 + mb * 512);
    st16((u32x4){~tag, ~tag, ~tag, ~tag}, rw, (lane >> 5) * 8192 + (lane & 31) * 16, WS_KVP + (hp * 16 + c * 4) * 8192 + mb * 512 + 16384);
    BAR();
    TS(4);
    *(LAS u32x2*)(K + k01) = w0;
    *(LAS u32x2*)(K + 512 + k01) = w1;
    {
      const int a0_ = WS_KVR + hp * 8192 + mb * 512 + c * 128, a1_ = WS_ZP + ((hp * 2 + (c & 1)) * 16 + mb) * 128;
      const int a2_ = WS_FP + (mb * 16 + hq) * 128, a3_ = WS_FY + bid * 128;
      const int g8 = (lane >> 3) & 3;
      int av = (g8 & 1) ? a1_ : a0_, aw = (g8 & 1) ? a3_ : a2_;
      asm("" : "+v"(av), "+v"(aw));
      st16((u32x4){~tag, ~tag, ~tag, ~tag}, rw, ((g8 & 2) ? aw : av) + (lane & 7) * 16, 0);
    }
    BAR();
    TS(5);
    *(LAS u32x4*)(K + 1024 + lane * 16) = w2;
    {
      const int f1 = (q4 >> 1) | ((g4 & 1) << 1);
      const unsigned up = (8 * g4 + q4) * 128 + (((p4 >> 1) ^ (f1 << 1)) << 4) + (p4 & 1) * 8;
#pragma unroll
      for (int j = 0; j < 4; ++j) ut[j] = tr_pair(uc + (up ^ (32u * j)), uc + (up ^ (32u * j)) + 512);
    }
    UST(ut[0], 0);
    LBAR();
    TS(6);
    *(LAS u32x4*)(K + 2048 + lane * 16) = w3;
    {
      const unsigned wv = (unsigned)(((tt16 * 2 + (c >> 1)) * 32 + (lane >> 2)) * 2048 + cg * 128 + (c & 1) * 64 + p4 * 16);
      DMA16(wv, (const char*)wo, (unsigned)(size_t)uc, "");
      DMA16(wv, (const char*)wo + 32768, (unsigned)(size_t)uc + 1024, "");
    }
    UST(ut[1], 1);
    BAR();
    TS(7);
    *(LAS u32x4*)(K + 3072 + lane * 16) = w4;
    UST(ut[2], 2);
    BAR();
    TS(13);
    if (lane < 8) *(LAS unsigned*)(ub + lane * 4) = 0u;
    UST(ut[3], 3);
    WAITVM(3);
    {
      LAS char* wp = uc + (4 * g4 + q4) * 64 + p4 * 16;
      ut[0] = tr_pair(wp, wp + 1024);
      ut[1] = tr_pair(wp + 8, wp + 1024 + 8);
    }
    const int ws_ = WS_WT + ((cg * 32 + tt16 * 2 + (c >> 1)) * 4 + (c & 1) * 2) * 1024;
    LAS char* fbp = ub + 2 * (16 - 12 + 32 * rg + 4 * (q4 + p4));
#pragma unroll
    for (int s = 0; s < 16; ++s) {
      if ((s & 3) == 0) {
        if (s == 12) WAITVM(0);
        BAR(); TS(s == 0 ? 14 : 15 + (s >> 2));
        if (s == 0) WST(ut[0], 0);
        if (s == 4) WST(ut[1], 1024);
        if (s == 12 && c == 0) st16((u32x4){tag, tag, tag, tag}, rw, (lane & 7) * 16, WS_FU + bid * 128);
      }
      F[s] = tr_frag(fbp + 256 * s, fbp + 256 * s + 32);
    }
    LBAR();
    TS(3);
    *(LAS unsigned*)(ub + lane * 4) = 0u;
    BAR();
    TS(19);
    char* fup = ws + WS_FU + ((((g4 & 1) ? nb : cg) * 16 + c16) << 7);
    BAR();
    TS(20);
    unsigned fu;
    asm volatile("global_load_dword %0, %1, off sc1" : "=v"(fu) : "v"(fup) : "memory");
    BAR();
    TS(21);
    asm volatile("s_waitcnt vmcnt(0)" : "+v"(fu) :: "memory");
    if (!__all(fu == tag)) gpoll(fup, tag);
#pragma unroll
    for (int i = 0; i < 4; ++i) DMA16(lane * 16, ws + WS_UT + d * 4096 + i * 1024, (unsigned)(size_t)ub + 256 + i * 1024, " sc1");
    BAR();
    TS(22);
    BAR();
    BAR();
    WAITVM(0);
    waitge(FL + F_XP, 4);
  }
  TS(8); TC(2);
  unsigned yp[16];
  {
    LAS char* gb = ub + 2 * (128 + 16 * p4 + 12 - 32 * rg - 4 * q4);
    float pc1 = 0.f, pc2 = 0.f, pc3 = 0.f, yprev = 0.f;
    const bool l0 = (lane == 0);
#define ROR1(v) __int_as_float(__builtin_amdgcn_update_dpp(0, __float_as_int(v), 0x13C, 0xf, 0xf, false))
#define FIN(i)                                                                       \
  do {                                                                               \
    float r3 = ROR1(ca[i][3]);                                                       \
    float s2 = ca[i][2] + (l0 ? pc3 : r3);                                           \
    float r2 = ROR1(s2);                                                             \
    float s1 = ca[i][1] + (l0 ? pc2 : r2);                                           \
    float r1 = ROR1(s1);                                                             \
    float y = ca[i][0] + (l0 ? pc1 : r1);                                            \
    pc1 = r1; pc2 = r2; pc3 = r3;                                                    \
    if ((i) & 1) yp[8 * pass + ((i) >> 1)] = pk_bf16(yprev, y); else yprev = y;      \
  } while (0)
#pragma unroll
    for (int pass = 0; pass < 2; ++pass) {
      f32x4 ca[16];
#pragma unroll
      for (int i = 0; i < 16; ++i) ca[i] = (f32x4){0, 0, 0, 0};
      bf16x8 G = tr_frag(gb, gb - 32);
#pragma unroll
      for (int v = 0; v < 16 * pass + 16; ++v) {
        bf16x8 Gn = G;
        if (v + 1 < 16 * pass + 16) Gn = tr_frag(gb + 128 * (v + 1), gb + 128 * (v + 1) - 32);
        SB();
#pragma unroll
        for (int i = 0; i < 16; ++i) {
          const int T = 16 * pass + i;
          if (T >= v && ((T - v) & 1) == 0) ca[i] = MFMA16(G, F[(T - v) >> 1], ca[i]);
        }
        if (v - 1 >= 16 * pass) FIN(v - 1 - 16 * pass);
        SB();
        G = Gn;
      }
      FIN(15);
      CBAR();
      SB();
    }
#undef FIN
#undef ROR1
  }
  CBAR();
  TS(9); TC(3);
  {
  const int lane = lane_id();
#pragma unroll
  for (int i = 0; i < 16; ++i) {
    ((LAS u16*)ub)[128 * i + lane] = (u16)yp[i];
    ((LAS u16*)ub)[128 * i + 64 + lane] = (u16)(yp[i] >> 16);
  }
  CBAR();
  {
    u32x4 yv[4];
#pragma unroll
    for (int i = 0; i < 4; ++i) yv[i] = *(LAS u32x4*)(ub + i * 1024 + lane * 16);
#pragma unroll
    for (int i = 0; i < 4; ++i) st16(yv[i], rw, i * 1024 + lane * 16, WS_YT + d * 4096);
  }
  WAITVM(0);
  if (sig_ret(FL + F_CY) == 3u) {
    gflag(ws, WS_FY + (cg * 16 + tt16) * 128, tag, lane);
    TS(10);
    gpoll(ws + WS_FY + ((cg * 16 + (lane & 15)) << 7), tag);
    sig(FL + F_Y);
  } else {
    TS(10);
    waitge(FL + F_Y, 1);
  }
  TS(11);
  {
    const int q4 = (lane & 15) >> 2, p4 = lane & 3, g4 = lane >> 4;
    LAS char* tb = sm + L_UB;
    u32x4 yr[4];
#pragma unroll
    for (int i = 0; i < 4; ++i)
      yr[i] = ld16(rw, (16 * c + 4 * i + g4) * 4096 + (lane & 15) * 16, WS_YT + cg * 64 * 4096 + tt16 * 256);
#pragma unroll
    for (int i = 0; i < 4; ++i) {
      const int row = 16 * c + 4 * i + g4;
      const int f = (row & 3) | (((row >> 3) & 1) << 2);
      *(LAS u32x4*)(tb + row * 256 + (((lane & 15) ^ (f << 1)) << 4)) = yr[i];
    }
    sig(FL + F_T2);
    waitge(FL + F_T2, 4);
    const int f = q4 | ((g4 & 1) << 2);
#pragma unroll
    for (int j = 0; j < 4; ++j) {
      const int tbk = 2 * c + (j >> 1), hf = j & 1;
      LAS char* pp = tb + (32 * hf + 8 * g4 + q4) * 256 + (((2 * tbk + (p4 >> 1)) ^ (f << 1)) << 4) + (p4 & 1) * 8;
      u32x4 v = tr_pair(pp, pp + 1024);
      ost16(out + (size_t)(tt16 * 128 + 16 * tbk + (lane & 15)) * 2048 + 1024 + cg * 64 + 32 * hf + 8 * g4, v);
    }
  }
  TS(12);
  TSDUMP();
  }
}

extern "C" void kernel_launch(void* const* d_in, const int* in_sizes, int n_in, void* d_out, int out_size,
                              void* d_ws, size_t ws_size, hipStream_t stream) {
  fused_kernel<<<256, 768, 0, stream>>>((const u16*)d_in[0], (const u16*)d_in[1], (const u16*)d_in[2], (const u16*)d_in[3],
                                        (const u16*)d_in[4], (const u16*)d_in[5], (const u16*)d_in[6], (const u16*)d_in[7],
                                        (const u16*)d_in[8], (const u16*)d_in[9], (u16*)d_out, (char*)d_ws);
}
```

```cpp
#include <hip/hip_runtime.h>
#include <stdint.h>
#pragma clang diagnostic ignored "-Winline-asm"

#define LAS __attribute__((address_space(3)))
#define GAS __attribute__((address_space(1)))
#define DI __device__ __forceinline__

typedef unsigned short u16;
typedef short s16x4 __attribute__((ext_vector_type(4)));
typedef __bf16 bf16x2 __attribute__((ext_vector_type(2)));
typedef __bf16 bf16x8 __attribute__((ext_vector_type(8)));
typedef _Float16 f16x2 __attribute__((ext_vector_type(2)));
typedef _Float16 f16x8 __attribute__((ext_vector_type(8)));
typedef float f32x2 __attribute__((ext_vector_type(2)));
typedef float f32x4 __attribute__((ext_vector_type(4)));
typedef float f32x16 __attribute__((ext_vector_type(16)));
typedef unsigned u32x2 __attribute__((ext_vector_type(2)));
typedef unsigned u32x4 __attribute__((ext_vector_type(4)));
typedef __amdgpu_buffer_rsrc_t rsrc_t;

#define CBAR() asm volatile("" ::: "memory")
#define SB() __builtin_amdgcn_sched_barrier(0)
#define WAITVM(n) asm volatile("s_waitcnt vmcnt(" #n ")" ::: "memory")
#define WAITVMX(n) WAITVM(n)
#define WAITVL(n) asm volatile("s_waitcnt vmcnt(" #n ") lgkmcnt(0)" ::: "memory")
#define BAR() asm volatile("s_barrier" ::: "memory")
#define LBAR() do { CBAR(); __builtin_amdgcn_s_waitcnt(0xC07F); asm volatile("s_barrier" ::: "memory"); } while (0)
#define TSDECL
#define TS(i)
#define TC(i)
#define TSDUMP()
#define WN13 4
#define WN14 4
#define WN15 0
#define WNX 12
#define WPACE 0
#define WGATE 0
#define UTB 14
#define XB 1
#define PKP 1
#define QD 1
#define KOFF (4 * (xcd >> 1) + 8 * (xcd & 1))
#define PRIO_C 2
#define PRIO_L 3
#define YD 1
#define YG 8
#define YS 0
#define FLC 1
#define FLS 1
#define OT 0
#define OTW 0
#define YGN 4
#define PSLEEP 3
#define CODE_LEN 28224u
#define NRING 10
#define NWB 2

DI void ost16(void* p, u32x4 v) { asm volatile("global_store_dwordx4 %0, %1, off sc1\n\ts_nop 1" :: "v"(p), "v"(v) : "memory"); }
DI float sum16(float x) {
  auto r = __builtin_amdgcn_permlane16_swap(__float_as_uint(x), __float_as_uint(x), false, false);
  float s = __uint_as_float(r[0]) + __uint_as_float(r[1]);
  asm("" : "+v"(s));
  return s;
}
DI float sum32(float x) {
  auto r = __builtin_amdgcn_permlane32_swap(__float_as_uint(x), __float_as_uint(x), false, false);
  float s = __uint_as_float(r[0]) + __uint_as_float(r[1]);
  asm("" : "+v"(s));
  return s;
}
DI float sum8(float x) {
  return x + __int_as_float(__builtin_amdgcn_update_dpp(0, __float_as_int(x), 0x128, 0xf, 0xf, false));
}
DI float wave_max(float x) {
#define DPPMAX(ctl) x = __builtin_fmaxf(x, __int_as_float(__builtin_amdgcn_update_dpp(0, __float_as_int(x), ctl, 0xf, 0xf, false)))
  DPPMAX(0xB1); DPPMAX(0x4E); DPPMAX(0x141); DPPMAX(0x140);
#undef DPPMAX
  auto r = __builtin_amdgcn_permlane16_swap(__float_as_uint(x), __float_as_uint(x), false, false);
  x = __builtin_fmaxf(__uint_as_float(r[0]), __uint_as_float(r[1]));
  r = __builtin_amdgcn_permlane32_swap(__float_as_uint(x), __float_as_uint(x), false, false);
  return __builtin_fmaxf(__uint_as_float(r[0]), __uint_as_float(r[1]));
}
DI int lane_id() {
  int l = __builtin_amdgcn_mbcnt_hi(~0u, __builtin_amdgcn_mbcnt_lo(~0u, 0u));
  asm volatile("" : "+v"(l));
  __builtin_assume(l >= 0 && l < 64);
  return l;
}
DI float bf2f(u16 v) { return __uint_as_float((unsigned)v << 16); }
DI unsigned pk_bf16(float a, float b) {
  f32x2 f = {a, b};
  return __builtin_bit_cast(unsigned, __builtin_convertvector(f, bf16x2));
}
DI unsigned pk_f16(float a, float b) {
  f32x2 f = {a, b};
  return __builtin_bit_cast(unsigned, __builtin_convertvector(f, f16x2));
}
DI float rbf(float a) { return __uint_as_float(pk_bf16(a, 0.f) << 16); }
DI u32x2 tr_read(LAS char* p) {
  s16x4 v = __builtin_amdgcn_ds_read_tr16_b64_v4i16((LAS s16x4*)p);
  return __builtin_bit_cast(u32x2, v);
}
DI u32x4 join(u32x2 a, u32x2 b) { u32x4 r = {a.x, a.y, b.x, b.y}; return r; }
DI u32x4 tr_pair(LAS char* p0, LAS char* p1) { return join(tr_read(p0), tr_read(p1)); }
DI bf16x8 asbf(u32x4 v) { return __builtin_bit_cast(bf16x8, v); }
DI bf16x8 tr_frag(LAS char* p0, LAS char* p1) { return asbf(tr_pair(p0, p1)); }
#define MFMA32(a, b, c) __builtin_amdgcn_mfma_f32_32x32x16_bf16((a), (b), (c), 0, 0, 0)
#define MFMA16(a, b, c) __builtin_amdgcn_mfma_f32_16x16x32_bf16((a), (b), (c), 0, 0, 0)
#define MFMA16H(a, b, c) __builtin_amdgcn_mfma_f32_16x16x32_f16((a), (b), (c), 0, 0, 0)
#define AMFMA16(c, a, b) asm volatile("v_mfma_f32_16x16x32_bf16 %0, %1, %2, %0" : "+v"(c) : "v"(a), "v"(b))
#define GLDS(src, dst) __builtin_amdgcn_global_load_lds((const GAS unsigned*)(src), (LAS unsigned*)(dst), 16, 0, 0)
#define GLDSA(src, dst, aux) __builtin_amdgcn_global_load_lds((const GAS unsigned*)(src), (LAS unsigned*)(dst), 16, 0, aux)
#define SC1 16
#define DMA16(voff_, sbase_, lds_, aux_) asm volatile("s_mov_b32 m0, %2\n\ts_nop 0\n\tglobal_load_lds_dwordx4 %0, %1" aux_ :: "v"(voff_), "s"(sbase_), "s"(lds_) : "memory", "m0")
DI u32x4 ld16(rsrc_t r, int voff, int soff) { return __builtin_amdgcn_raw_buffer_load_b128(r, voff, soff, SC1); }
DI void st16(u32x4 v, rsrc_t r, int voff, int soff) {
  __builtin_amdgcn_raw_buffer_store_b128(v, r, voff, soff, SC1);
  asm volatile("s_nop 1" :: "v"(v) : "memory");
}

constexpr int WS_EPOCH = 0;
constexpr int WS_FY = 65536, WS_FU = 131072, WS_FP = WS_FU + 131072;
constexpr int WS_O = 524288;
constexpr int WS_UT = WS_O + (4 << 20);
constexpr int WS_YT = WS_UT + (4 << 20);
constexpr int WS_WT = WS_YT + (4 << 20);
constexpr int WS_KVP = WS_WT + (2 << 20);
constexpr int WS_ZP = WS_KVP + 16 * 16 * 16 * 512;
constexpr int WS_KVR = WS_ZP + 32 * 16 * 128;
constexpr int WS_END = WS_KVR + 32 * 4096;

constexpr int SLOT = 40960;
constexpr int L_UB = 131072, UB_CH = 4352;
constexpr int L_X = L_UB + 4 * UB_CH;
constexpr int L_Z = L_X + 4 * 2176;
constexpr int L_FLAG = L_Z + 512;
constexpr int L_DUM = L_FLAG + 256;
constexpr int LDS_BYTES = L_DUM + 256;
enum { F_KC = 0, F_XE = 8, F_LW = 11, F_CY, F_T2, F_XP, F_KV = 16, F_OD = 18, F_OS, F_Z = 24, F_Y = 26, F_G };

typedef f32x2 cplx;
DI cplx cmul(cplx a, cplx b) {
  cplx t, r;
  asm("v_pk_mul_f32 %0, %1, %2 op_sel:[1,1] op_sel_hi:[1,0] neg_lo:[1,0]" : "=v"(t) : "v"(a), "v"(b));
  asm("v_pk_fma_f32 %0, %1, %2, %3 op_sel:[0,0,0] op_sel_hi:[0,1,1]" : "=v"(r) : "v"(a), "v"(b), "v"(t));
  return r;
}
DI cplx zpow(float arl2, float ai, float p) {
  const float INV2PI = 0.15915494309189535f;
  const float INV2PI_LO = (float)(0.15915494309189533576888 - (double)0.15915494309189535f);
  float e = __builtin_amdgcn_exp2f(arl2 * p);
  float r = ai * p;
  float hi = r * INV2PI;
  float lo = __builtin_fmaf(r, INV2PI_LO, __builtin_fmaf(r, INV2PI, -hi));
  float f = (hi - __builtin_rintf(hi)) + lo;
  cplx z = {e * __builtin_amdgcn_cosf(f), e * __builtin_amdgcn_sinf(f)};
  return z;
}
template <bool NOPK> DI float phi_f(float t) {
  float a = t * 1.4426950408889634f, b = t + 1.f;
  if (NOPK) { asm("" : "+v"(a)); asm("" : "+v"(b)); }
  float r = __builtin_amdgcn_fmed3f(__builtin_amdgcn_exp2f(a), b, 1.f) + 1e-6f;
  if (NOPK) asm("" : "+v"(r));
  return r;
}
DI unsigned phi_pk(float t0, float t1) {
  const f32x2 t = {t0, t1};
  const f32x2 a = t * 1.4426950408889634f, b = t + 1.f;
  f32x2 r = {__builtin_amdgcn_fmed3f(__builtin_amdgcn_exp2f(a[0]), b[0], 1.f), __builtin_amdgcn_fmed3f(__builtin_amdgcn_exp2f(a[1]), b[1], 1.f)};
  r += 1e-6f;
  return pk_bf16(r[0], r[1]);
}
DI u32x4 tag_put(f32x4 v, unsigned tag) {
  u32x4 b = __builtin_bit_cast(u32x4, v), r;
  r[0] = __builtin_amdgcn_perm(b[0], tag, 0x07060500u);
  r[1] = __builtin_amdgcn_perm(b[1], tag, 0x07060501u);
  r[2] = __builtin_amdgcn_perm(b[2], tag, 0x07060502u);
  r[3] = __builtin_amdgcn_perm(b[3], tag, 0x07060503u);
  return r;
}
DI unsigned tag_get(u32x4 b) {
  unsigned lo = __builtin_amdgcn_perm(b[1], b[0], 0x0c0c0400u), hi = __builtin_amdgcn_perm(b[3], b[2], 0x04000c0cu);
  return lo | hi;
}
DI f32x4 tag_val(u32x4 b) { return __builtin_bit_cast(f32x4, b & 0xffffff00u); }

DI void sig(LAS unsigned* f) {
  asm volatile("s_mov_b64 exec, 1\n\tds_add_u32 %0, %1\n\ts_mov_b64 exec, -1" :: "v"((unsigned)(size_t)f), "v"(1u) : "memory");
}
DI unsigned sig_ret(LAS unsigned* f) {
  unsigned r;
  asm volatile("s_mov_b64 exec, 1\n\tds_add_rtn_u32 %0, %1, %2\n\ts_mov_b64 exec, -1\n\ts_waitcnt lgkmcnt(0)"
               : "=&v"(r) : "v"((unsigned)(size_t)f), "v"(1u) : "memory");
  return __builtin_amdgcn_readfirstlane(r);
}
DI unsigned ldsword(LAS unsigned* f) { return __builtin_amdgcn_readfirstlane(*(volatile LAS unsigned*)f); }
DI void waitge(LAS unsigned* f, unsigned n) {
  CBAR();
#pragma unroll 1
  for (int it = 0; it < (1 << 22); ++it) {
    if (ldsword(f) >= n) break;
    __builtin_amdgcn_s_sleep(1);
  }
  CBAR();
}
DI void gflag(char* ws, int off, unsigned tag, int lane) {
  CBAR();
  if (lane < 32) __hip_atomic_store((unsigned*)(ws + off) + lane, tag, __ATOMIC_RELAXED, __HIP_MEMORY_SCOPE_AGENT);
  CBAR();
}
DI void gpoll(char* p, unsigned tag) {
  CBAR();
  unsigned v = ~tag;
#pragma unroll 1
  for (int it = 0; it < (1 << 18); ++it) {
    if (v != tag) v = __hip_atomic_load((unsigned*)p, __ATOMIC_RELAXED, __HIP_MEMORY_SCOPE_AGENT);
    if (__all(v == tag)) break;
    __builtin_amdgcn_s_sleep(PSLEEP);
  }
  CBAR();
}

__global__ __launch_bounds__(768, 3) void fused_kernel(const u16* __restrict__ x, const u16* __restrict__ wqkv,
                                                       const u16* __restrict__ wo, const u16* __restrict__ u,
                                                       const u16* __restrict__ lar, const u16* __restrict__ aim,
                                                       const u16* __restrict__ cre, const u16* __restrict__ cim,
                                                       const u16* __restrict__ ldt, const u16* __restrict__ dsk,
                                                       u16* __restrict__ out, char* __restrict__ ws) {
  __shared__ __attribute__((aligned(16))) char smem[LDS_BYTES];
  LAS char* sm = (LAS char*)smem;
  LAS unsigned* FL = (LAS unsigned*)(sm + L_FLAG);
  const int tid = threadIdx.x, lane = tid & 63;
  const int wave = __builtin_amdgcn_readfirstlane(tid >> 6);
  const int bid = blockIdx.x, xcd = bid & 7, idx = bid >> 3;
  const int ml = idx & 7, hl = idx >> 3;
  const int mb = (xcd & 1) * 8 + ml, hp = (xcd >> 1) * 4 + hl;
  const int hq = hp ^ 1;
  const int mb2 = (xcd & 3) * 4 + (idx & 3), nb = (xcd >> 2) * 8 + (idx >> 2);
  unsigned pf = 0u;
  {
    const unsigned po = (unsigned)(tid * 64 + (idx & 15) * 4096) | (tid >= 64 ? 0x40000000u : 0u);
    asm volatile("v_cmp_gt_u32 vcc, %3, %1\n\ts_mov_b64 exec, vcc\n\tglobal_load_dword %0, %1, %2\n\ts_mov_b64 exec, -1"
                 : "+v"(pf) : "v"(po), "s"(__builtin_amdgcn_s_getpc()), "s"(CODE_LEN) : "memory", "vcc");
  }
#define PFDONE() asm volatile("" :: "v"(pf))
  asm volatile("ds_write_b32 %0, %1" :: "v"((unsigned)(size_t)FL + lane_id() * 4), "v"(0u) : "memory");
  SB();
  TSDECL;
  const rsrc_t rw = __builtin_amdgcn_make_buffer_rsrc((void*)ws, 0, WS_END, 0x00020000);
  asm volatile("" :: "s"(x), "s"(wqkv), "s"(wo), "s"(u), "s"(lar), "s"(aim), "s"(cre), "s"(cim), "s"(ldt), "s"(dsk), "s"(out), "s"(ws));
  asm volatile("s_waitcnt lgkmcnt(0)" ::: "memory");
  unsigned tag;
#define TAGLD() asm volatile("s_load_dword %0, %1, 0x0" : "=s"(tag) : "s"(ws) : "memory")
#define TAGRD() do { asm volatile("s_waitcnt lgkmcnt(0)" : "+s"(tag) :: "memory"); tag = (tag * 2654435761u + 0x9E3779B1u) | 1u; } while (0)
  const int q4 = (lane & 15) >> 2, p4 = lane & 3, g4 = lane >> 4, c16 = lane & 15;

  if ((wave >> 2) == 2) {
    __builtin_amdgcn_s_setprio(PRIO_L);
    const int hw = wave & 3;
    const int lane = lane_id(), q4 = (lane & 15) >> 2, p4 = lane & 3, g4 = lane >> 4, c16 = lane & 15;
    const int koff = KOFF;
    unsigned vo[10];
    {
      const unsigned vx0 = (unsigned)((mb * 128 + hw * 32 + (lane >> 3)) * 2048) + (((lane & 7) ^ (lane >> 4)) << 4);
      const unsigned vw0 = (unsigned)((lane >> 3) * 6144 + (((lane & 7) ^ (((lane >> 4) & 1) << 1)) << 4));
#pragma unroll
      for (int i = 0; i < 4; ++i) vo[i] = (vx0 ^ ((i & 1) * 64)) + i * 16384;
#pragma unroll
      for (int i = 0; i < 6; ++i) {
        const int qq = hw * 6 + i;
        vo[4 + i] = (vw0 ^ ((i & 1) * 64)) + ((qq & 7) * 49152 + (qq >> 3) * 2048 + (qq < 8 ? hq : hp) * 128);
      }
    }
    const unsigned lx = (unsigned)(size_t)sm + hw * 4096, lw = (unsigned)(size_t)sm + 16384 + hw * 6144;
#define DM(v_, s_) "s_nop 0\n\tglobal_load_lds_dwordx4 " v_ ", " s_ "\n\t"
#define M0A "s_add_u32 m0, m0, 0x400\n\t"
#define ISSUE_(xa_, wa_, lx_, lw_)                                                                                          \
    asm volatile("s_mov_b32 m0, %12\n\t" DM("%0", "%10") M0A DM("%1", "%10") M0A DM("%2", "%10") M0A DM("%3", "%10")          \
                 "s_mov_b32 m0, %13\n\t" DM("%4", "%11") M0A DM("%5", "%11") M0A DM("%6", "%11") M0A DM("%7", "%11") M0A      \
                 DM("%8", "%11") M0A DM("%9", "%11")                                                                        \
                 :: "v"(vo[0]), "v"(vo[1]), "v"(vo[2]), "v"(vo[3]), "v"(vo[4]), "v"(vo[5]), "v"(vo[6]), "v"(vo[7]), "v"(vo[8]), "v"(vo[9]), \
                    "s"(xa_), "s"(wa_), "s"(lx_), "s"(lw_) : "memory", "m0", "scc")
#define ISSUE(tt_, slot_) do {                                                                                              \
      const int k_ = ((tt_) + koff) & 15;                                                                                   \
      ISSUE_((const char*)x + k_ * 128, (const char*)wqkv + (size_t)k_ * 393216, lx + (slot_) * SLOT, lw + (slot_) * SLOT); } while (0)
    BAR();
    const int cg = bid >> 4, tt16 = bid & 15;
    const unsigned xo = (unsigned)((mb * 128 + hl * 32 + hw * 8 + (lane & 7)) * 2048);
    const unsigned l8 = idx * 32 + hw * 8 + (lane & 7);
    const unsigned wo8 = (l8 >> 2) * 2048u + (l8 & 3u) * 128u + (xcd >> 1) * 512;
#define TOUCH(xt_, nx_, wt_, nw_) do {                                                                                     \
      const unsigned ow_ = (((wt_) + koff + (lane >> 3) - (nx_)) & 15u) * 393216u + wo8;                                   \
      const unsigned ox_ = (((xt_) + koff + (lane >> 3)) & 15u) * 128u + xo;                                               \
      const char* p_ = (lane < 8 * (nx_) || idx >= 24) ? (const char*)x + ((lane < 8 * (nx_)) ? ox_ : xo + (koff & 15) * 128u) : (const char*)wqkv + ow_; \
      if (lane < 8 * (nx_) + 8 * (nw_)) __builtin_amdgcn_global_load_lds((const GAS unsigned*)p_, (LAS unsigned*)(sm + L_DUM), 4, 0, 0); } while (0)
#define TOUCHW(wt_, nw_) do {                                                                                              \
      const unsigned ow_ = (((wt_) + koff + (lane >> 3) - 2) & 15u) * 393216u + wo8;                                       \
      const char* p_ = (lane < 16 || idx >= 24) ? (const char*)wo + (unsigned)((tt16 * 64 + hw * 16 + c16) * 2048 + cg * 128) : (const char*)wqkv + ow_; \
      if (lane < 16 + 8 * (nw_)) __builtin_amdgcn_global_load_lds((const GAS unsigned*)p_, (LAS unsigned*)(sm + L_DUM), 4, 0, 0); } while (0)
#define ULD() do {                                                                                                         \
      LAS char* uc = (hw < 2) ? sm + 3 * SLOT + hw * 4096 : sm + L_X + (hw - 2) * 4096;                                    \
      const unsigned uo0 = (unsigned)((tt16 * 128 + hw * 32 + (lane >> 3)) * 2048 + cg * 128 + (((lane & 7) ^ (((lane >> 4) & 1) << 1)) << 4)), uo1 = (uo0 ^ 64) + 16384; \
      GLDSA((const char*)u + uo0, uc, 2); GLDSA((const char*)u + uo1, uc + 1024, 2);                                 \
      GLDSA((const char*)u + 32768 + uo0, uc + 2048, 2); GLDSA((const char*)u + 32768 + uo1, uc + 3072, 2); } while (0)
#define PWR() do {                                                                                                         \
      const int a0_ = WS_KVR + hp * 8192 + mb * 512 + hw * 128, a1_ = WS_ZP + ((hp * 2 + (hw & 1)) * 16 + mb) * 128;       \
      const int a2_ = WS_FP + (mb * 16 + hq) * 128, a3_ = WS_FY + bid * 128;                                               \
      int av = (lane & 8) ? a1_ : a0_, aw = (lane & 8) ? a3_ : a2_;                                                        \
      asm("" : "+v"(av), "+v"(aw));                                                                                        \
      u32x4 zz = {0u, 0u, 0u, 0u};                                                                                         \
      asm volatile("" : "+v"(zz));                                                                                         \
      st16(zz, rw, ((lane & 16) ? aw : av) + (lane & 7) * 16, 0);                                                          \
      st16(zz, rw, (lane >> 5) * 8192 + (lane & 31) * 16, WS_KVP + (hp * 16 + hw * 4) * 8192 + mb * 512);                  \
      st16(zz, rw, (lane >> 5) * 8192 + (lane & 31) * 16, WS_KVP + (hp * 16 + hw * 4) * 8192 + mb * 512 + 16384); } while (0)
#define PRO ISSUE(0, 0); TS(13); TOUCH(1, 3, 1, 5); ISSUE(1, 1); TOUCHW(6, NWB); ULD()
#define WM1 16
#define POST ISSUE(2, 2)
#define W00 15
    PRO;
    TS(14);
    TAGLD();
    WAITVMX(WM1);
    TAGRD();
    TS(1);
    BAR();
    POST;
#undef ULD
#undef TOUCHW
#undef TOUCH
    {
      int k = (3 + koff) & 15;
      const char* xa = (const char*)x + k * 128;
      const char* wa = (const char*)wqkv + (size_t)k * 393216;
      unsigned l0 = lx, l1 = lw;
      asm volatile("" : "+s"(xa), "+s"(wa), "+s"(l0), "+s"(l1));
      WAITVMX(W00);
#pragma unroll 1
      for (int t = 0; t < 13; ++t) {
        BAR();
        ISSUE_(xa, wa, l0, l1);
        k = (k + 1) & 15;
        xa = (const char*)x + k * 128;
        wa = (const char*)wqkv + (size_t)k * 393216;
        const unsigned dl = (l0 == lx + 2 * SLOT) ? (unsigned)(-2 * SLOT) : (unsigned)SLOT;
        l0 += dl; l1 += dl;
        asm volatile("" : "+s"(xa), "+s"(wa), "+s"(l0), "+s"(l1));
        WAITVM(10);
      }
    }
#undef ISSUE
#undef ISSUE_
#undef M0A
#undef DM
    TS(2);
    const char* src = ws + WS_WT + nb * 131072 + hw * 1024 + lane * 16;
#define WOP(s_) __builtin_amdgcn_global_load_lds((const GAS unsigned*)(src + ((s_) & 31) * 4096), (LAS unsigned*)(sm + (((s_) & 31) * 4 + hw) * 1024), 16, 0, SC1)
    constexpr int W1 = 10 + WN13, W2 = W1 + WN14, W3 = W2 + WN15, W4 = W3 + WNX;
    BAR();
#pragma unroll
    for (int i = 10; i < W1; ++i) WOP(i);
    WAITVMX(WN13);
    TS(8);
    BAR();
    TS(7);
#pragma unroll
    for (int i = W1; i < W2; ++i) WOP(i);
    TS(9);
    BAR();
#pragma unroll
    for (int i = W2; i < W3; ++i) WOP(i);
    TS(3);
    if (XB) BAR();
    if (W3 == 42) { WAITVM(0); sig(FL + F_LW); }
    waitge(FL + F_XP, 4);
#pragma unroll
    for (int i = W3; i < W4; ++i) { WOP(i); if (WPACE) __builtin_amdgcn_s_sleep(WPACE); }
    CBAR();
    {
      const int so = WS_KVP + (hp * 16 + mb) * 8192;
      const int vo = (lane >> 3) * 512 + (hw * 8 + (lane & 7)) * 16;
      u32x4 a = {0u, 0u, 0u, 0u}, b = a;
      bool na = true, nb_ = true;
#pragma unroll 1
      for (int it = 0; it < (1 << 17); ++it) {
        CBAR();
        if (na) a = ld16(rw, vo, so);
        if (nb_) b = ld16(rw, vo + 4096, so);
        na = tag_get(a) != tag; nb_ = tag_get(b) != tag;
        if (!__any(na || nb_)) break;
        __builtin_amdgcn_s_sleep(PSLEEP);
      }
      TS(5);
      const f32x4 fa = tag_val(a), fb = tag_val(b);
      float s[4];
#pragma unroll
      for (int i = 0; i < 4; ++i) { s[i] = fa[i] + fb[i]; asm("" : "+v"(s[i])); }
#pragma unroll
      for (int i = 0; i < 4; ++i) s[i] = sum32(sum16(sum8(s[i])));
      const unsigned p0 = pk_bf16(s[0], s[1]), p1 = pk_bf16(s[2], s[3]);
      const u32x4 w = {__builtin_amdgcn_perm(p0, tag, 0x05040100u), __builtin_amdgcn_perm(p0, tag, 0x07060302u),
                       __builtin_amdgcn_perm(p1, tag, 0x05040100u), __builtin_amdgcn_perm(p1, tag, 0x07060302u)};
      if (lane < 8) st16(w, rw, lane * 16, WS_KVR + hp * 8192 + mb * 512 + hw * 128);
      TS(6);
    }
    if (W3 < 42) {
      CBAR();
      if (WGATE) waitge(FL + F_OD, WGATE);
#pragma unroll
      for (int i = W4; i < 42; ++i) { WOP(i); if (WPACE) __builtin_amdgcn_s_sleep(WPACE); }
      WAITVM(0);
      sig(FL + F_LW);
    }
    TS(4);
#undef WOP
    TS(12);
    TSDUMP();
    return;
  }

  const int c = wave & 3, d = bid * 4 + c;
  LAS char* ub = sm + L_UB + c * UB_CH;
  LAS char* kc = sm + 2 * SLOT + c * 4096;
  f32x4 ta = {0, 0, 0, 0}, tb = {0, 0, 0, 0}, tc_ = {0, 0, 0, 0};
  float v_dsk;
  {
    const int pi = d * 128 + (wave < 4 ? 64 : 0) + lane;
    u16 r0 = lar[pi], r1 = aim[pi], r2 = cre[pi], r3 = cim[pi], r4 = ldt[d], r5 = dsk[d];
    LAS char* ks = (wave < 4) ? kc : ub;
    TAGLD();
    BAR();
    asm volatile("" : "+v"(r4));
    const float dt = rbf(expf(bf2f((u16)r4)));
    asm volatile("" : "+v"(r2), "+v"(r3));
    const float cr = bf2f((u16)r2), ci = bf2f((u16)r3);
    asm volatile("" : "+v"(r0), "+v"(r1), "+v"(r5));
    const float lr = bf2f((u16)r0), aiv = bf2f((u16)r1);
    v_dsk = bf2f((u16)r5);
    const float mx = wave_max(__builtin_fmaxf(__builtin_fabsf(cr), __builtin_fabsf(ci)));
    const int ex = (mx > 0.f && mx < 3.0e38f) ? __builtin_amdgcn_frexp_expf(mx) : 0;
    const float mult = __builtin_amdgcn_ldexpf(2.f * dt, ex);
    asm volatile("" : "+v"(v_dsk));
    PFDONE();
    TAGRD();
    TS(15);
    const float ar = -rbf(expf(lr)) * dt;
    const float ai = aiv * dt;
    const float arl2 = ar * 1.4426950408889634f;
    const cplx z1 = zpow(arl2, ai, 1.f), z8 = zpow(arl2, ai, 8.f), z128 = zpow(arl2, ai, 128.f);
    LAS char* wp = ks + lane * 64 + (((lane >> 1) & 3) << 4);
    const int h0 = g4 & 1;
    LAS char* rp0 = ks + (g4 * 256 + h0 * 128 + (q4 >> 1) * 64 + (((2 * (q4 & 1) + (p4 >> 1)) ^ ((2 * g4 + h0) & 3)) << 4) + (p4 & 1) * 8);
    LAS char* rp1 = ks + ((unsigned)(rp0 - ks) ^ 144u);
#define KW(v, sg)                                                                                                    \
  _Pragma("unroll") for (int hh = 0; hh < 2; ++hh) {                                                                 \
    u32x4 wr_, wi_;                                                                                                  \
    _Pragma("unroll") for (int i = 0; i < 4; ++i) {                                                                  \
      wr_[i] = pk_f16(v[8 * hh + 2 * i][0], v[8 * hh + 2 * i + 1][0]);                                               \
      wi_[i] = pk_f16(sg v[8 * hh + 2 * i][1], sg v[8 * hh + 2 * i + 1][1]);                                         \
    }                                                                                                                \
    *(LAS u32x4*)(ks + ((unsigned)(wp - ks) ^ (unsigned)(hh << 4))) = wr_;                                           \
    *(LAS u32x4*)(ks + ((unsigned)(wp - ks) ^ (unsigned)((2 + hh) << 4))) = wi_;                                     \
  }
#define KR(ks_) __builtin_bit_cast(f16x8, tr_pair(rp0 + (ks_) * 1024, rp1 + (ks_) * 1024))
    f16x8 bfr[4];
    cplx v[16], X[17], zp[9];
    v[0] = (cplx){1.f, 0.f};
    v[1] = z8;
    v[2] = cmul(z8, z8);
#pragma unroll
    for (int k = 3; k < 16; ++k) v[k] = cmul(v[k - 2], v[2]);
    KW(v, -);
    X[0] = (cplx){__builtin_amdgcn_ldexpf(cr, -ex), __builtin_amdgcn_ldexpf(ci, -ex)};
    X[1] = cmul(X[0], z128);
    {
      const cplx z256 = cmul(z128, z128);
#pragma unroll
      for (int r = 2; r < 17; ++r) X[r] = cmul(X[r - 2], z256);
    }
    zp[1] = z1; zp[8] = z8;
    zp[2] = cmul(z1, z1); zp[3] = cmul(zp[2], z1); zp[4] = cmul(zp[2], zp[2]);
    zp[5] = cmul(zp[4], z1); zp[6] = cmul(zp[4], zp[2]); zp[7] = cmul(zp[4], zp[3]);
#pragma unroll
    for (int k = 0; k < 4; ++k) bfr[k] = KR(k);
#pragma unroll
    for (int g = 0; g < 4; ++g) { v[4 * g] = X[4 + g]; v[4 * g + 1] = cmul(X[4 + g], zp[4]); v[4 * g + 2] = X[8 + g]; v[4 * g + 3] = X[12 + g]; }
    KW(v, );
#pragma unroll
    for (int j = 0; j < 16; ++j) v[j] = (j & 7) ? cmul(X[j >> 3], zp[j & 7]) : X[j >> 3];
    {
      f16x8 a[4];
#pragma unroll
      for (int k = 0; k < 4; ++k) a[k] = KR(k);
      KW(v, );
#pragma unroll
      for (int k = 0; k < 4; ++k) ta = MFMA16H(a[k], bfr[k], ta);
    }
#pragma unroll
    for (int a = 0; a < 2; ++a) {
      v[8 * a] = X[2 + a];
      v[8 * a + 1] = cmul(X[2 + a], zp[2]);
      v[8 * a + 2] = v[8 * a + 4] = cmul(X[2 + a], zp[4]);
      v[8 * a + 5] = cmul(X[2 + a], zp[6]);
      v[8 * a + 6] = cmul(X[2 + a], zp[8]);
      v[8 * a + 3] = v[8 * a + 7] = X[16];
    }
    {
      f16x8 a[4];
#pragma unroll
      for (int k = 0; k < 4; ++k) a[k] = KR(k);
      KW(v, );
#pragma unroll
      for (int k = 0; k < 4; ++k) tc_ = MFMA16H(a[k], bfr[k], tc_);
    }
#pragma unroll
    for (int k = 0; k < 4; ++k) { f16x8 a = KR(k); tb = MFMA16H(a, bfr[k], tb); }
#undef KW
#undef KR
    ta *= mult; tb *= mult; tc_ *= mult;
    TS(2);
  }

  if (wave < 4) {
    *(LAS f32x4*)(kc + lane * 16) = ta;
    *(LAS f32x4*)(kc + 1024 + lane * 16) = tb;
    *(LAS f32x4*)(kc + 2048 + lane * 16) = tc_;
    sig(FL + F_KC + c);
    __builtin_amdgcn_s_setprio(PRIO_C);
    const int lane = lane_id(), q4 = (lane & 15) >> 2, p4 = lane & 3, g4 = lane >> 4, c16 = lane & 15;
    const int mh = wave & 1, hd = wave >> 1;
    const int a0 = (mh * 64 + c16) * 128 + ((g4 ^ (c16 >> 1)) << 4);
    const int b0 = 16384 + (8 * g4 + q4) * 128 + (((hd * 4 + (p4 >> 1)) ^ (((q4 >> 1) | ((g4 & 1) << 1)) << 1)) << 4) + (p4 & 1) * 8;
    f32x4 acc[4][6];
#pragma unroll
    for (int m = 0; m < 4; ++m)
#pragma unroll
      for (int g = 0; g < 6; ++g) acc[m][g] = (f32x4){0.f, 0.f, 0.f, 0.f};
#pragma unroll
    for (int m = 0; m < 4; ++m)
      asm volatile("" : "+v"(acc[m][0]), "+v"(acc[m][1]), "+v"(acc[m][2]), "+v"(acc[m][3]), "+v"(acc[m][4]), "+v"(acc[m][5]));
    bf16x8 fa[2][4];
    u32x2 bl[6], bh[6];
#define RA(set, m, pa) fa[set][m] = *(LAS bf16x8*)((pa) + (m) * 2048)
#define PB(j) (((j) + 2) % 6)
#define RL(j, pe, po) bl[(j) % 6] = tr_read((((j) & 1) ? (po) : (pe)) + (PB(j) >> 1) * 8192 + ((j) / 6) * 4096)
#define RH(j, pe, po) bh[(j) % 6] = tr_read((((j) & 1) ? (po) : (pe)) + (PB(j) >> 1) * 8192 + ((j) / 6) * 4096 + 512)
#define FB(j) asbf(join(bl[(j) % 6], bh[(j) % 6]))
#define MM(j, m) acc[m][PB(j)] = (PB(j) < 2) ? MFMA16(FB(j), fa[(j) / 6][m], acc[m][PB(j)]) : MFMA16(fa[(j) / 6][m], FB(j), acc[m][PB(j)])
#define GRP(j, X0, X1, X2, X3)                \
  do {                                        \
    MM(j, 0); X0; SB(); MM(j, 1); X1; SB();   \
    MM(j, 2); X2; SB(); MM(j, 3); X3; SB();   \
  } while (0)
    LAS char* pa = sm + a0;
    LAS char* pa1 = sm + (a0 ^ 64);
    LAS char* pe = sm + b0;
    LAS char* po = sm + (b0 ^ 32);
#pragma unroll
    for (int i = 0; i < 4; ++i) { fa[1][i] = asbf((u32x4){0u, 0u, 0u, 0u}); asm volatile("" : "+v"(fa[1][i])); }
#pragma unroll
    for (int i = 3; i < 6; ++i) { bl[i] = bh[i] = (u32x2){0u, 0u}; asm volatile("" : "+v"(bl[i]), "+v"(bh[i])); }
    TS(3); TC(0);
    {
      int slot = 0;
#pragma unroll 1
      for (int t = 0; t < 16; ++t) {
        const int dl = (slot == 2) ? -2 * SLOT : SLOT;
        slot = (slot == 2) ? 0 : slot + 1;
        SB();
        LBAR();
        SB();
        GRP(9, RA(0, 0, pa), RL(0, pe, po), RH(0, pe, po), RA(0, 1, pa));
        GRP(10, RA(0, 2, pa), RA(0, 3, pa), RL(1, pe, po), RH(1, pe, po));
        GRP(11, RL(2, pe, po), RH(2, pe, po), , );
        GRP(0, RL(3, pe, po), RH(3, pe, po), , );
        GRP(1, RL(4, pe, po), RH(4, pe, po), , );
        GRP(2, RL(5, pe, po), RH(5, pe, po), RA(1, 0, pa1), RA(1, 1, pa1));
        GRP(3, RL(6, pe, po), RH(6, pe, po), RA(1, 2, pa1), RA(1, 3, pa1));
        GRP(4, RL(7, pe, po), RH(7, pe, po), RL(8, pe, po), RH(8, pe, po));
        GRP(5, RL(9, pe, po), RH(9, pe, po), RL(10, pe, po), RH(10, pe, po));
        GRP(6, RL(11, pe, po), RH(11, pe, po), , );
        GRP(7, pa += dl, pa1 += dl, pe += dl, po += dl);
        asm volatile("" : "+v"(pa), "+v"(pa1), "+v"(pe), "+v"(po));
        GRP(8, , , , );
      }
    }
    LBAR();
    TS(4); TC(1);
    SB();
    MM(9, 0); MM(9, 1); MM(9, 2); MM(9, 3);
#define QREST() do { MM(10, 0); MM(10, 1); MM(10, 2); MM(10, 3); MM(11, 0); MM(11, 1); MM(11, 2); MM(11, 3); } while (0)
    if (!QD) QREST();
#undef RA
#undef RL
#undef RH
#undef GRP
    {
    const int lane = lane_id(), g4 = lane >> 4, c16 = lane & 15;
    const int head = hp * 2 + hd, headq = hq * 2 + hd;
    {
      f32x4 kvt[2][2], zt[2];
#pragma unroll
      for (int i = 0; i < 2; ++i) { kvt[i][0] = kvt[i][1] = zt[i] = (f32x4){0.f, 0.f, 0.f, 0.f}; }
      const u32x4 ones = {0x3F803F80u, 0x3F803F80u, 0x3F803F80u, 0x3F803F80u};
#pragma unroll
      for (int j = 0; j < 2; ++j) {
        u32x4 fk[2], fv[2];
#pragma unroll
        for (int n = 0; n < 2; ++n)
#pragma unroll
          for (int i = 0; i < 4; ++i) {
            const f32x4 kk = acc[2 * j + (i >> 1)][2 + n], vv = acc[2 * j + (i >> 1)][4 + n];
            fk[n][i] = PKP ? phi_pk(kk[2 * (i & 1)], kk[2 * (i & 1) + 1]) : pk_bf16(phi_f<false>(kk[2 * (i & 1)]), phi_f<false>(kk[2 * (i & 1) + 1]));
            fv[n][i] = pk_bf16(vv[2 * (i & 1)], vv[2 * (i & 1) + 1]);
          }
#pragma unroll
        for (int td = 0; td < 2; ++td) {
          kvt[0][td] = MFMA16(asbf(fk[td]), asbf(fv[0]), kvt[0][td]);
          kvt[1][td] = MFMA16(asbf(fk[td]), asbf(fv[1]), kvt[1][td]);
          zt[td] = MFMA16(asbf(ones), asbf(fk[td]), zt[td]);
        }
      }
      LAS char* xo = sm + L_X + wave * 2176;
      LAS char* xi = sm + L_X + (wave ^ 1) * 2176;
      f32x4 keep[2];
#pragma unroll
      for (int td = 0; td < 2; ++td) {
        f32x4 ka = kvt[0][td], kb = kvt[1][td];
        asm("" : "+v"(ka), "+v"(kb));
        *(LAS f32x4*)(xo + td * 1024 + lane * 16) = mh ? ka : kb;
        keep[td] = mh ? kb : ka;
      }
      *(LAS float*)(xo + 2048 + (lane & 31) * 4) = (g4 & 1) ? zt[1][0] : zt[0][0];
      if (XB) LBAR();
      else { sig(FL + F_XE + hd); waitge(FL + F_XE + hd, 2); }
      if (mh == 0) {
        f32x4 zz = *(LAS f32x4*)(xo + 2048 + (lane & 7) * 16) + *(LAS f32x4*)(xi + 2048 + (lane & 7) * 16);
        if (lane < 8) st16(tag_put(zz, tag), rw, lane * 16, WS_ZP + (head * 16 + mb) * 128);
      }
      const int so = WS_KVP + (hp * 16 + hd * 8 + mh * 4) * 8192 + mb * 512;
#pragma unroll
      for (int td = 0; td < 2; ++td) {
        f32x4 o = *(LAS f32x4*)(xi + td * 1024 + lane * 16) + keep[td];
        st16(tag_put(o, tag), rw, td * 16384 + (lane >> 5) * 8192 + (lane & 31) * 16, so);
      }
      sig(FL + F_XP);
    }
    TS(5);
    SB();
    if (QD) QREST();
#undef QREST
#undef FB
#undef MM
#undef PB
#pragma unroll
    for (int m = 0; m < 4; ++m)
#pragma unroll
      for (int i = 0; i < 8; ++i) acc[m][i >> 2][i & 3] = phi_f<true>(acc[m][i >> 2][i & 3]);
    asm volatile("" : "+v"(acc[0][0]), "+v"(acc[1][0]), "+v"(acc[2][0]), "+v"(acc[3][0]),
                      "+v"(acc[0][1]), "+v"(acc[1][1]), "+v"(acc[2][1]), "+v"(acc[3][1]));
    u32x4 fq[4];
    {
      LAS f32x4* zl = (LAS f32x4*)(sm + L_Z + hd * 128);
      if (mh == 1) {
        u32x4 za = {0u, 0u, 0u, 0u}, zb = za;
        bool na = true, nb_ = true;
#pragma unroll 1
        for (int it = 0; it < (1 << 17); ++it) {
          CBAR();
          if (na) za = ld16(rw, lane * 16, WS_ZP + headq * 2048);
          if (nb_) zb = ld16(rw, 1024 + lane * 16, WS_ZP + headq * 2048);
          na = tag_get(za) != tag; nb_ = tag_get(zb) != tag;
          if (!__any(na || nb_)) break;
          __builtin_amdgcn_s_sleep(PSLEEP);
        }
        const f32x4 fa_ = tag_val(za), fb_ = tag_val(zb);
        f32x4 z;
#pragma unroll
        for (int i = 0; i < 4; ++i) { float s = fa_[i] + fb_[i]; asm("" : "+v"(s)); z[i] = sum32(sum16(sum8(s))); }
        if (lane < 8) zl[lane] = z;
        sig(FL + F_Z + hd);
      } else {
        waitge(FL + F_Z + hd, 1);
      }
      TS(6);
      const f32x4 z0 = zl[g4], z1 = zl[4 + g4];
#pragma unroll
      for (int m = 0; m < 4; ++m) {
        float d0 = acc[m][0][0] * z0[0], d1 = acc[m][1][0] * z1[0];
#pragma unroll
        for (int i = 1; i < 4; ++i) { d0 = __builtin_fmaf(acc[m][0][i], z0[i], d0); d1 = __builtin_fmaf(acc[m][1][i], z1[i], d1); }
        const float den = sum32(sum16(d0 + d1));
        const float rc = __builtin_amdgcn_rcpf(den + 1e-6f);
        float e[8];
#pragma unroll
        for (int i = 0; i < 8; ++i) { e[i] = acc[m][i >> 2][i & 3] * rc; asm("" : "+v"(e[i])); }
        fq[m] = (u32x4){pk_bf16(e[0], e[1]), pk_bf16(e[2], e[3]), pk_bf16(e[4], e[5]), pk_bf16(e[6], e[7])};
      }
    }
    asm volatile("" : "+v"(fq[0]), "+v"(fq[1]), "+v"(fq[2]), "+v"(fq[3]));
    TS(13);
    {
      u32x4 k[4];
      bool nk[4];
#pragma unroll
      for (int i = 0; i < 4; ++i) { k[i] = (u32x4){0u, 0u, 0u, 0u}; nk[i] = true; }
      const unsigned t0 = tag & 0xffffu, t1 = tag >> 16;
#pragma unroll 1
      for (int it = 0; it < (1 << 17); ++it) {
        CBAR();
#pragma unroll
        for (int i = 0; i < 4; ++i) if (nk[i]) k[i] = ld16(rw, i * 1024 + lane * 16, WS_KVR + headq * 4096);
#pragma unroll
        for (int i = 0; i < 4; ++i) nk[i] = (((k[i][0] ^ t0) | (k[i][1] ^ t1) | (k[i][2] ^ t0) | (k[i][3] ^ t1)) & 0xffffu) != 0u;
        if (!__any(nk[0] || nk[1] || nk[2] || nk[3])) break;
        __builtin_amdgcn_s_sleep(PSLEEP);
      }
      TS(14);
      u32x4 ka[2];
#pragma unroll
      for (int te = 0; te < 2; ++te)
#pragma unroll
        for (int i = 0; i < 4; ++i) ka[te][i] = __builtin_amdgcn_perm(k[2 * te + (i >> 1)][2 * (i & 1) + 1], k[2 * te + (i >> 1)][2 * (i & 1)], 0x07060302u);
#pragma unroll
      for (int m = 0; m < 4; ++m) {
        const f32x4 z4 = {0.f, 0.f, 0.f, 0.f};
        const f32x4 d0 = MFMA16(asbf(ka[0]), asbf(fq[m]), z4), d1 = MFMA16(asbf(ka[1]), asbf(fq[m]), z4);
        const u32x4 ov = {pk_bf16(d0[0], d0[1]), pk_bf16(d0[2], d0[3]), pk_bf16(d1[0], d1[1]), pk_bf16(d1[2], d1[3])};
        st16(ov, rw, (m & 1) * 1024 + lane * 16, WS_O + ((mb * 4 + mh * 2 + (m >> 1)) * 32 + headq) * 2048);
      }
    }
    TS(7);
    WAITVM(0);
    if (sig_ret(FL + F_OD) == 3u) {
      gflag(ws, WS_FP + (mb * 16 + hq) * 128, tag, lane);
      TS(8);
      gpoll(ws + WS_FP + ((mb2 * 16 + c16) << 7), tag);
      sig(FL + F_OS);
    } else {
      TS(8);
      waitge(FL + F_OS, 1);
    }
    TS(9);
    {
      const int pqs = WS_O + (mb2 * 4 + wave) * 65536;
      u32x4 pb[NRING][2];
#define LDH(i_, h_) pb[(i_) % NRING][h_] = __builtin_amdgcn_raw_buffer_load_b128(rw, (h_) * 1024 + lane * 16, pqs + ((i_) << 11), SC1)
#pragma unroll
      for (int i = 0; i < NRING; ++i) { LDH(i, 0); LDH(i, 1); }
      f32x4 oc[2][4];
#pragma unroll
      for (int s = 0; s < 2; ++s)
#pragma unroll
        for (int t = 0; t < 4; ++t) oc[s][t] = (f32x4){0.f, 0.f, 0.f, 0.f};
      asm volatile("" : "+v"(oc[0][0]), "+v"(oc[0][1]), "+v"(oc[0][2]), "+v"(oc[0][3]),
                   "+v"(oc[1][0]), "+v"(oc[1][1]), "+v"(oc[1][2]), "+v"(oc[1][3]));
      LAS char* mp0 = sm + lane * 16;
      bf16x8 ma[4];
      waitge(FL + F_LW, 4);
      if (bid == 0 && wave == 0) gflag(ws, WS_EPOCH, tag, lane);
      TS(10); TC(2);
#pragma unroll
      for (int t = 0; t < 4; ++t) ma[t] = *(LAS bf16x8*)(mp0 + t * 1024);
#pragma unroll
      for (int i = 0; i < 32; ++i) {
        SB();
        const u32x4 b0_ = pb[i % NRING][0], b1_ = pb[i % NRING][1];
#pragma unroll
        for (int t = 0; t < 4; ++t) {
          AMFMA16(oc[0][t], ma[t], b0_);
          if (i > 0 && i - 1 + NRING < 32 && (t & 1) == 0) LDH(i - 1 + NRING, t >> 1);
          SB();
          AMFMA16(oc[1][t], ma[t], b1_);
          if (i < 31) ma[t] = *(LAS bf16x8*)(mp0 + (i + 1) * 4096 + t * 1024);
          if (YG && i == YG && t == 3) sig(FL + F_G);
          if (YS && i == YS && t == 3) sig(FL + F_G + 1);
          SB();
        }
      }
#undef LDH
      asm volatile("s_nop 7\n\ts_nop 3" : "+v"(oc[0][0]), "+v"(oc[0][1]), "+v"(oc[0][2]), "+v"(oc[0][3]),
                   "+v"(oc[1][0]), "+v"(oc[1][1]), "+v"(oc[1][2]), "+v"(oc[1][3]));
      TS(11); TC(3);
#pragma unroll
      for (int s = 0; s < 2; ++s) {
        u32x4 w[2];
#pragma unroll
        for (int j = 0; j < 2; ++j)
          w[j] = (u32x4){pk_bf16(oc[s][2 * j][0], oc[s][2 * j][1]), pk_bf16(oc[s][2 * j][2], oc[s][2 * j][3]),
                         pk_bf16(oc[s][2 * j + 1][0], oc[s][2 * j + 1][1]), pk_bf16(oc[s][2 * j + 1][2], oc[s][2 * j + 1][3])};
        u32x4 wa, wb;
#pragma unroll
        for (int i = 0; i < 4; ++i) {
          wa[i] = __builtin_amdgcn_update_dpp(w[0][i], w[1][i], 0x128, 0xf, 0xc, false);
          wb[i] = __builtin_amdgcn_update_dpp(w[1][i], w[0][i], 0x128, 0xf, 0x3, false);
        }
        u16* op = out + (size_t)((mb2 * 4 + wave) * 32 + s * 16 + (c16 & 7)) * 2048 + nb * 64 + 32 * (c16 >> 3) + 8 * g4;
        ost16(op, wa);
        ost16(op + 8 * 2048, wb);
      }
    }
    TS(12);
    TSDUMP();
    }
    return;
  }

  const int cg = bid >> 4, tt16 = bid & 15;
  const int rg = lane >> 4;
  bf16x8 F[16];
  {
    waitge(FL + F_KC + c, 1);
    {
      const f32x4 oa = *(LAS f32x4*)(kc + lane * 16), ob = *(LAS f32x4*)(kc + 1024 + lane * 16), oc = *(LAS f32x4*)(kc + 2048 + lane * 16);
      LBAR();
      TS(1);
#pragma unroll
      for (int i = 0; i < 4; ++i) {
        float x0 = ta[i] + oa[i], x1 = tb[i] + ob[i], x2 = tc_[i] + oc[i];
        asm volatile("" : "+v"(x0)); asm volatile("" : "+v"(x1)); asm volatile("" : "+v"(x2));
        ta[i] = x0; tb[i] = x1; tc_[i] = x2;
      }
    }
#define ROL1(v) (unsigned)__builtin_amdgcn_update_dpp(0, (int)(v), 0x134, 0xf, 0xf, false)
#define MIX(lo, hi) __builtin_amdgcn_perm((hi), (lo), 0x07060100u)
    const bool l0 = (lane == 0);
    const u32x2 w0 = {pk_bf16(tc_[0] + (l0 ? v_dsk : 0.f), tc_[1]), pk_bf16(tc_[2], tc_[3])};
    const u32x2 w1 = {pk_bf16(tb[0], tb[0]), pk_bf16(tb[1], tb[2])};
    const unsigned pa0 = pk_bf16(ta[0], ta[0]), pa1 = pk_bf16(ta[1], ta[1]), pa2 = pk_bf16(ta[2], ta[2]), pa3 = pk_bf16(ta[3], ta[3]);
    const unsigned pe = pk_bf16(tb[3], tb[3]);
    const unsigned n0 = ROL1(l0 ? pa2 : pa0), n2 = ROL1(l0 ? pa3 : pa2), n3 = ROL1(l0 ? pe : pa3);
    const u32x4 w2 = {pa0, MIX(pa0, pa1), pa1, n0};
    const u32x4 w3 = {pa2, pa2, (lane & 1) ? n2 : MIX(pa2, n2), n2};
    const u32x4 w4 = {pa3, pa3, (lane & 1) ? n3 : MIX(pa3, n3), n3};
#undef ROL1
#undef MIX
    LAS char* K = ub + 32;
    const int k01 = 2 * (128 * (rg >> 1) + 8 * c16 + 4 * (rg & 1));
    LAS char* uc = (c < 2) ? sm + 3 * SLOT + c * 4096 : sm + L_X + (c - 2) * 4096;
    const int uo = q4 * 16384 + p4 * 64 + 16 * g4, us = WS_UT + cg * 262144 + (tt16 * 4 + c) * 256;
#define USTEP 65536
#define UST(v, o) st16(v, rw, uo, us + (o) * USTEP)
#define WST(v, o) st16(v, rw, lane * 16, ws_ + (o))
    u32x4 ut[4];
    st16((u32x4){~tag, ~tag, ~tag, ~tag}, rw, (lane >> 5) * 8192 + (lane & 31) * 16, WS_KVP + (hp * 16 + c * 4) * 8192 + mb * 512);
    st16((u32x4){~tag, ~tag, ~tag, ~tag}, rw, (lane >> 5) * 8192 + (lane & 31) * 16, WS_KVP + (hp * 16 + c * 4) * 8192 + mb * 512 + 16384);
    BAR();
    TS(4);
    *(LAS u32x2*)(K + k01) = w0;
    *(LAS u32x2*)(K + 512 + k01) = w1;
    {
      const int a0_ = WS_KVR + hp * 8192 + mb * 512 + c * 128, a1_ = WS_ZP + ((hp * 2 + (c & 1)) * 16 + mb) * 128;
      const int a2_ = WS_FP + (mb * 16 + hq) * 128, a3_ = WS_FY + bid * 128;
      const int g8 = (lane >> 3) & 3;
      int av = (g8 & 1) ? a1_ : a0_, aw = (g8 & 1) ? a3_ : a2_;
      asm("" : "+v"(av), "+v"(aw));
      st16((u32x4){~tag, ~tag, ~tag, ~tag}, rw, ((g8 & 2) ? aw : av) + (lane & 7) * 16, 0);
    }
    BAR();
    TS(5);
    *(LAS u32x4*)(K + 1024 + lane * 16) = w2;
    {
      const int f1 = (q4 >> 1) | ((g4 & 1) << 1);
      const unsigned up = (8 * g4 + q4) * 128 + (((p4 >> 1) ^ (f1 << 1)) << 4) + (p4 & 1) * 8;
#pragma unroll
      for (int j = 0; j < 4; ++j) ut[j] = tr_pair(uc + (up ^ (32u * j)), uc + (up ^ (32u * j)) + 512);
    }
    UST(ut[0], 0);
    LBAR();
    TS(6);
    *(LAS u32x4*)(K + 2048 + lane * 16) = w3;
    {
      const unsigned wv = (unsigned)(((tt16 * 2 + (c >> 1)) * 32 + (lane >> 2)) * 2048 + cg * 128 + (c & 1) * 64 + p4 * 16);
      DMA16(wv, (const char*)wo, (unsigned)(size_t)uc, "");
      DMA16(wv, (const char*)wo + 32768, (unsigned)(size_t)uc + 1024, "");
    }
    UST(ut[1], 1);
    BAR();
    TS(7);
    *(LAS u32x4*)(K + 3072 + lane * 16) = w4;
    UST(ut[2], 2);
    BAR();
    TS(13);
    if (lane < 8) *(LAS unsigned*)(ub + lane * 4) = 0u;
    UST(ut[3], 3);
    WAITVM(3);
    {
      LAS char* wp = uc + (4 * g4 + q4) * 64 + p4 * 16;
      ut[0] = tr_pair(wp, wp + 1024);
      ut[1] = tr_pair(wp + 8, wp + 1024 + 8);
    }
    const int ws_ = WS_WT + ((cg * 32 + tt16 * 2 + (c >> 1)) * 4 + (c & 1) * 2) * 1024;
    LAS char* fbp = ub + 2 * (16 - 12 + 32 * rg + 4 * (q4 + p4));
#pragma unroll
    for (int s = 0; s < 16; ++s) {
      if ((s & 3) == 0) {
        if (s == 12) WAITVM(0);
        BAR(); TS(s == 0 ? 14 : 15 + (s >> 2));
        if (s == 0) WST(ut[0], 0);
        if (s == 4) WST(ut[1], 1024);
        if (s == 12 && c == 0) st16((u32x4){tag, tag, tag, tag}, rw, (lane & 7) * 16, WS_FU + bid * 128);
      }
      F[s] = tr_frag(fbp + 256 * s, fbp + 256 * s + 32);
    }
    LBAR();
    TS(3);
    *(LAS unsigned*)(ub + lane * 4) = 0u;
    BAR();
    TS(19);
    char* fup = ws + WS_FU + ((((g4 & 1) ? nb : cg) * 16 + c16) << 7);
    BAR();
    TS(20);
    unsigned fu;
    asm volatile("global_load_dword %0, %1, off sc1" : "=v"(fu) : "v"(fup) : "memory");
    BAR();
    TS(21);
    asm volatile("s_waitcnt vmcnt(0)" : "+v"(fu) :: "memory");
    if (!__all(fu == tag)) gpoll(fup, tag);
#define UTD() _Pragma("unroll") for (int i = 0; i < 4; ++i) DMA16((lane >> 2) * 256 + p4 * 16, ws + WS_UT + bid * 16384 + c * 64 + i * 4096, (unsigned)(size_t)ub + 256 + i * 1024, " sc1")
    if (UTB == 12) UTD();
    BAR();
    TS(22);
    if (UTB == 13) UTD();
    BAR();
    if (UTB == 14) UTD();
    BAR();
    if (UTB == 15) UTD();
    if (XB) BAR();
#undef UTD
    WAITVM(0);
    waitge(FL + F_XP, 4);
  }
  TS(8); TC(2);
  {
    LAS char* gb = ub + 2 * (128 + 16 * p4 + 12 - 32 * rg - 4 * q4);
    float pc1 = 0.f, pc2 = 0.f, pc3 = 0.f, yprev = 0.f;
    const bool l63 = (lane == 63);
    const int yo = lane * 4;
#define ROR1(v) __int_as_float(__builtin_amdgcn_update_dpp(0, __float_as_int(v), 0x13C, 0xf, 0xf, true))
#define YOUT(k, v) __builtin_amdgcn_raw_buffer_store_b32((v), rw, yo + (k) * 256, WS_YT + d * 4096, SC1)
#define FIN(i)                                                                       \
  do {                                                                               \
    const float s2 = ca[i][2] + ROR1(l63 ? pc3 : ca[i][3]);                          \
    const float s1 = ca[i][1] + ROR1(l63 ? pc2 : s2);                                \
    float y = ca[i][0] + ROR1(l63 ? pc1 : s1);                                       \
    asm("" : "+v"(y));                                                               \
    pc1 = s1; pc2 = s2; pc3 = ca[i][3];                                              \
    if ((i) & 1) YOUT(8 * pass + ((i) >> 1), pk_bf16(yprev, y)); else yprev = y;     \
  } while (0)
#pragma unroll
    for (int pass = 0; pass < 2; ++pass) {
      f32x4 ca[16];
#pragma unroll
      for (int i = 0; i < 16; ++i) ca[i] = (f32x4){0, 0, 0, 0};
      bf16x8 G = tr_frag(gb, gb - 32);
#pragma unroll
      for (int v = 0; v < 16 * pass + 16; ++v) {
        bf16x8 Gn = G;
        if (v + 1 < 16 * pass + 16) Gn = tr_frag(gb + 128 * (v + 1), gb + 128 * (v + 1) - 32);
        SB();
#pragma unroll
        for (int i = 0; i < 16; ++i) {
          const int T = 16 * pass + i;
          if (T >= v && ((T - v) & 1) == 0) ca[i] = MFMA16(G, F[(T - v) >> 1], ca[i]);
        }
        if (v - 1 >= 16 * pass) FIN(v - 1 - 16 * pass);
        SB();
        G = Gn;
      }
      FIN(15);
      CBAR();
      SB();
    }
#undef FIN
#undef ROR1
#undef YOUT
  }
  CBAR();
  TS(9); TC(3);
  {
  const int lane = lane_id();
  WAITVM(0);
  if (sig_ret(FL + F_CY) == 3u) {
    gflag(ws, WS_FY + (cg * 16 + tt16) * 128, tag, lane);
    TS(10);
    gpoll(ws + WS_FY + ((cg * 16 + (lane & 15)) << 7), tag);
    sig(FL + F_Y);
  } else {
    TS(10);
    waitge(FL + F_Y, 1);
  }
  if (YG) waitge(FL + F_G, YGN);
  TS(11);
  {
    const int q4 = (lane & 15) >> 2, p4 = lane & 3, g4 = lane >> 4;
    LAS char* tb = sm + L_UB;
    u32x4 yr[4];
#pragma unroll
    for (int i = 0; i < 4; ++i)
      yr[i] = ld16(rw, (16 * c + 4 * i + g4) * 4096 + (lane & 15) * 16, WS_YT + cg * 64 * 4096 + tt16 * 256);
#pragma unroll
    for (int i = 0; i < 4; ++i) {
      const int row = 16 * c + 4 * i + g4;
      const int f = (row & 3) | (((row >> 3) & 1) << 2);
      *(LAS u32x4*)(tb + row * 256 + (((lane & 15) ^ (f << 1)) << 4)) = yr[i];
    }
    sig(FL + F_T2);
    waitge(FL + F_T2, 4);
    TS(13);
    if (YS) waitge(FL + F_G + 1, YGN);
    const int f = q4 | ((g4 & 1) << 2);
#pragma unroll
    for (int j = 0; j < 4; ++j) {
      LAS char* pp = tb + (32 * (p4 >> 1) + 8 * g4 + q4) * 256 + (((4 * c + j) ^ (f << 1)) << 4) + (p4 & 1) * 8;
      u32x4 v = tr_pair(pp, pp + 1024);
      ost16(out + (size_t)(tt16 * 128 + 16 * c + 4 * j + ((lane & 7) >> 1) + 64 * (lane & 1)) * 2048 + 1024 + cg * 64 + 32 * ((lane >> 3) & 1) + 8 * g4, v);
    }
  }
  TS(12);
  TSDUMP();
  }
}

extern "C" void kernel_launch(void* const* d_in, const int* in_sizes, int n_in, void* d_out, int out_size,
                              void* d_ws, size_t ws_size, hipStream_t stream) {
  fused_kernel<<<256, 768, 0, stream>>>((const u16*)d_in[0], (const u16*)d_in[1], (const u16*)d_in[2], (const u16*)d_in[3],
                                        (const u16*)d_in[4], (const u16*)d_in[5], (const u16*)d_in[6], (const u16*)d_in[7],
                                        (const u16*)d_in[8], (const u16*)d_in[9], (u16*)d_out, (char*)d_ws);
}
```
